```python
import torch

def _softmax(x, axis=-1):
    return torch.softmax(x, dim=axis)
def _gelu(x):
    return torch.nn.functional.gelu(x, approximate="tanh")
def _silu(x):
    return torch.nn.functional.silu(x)
def _sigmoid(x):
    return torch.sigmoid(x)
def _layernorm(x, axis=-1, eps=1e-5):
    m = x.mean(dim=axis, keepdim=True)
    v = x.var(dim=axis, keepdim=True, unbiased=False)
    return (x - m) / torch.sqrt(v + eps)
def _scatter_add(tgt, idx, src, axis=0):
    return tgt.clone().index_add_(axis, idx, src)
def _segment_sum(src, seg, n):
    out = torch.zeros((n,) + src.shape[1:], dtype=src.dtype)
    return out.index_add_(0, seg, src)
def _pad(x, pw):
    flat = tuple(v for pair in reversed(pw) for v in pair)
    return torch.nn.functional.pad(x, flat)

kernel_name = 'interleave_layers__linear_attn_block__glu_gated_swiglu__prompt'

def setup_inputs():

    _w = (lambda a: (lambda t: t.to(torch.bfloat16) if t.is_floating_point() else t)(torch.as_tensor(a)))
    def _A_setup():
        B, S, D = 1, 2048, 448
        return {
            "x": _w((lambda *s: torch.randn(*s, dtype=torch.bfloat16) * 0.05)(B, S, D)),
            "wqkv": _w((lambda *s: torch.randn(*s, dtype=torch.bfloat16) * 0.05)(D, 3 * D)),
            "wo": _w((lambda *s: torch.randn(*s, dtype=torch.bfloat16) * 0.05)(D, D)),
        }
    _A_in = _A_setup()

    def _B_setup():
        B, S, D, F = 1, 2048, 448, 1792
        return {
            "x": _w((lambda *s: torch.randn(*s, dtype=torch.bfloat16) * 0.05)(B, S, D)),
            "w1": _w((lambda *s: torch.randn(*s, dtype=torch.bfloat16) * 0.4)(D, F) * (2.0 / D) ** 0.5),
            "w3": _w((lambda *s: torch.randn(*s, dtype=torch.bfloat16) * 0.4)(D, F) * (2.0 / D) ** 0.5),
            "w2": _w((lambda *s: torch.randn(*s, dtype=torch.bfloat16) * 0.4)(F, D) * (2.0 / F) ** 0.5),
        }
    _B_in = _B_setup()

    return {
        "A_x": _A_in["x"],
        "A_wqkv": _A_in["wqkv"],
        "A_wo": _A_in["wo"],
        "B_x": _B_in["x"],
        "B_w1": _B_in["w1"],
        "B_w3": _B_in["w3"],
        "B_w2": _B_in["w2"],
    }

def reference(A_x, A_wqkv, A_wo, B_x, B_w1, B_w3, B_w2):
    def _A_ref(x, wqkv, wo):
        B, S, D = x.shape
        H = 16; Dh = D // H
        qkv = (lambda x, *d: x.permute(*d))((x @ wqkv).reshape(B, S, 3, H, Dh), 2, 0, 3, 1, 4)
        q, k, v = qkv[0], qkv[1], qkv[2]
        phi = lambda t: torch.where(t > 0, t + 1, torch.exp(t)) + 1e-6
        pq, pk = phi(q), phi(k)
        kv = torch.einsum("bhsd,bhse->bhde", pk, v)
        z = torch.einsum("bhsd->bhd", pk)
        num = torch.einsum("bhqd,bhde->bhqe", pq, kv)
        den = torch.einsum("bhqd,bhd->bhq", pq, z)[..., None] + 1e-6
        o = num / den
        return (lambda x, *d: x.permute(*d))(o, 0, 2, 1, 3).reshape(B, S, D) @ wo

    def _B_ref(x, w1, w3, w2):
        gate = torch.einsum("bsd,df->bsf", x, w1)
        up = torch.einsum("bsd,df->bsf", x, w3)
        h = _silu(gate) * up
        out = torch.einsum("bsf,fd->bsd", h, w2)
        return out

    x = A_x
    x = x + _A_ref(_layernorm(x, axis=-1), A_wqkv, A_wo)
    x = x + _B_ref(_layernorm(x, axis=-1), B_w1, B_w3, B_w2)
    x = x + _A_ref(_layernorm(x, axis=-1), A_wqkv, A_wo)
    x = x + _B_ref(_layernorm(x, axis=-1), B_w1, B_w3, B_w2)
    return x
```

```cpp
#include <hip/hip_runtime.h>
#include <hip/hip_bf16.h>
typedef __hip_bfloat16 bf16;
constexpr int S=2048, D=448, F=1792, H=16, DH=28;
__device__ inline float b2f(bf16 x){return __bfloat162float(x);}
__global__ void k_cvt(const bf16* x, float* y, int n){int i=blockIdx.x*blockDim.x+threadIdx.x; if(i<n) y[i]=b2f(x[i]);}
__global__ void k_out(const float* x, bf16* y, int n){int i=blockIdx.x*blockDim.x+threadIdx.x; if(i<n) y[i]=__float2bfloat16(x[i]);}
__global__ void k_ln(const float* x, float* y){
  int r=blockIdx.x; int l=threadIdx.x; float s=0; for(int i=l;i<D;i+=64) s+=x[r*D+i];
  for(int m=32;m>=1;m>>=1) s+=__shfl_xor(s,m); float mean=s/D; float v=0;
  for(int i=l;i<D;i+=64){float d=x[r*D+i]-mean; v+=d*d;}
  for(int m=32;m>=1;m>>=1) v+=__shfl_xor(v,m); float rs=1.f/sqrtf(v/D+1e-5f);
  for(int i=l;i<D;i+=64) y[r*D+i]=b2f(__float2bfloat16((x[r*D+i]-mean)*rs));
}
__global__ void k_gemm(const float* A, const bf16* B, float* C, const float* R, int M,int N,int K){
  __shared__ float As[16][17], Bs[16][17];
  int tx=threadIdx.x, ty=threadIdx.y; int row=blockIdx.y*16+ty, col=blockIdx.x*16+tx; float acc=0;
  for(int k0=0;k0<K;k0+=16){ As[ty][tx]=A[row*K+k0+tx]; Bs[ty][tx]=b2f(B[(k0+ty)*N+col]); __syncthreads();
    for(int k=0;k<16;k++) acc+=As[ty][k]*Bs[k][tx]; __syncthreads(); }
  if(R) acc=b2f(__float2bfloat16(acc+R[row*N+col]));
  C[row*N+col]=acc;
}
__device__ inline float phi(float t){return (t>0? t+1.f: __expf(t))+1e-6f;}
__global__ void k_kv(const float* qkv, float* kv){
  int h=blockIdx.x; int d=threadIdx.x/32, e=threadIdx.x%32; if(d>=DH||e>DH) return; float acc=0;
  for(int s=0;s<S;s++){ float pk=phi(qkv[s*3*D+D+h*DH+d]); float v= e<DH? qkv[s*3*D+2*D+h*DH+e]:1.f; acc+=pk*v; }
  kv[(h*32+d)*32+e]=acc;
}
__global__ void k_o(const float* qkv, const float* kv, float* o){ int i=blockIdx.x*blockDim.x+threadIdx.x; int s=i/D, c=i%D, h=c/DH, e=c%DH;
  float num=0, den=0; for(int d=0;d<DH;d++){float pq=phi(qkv[s*3*D+h*DH+d]); num+=pq*kv[(h*32+d)*32+e]; den+=pq*kv[(h*32+d)*32+DH];}
  o[i]=num/(den+1e-6f);
}
__global__ void k_silu(const float* g, const float* u, float* h, int n){int i=blockIdx.x*blockDim.x+threadIdx.x; if(i<n){float x=g[i]; h[i]=x/(1.f+__expf(-x))*u[i];}}

extern "C" void kernel_launch(void* const* d_in, const int* in_sizes, int n_in, void* d_out, int out_size, void* d_ws, size_t ws_size, hipStream_t stream){
  const bf16* x=(const bf16*)d_in[0]; const bf16* wqkv=(const bf16*)d_in[1]; const bf16* wo=(const bf16*)d_in[2];
  const bf16* w1=(const bf16*)d_in[4]; const bf16* w3=(const bf16*)d_in[5]; const bf16* w2=(const bf16*)d_in[6];
  float* ws=(float*)d_ws; float* X=ws; float* XN=X+S*D; float* QKV=XN+S*D; float* KV=QKV+S*3*D; float* O=KV+16*32*32; float* G=O+S*D; float* U=G+S*F; float* Hh=U+S*F;
  k_cvt<<<S*D/256,256,0,stream>>>(x,X,S*D);
  for(int l=0;l<2;l++){
    k_ln<<<S,64,0,stream>>>(X,XN);
    k_gemm<<<dim3(3*D/16,S/16),dim3(16,16),0,stream>>>(XN,wqkv,QKV,nullptr,S,3*D,D);
    k_kv<<<16,1024,0,stream>>>(QKV,KV);
    k_o<<<S*D/256,256,0,stream>>>(QKV,KV,O);
    k_gemm<<<dim3(D/16,S/16),dim3(16,16),0,stream>>>(O,wo,X,X,S,D,D);
    k_ln<<<S,64,0,stream>>>(X,XN);
    k_gemm<<<dim3(F/16,S/16),dim3(16,16),0,stream>>>(XN,w1,G,nullptr,S,F,D);
    k_gemm<<<dim3(F/16,S/16),dim3(16,16),0,stream>>>(XN,w3,U,nullptr,S,F,D);
    k_silu<<<S*F/256,256,0,stream>>>(G,U,Hh,S*F);
    k_gemm<<<dim3(D/16,S/16),dim3(16,16),0,stream>>>(Hh,w2,X,X,S,D,F);
  }
  k_out<<<S*D/256,256,0,stream>>>(X,(bf16*)d_out,S*D);
}
```

```cpp
#include <hip/hip_runtime.h>
#include <hip/hip_bf16.h>
#define MAPMODE 2
#define P0SLEEP 0
typedef unsigned u32;
constexpr u32 XSZ = 64u * 28u * 1024u, STSZ = 64u * 4096u, PSZ = 1048576u, OSZ = 64u * 32u * 1024u, HSZ = 64u * 112u * 1024u;
constexpr u32 OFF_X = 131072, OFF_ST = OFF_X + 2 * XSZ, OFF_PART = OFF_ST + 2 * STSZ, OFF_O = OFF_PART + 2 * PSZ, OFF_H = OFF_O + OSZ;
constexpr u32 OFF_DBG = 23437312;

__global__ void __launch_bounds__(256, 1)
fused_kernel(const void* gx, const void* gwqkv, const void* gwo, const void* gw1, const void* gw3, const void* gw2, void* gout, void* ws) {
  __shared__ __attribute__((aligned(16))) char lds_[163840];
  const u32 w = __builtin_amdgcn_readfirstlane(threadIdx.x >> 6);
  asm volatile("" ::"s"((u32)(size_t)lds_));
  asm volatile(
      "s_load_dwordx16 s[4:19], %0, 0x0\n"
      "s_mov_b32 s52, %1\n"
      "s_mov_b32 s51, %2\n"
      "v_mbcnt_lo_u32_b32 v238, -1, 0\n"
      "v_mbcnt_hi_u32_b32 v238, -1, v238\n"
      "s_getpc_b64 s[40:41]\n"
      ".Lpc0:\n"
      "s_lshr_b32 s85, s52, 3\n"
      "s_lshl_b32 s85, s85, 2\n"
      "s_add_u32 s85, s85, s51\n"
      "s_lshl_b32 s85, s85, 9\n"
      "v_min_u32 v2, 3, v238\n"
      "v_lshlrev_b32 v2, 7, v2\n"
      "v_add_u32 v2, s85, v2\n"
      "v_min_u32 v2, .Lcodeend-.Lpc0-4, v2\n"
      "global_load_dword v101, v2, s[40:41]\n"
      "s_mov_b32 s46, 0x1c0000\n"
      "s_mov_b32 s47, 0x20000\n"
      "s_mov_b32 s38, 0x16da000\n"
      "s_mov_b32 s39, 0x20000\n"
      "s_mov_b32 s31, 0x20000\n"
      "s_bfe_u32 s85, s52, 0x20001\n"
      "s_lshl_b32 s85, s85, 2\n"
      "s_lshr_b32 s49, s52, 6\n"
      "s_add_u32 s49, s49, s85\n"
      "s_and_b32 s85, s52, 1\n"
      "s_lshl_b32 s85, s85, 3\n"
      "s_bfe_u32 s50, s52, 0x30003\n"
      "s_add_u32 s50, s50, s85\n"
      "s_and_b32 s78, s50, 7\n"
      "s_lshr_b32 s79, s50, 3\n"
      "v_lshlrev_b32 v224, 4, v238\n"
      "v_and_b32 v0, 31, v238\n"
      "v_lshrrev_b32 v1, 5, v238\n"
      "s_lshl_b32 s71, s51, 8\n"
      "s_add_u32 s71, s71, s52\n"
      "s_and_b32 s72, s71, 1\n"
      "s_mov_b32 s88, 0\n"
      "s_mov_b32 s43, 0x20000\n"
      "s_mov_b32 s95, 0x20000\n"
      "s_mul_i32 s74, s71, 0x2aab\n"
      "s_lshr_b32 s74, s74, 16\n"
      "s_mul_i32 s85, s74, 6\n"
      "s_sub_u32 s75, s71, s85\n"
      "s_lshr_b32 s75, s75, 1\n"
      "s_mul_i32 s20, s74, 0xa800\n"
      "s_add_u32 s85, s75, 1\n"
      "s_mul_i32 s85, s85, 0x380\n"
      "s_cmp_eq_u32 s75, 2\n"
      "s_cselect_b32 s85, 0, s85\n"
      "s_add_u32 s20, s20, s85\n"
      "s_mul_i32 s85, s72, 24\n"
      "s_add_u32 s85, s85, s75\n"
      "s_mul_i32 s24, s85, 0x7000\n"
      "s_mul_i32 s27, s85, 0xe00\n"
      "s_lshl_b32 s85, s74, 10\n"
      "s_add_u32 s24, s24, s85\n"
      "s_add_u32 s24, s24, 0x1470000\n"
      "s_lshl_b32 s85, s74, 7\n"
      "s_add_u32 s27, s27, s85\n"
      "s_add_u32 s27, s27, 0x15c0000\n"
      "s_sub_u32 s73, s71, 0x230\n"
      "s_lshr_b32 s73, s73, 1\n"
      "s_mul_i32 s74, s73, 0x3800\n"
      "s_and_b32 s85, s73, 15\n"
      "s_mul_i32 s85, s85, 7\n"
      "s_lshr_b32 s75, s73, 4\n"
      "s_add_u32 s75, s75, s85\n"
      "s_lshl_b32 s75, s75, 11\n"
      "s_mul_i32 s85, s72, 0xe0000\n"
      "s_add_u32 s75, s75, s85\n"
      "s_add_u32 s75, s75, 0x1230000\n"
      "s_cmp_ge_u32 s71, 0x230\n"
      "s_cselect_b32 s20, s74, s20\n"
      "s_cselect_b32 s24, s75, s24\n"
      "s_cselect_b32 s27, 0, s27\n"
      "s_mov_b32 s63, 0xa80\n"
      "s_cselect_b32 s63, 0x380, s63\n"
      "s_cselect_b32 s66, 4, 8\n"
      "s_mov_b32 s21, 0x700\n"
      "s_cselect_b32 s21, 0xe00, s21\n"
      "s_cselect_b32 s22, 64, 56\n"
      "s_mov_b32 s25, 0x15000\n"
      "s_cselect_b32 s25, 0x400, s25\n"
      "s_mov_b32 s26, 0x2a000\n"
      "s_cselect_b32 s26, 0x38000, s26\n"
      "s_mov_b32 s42, 0x126000\n"
      "s_cselect_b32 s42, 0x188000, s42\n"
      "s_cselect_b32 s33, 0, 2\n"
      "s_sub_u32 s73, s71, 0x310\n"
      "s_lshr_b32 s73, s73, 1\n"
      "s_lshr_b32 s85, s73, 1\n"
      "s_mul_i32 s74, s85, 0x6200\n"
      "s_and_b32 s76, s73, 1\n"
      "s_mul_i32 s85, s76, 0x3800\n"
      "s_add_u32 s74, s74, s85\n"
      "s_lshl_b32 s75, s73, 11\n"
      "s_mul_i32 s85, s72, 0x40000\n"
      "s_add_u32 s75, s75, s85\n"
      "s_add_u32 s75, s75, 0x13f0000\n"
      "s_sub_u32 s76, 0, s76\n"
      "s_cmp_ge_u32 s71, 0x310\n"
      "s_cselect_b32 s20, s74, s20\n"
      "s_cselect_b32 s24, s75, s24\n"
      "s_cselect_b32 s26, 0x10000, s26\n"
      "s_cselect_b32 s42, 0x62000, s42\n"
      "s_cselect_b32 s33, 1, s33\n"
      "s_cselect_b32 s89, s76, 0\n"
      "s_mul_i32 s85, s72, 0x1c0\n"
      "s_add_u32 s20, s20, s85\n"
      "s_sub_u32 s73, s71, 0xa8\n"
      "s_mul_i32 s76, s73, 0x124a\n"
      "s_lshr_b32 s76, s76, 16\n"
      "s_mul_i32 s85, s76, 14\n"
      "s_sub_u32 s77, s73, s85\n"
      "s_mul_i32 s74, s77, 0x1c000\n"
      "s_lshl_b32 s85, s76, 7\n"
      "s_add_u32 s74, s74, s85\n"
      "s_mul_i32 s75, s76, 0x1c000\n"
      "s_lshl_b32 s85, s77, 11\n"
      "s_add_u32 s75, s75, s85\n"
      "s_add_u32 s75, s75, 0xf20000\n"
      "s_mul_i32 s72, s76, 0x3800\n"
      "s_lshl_b32 s85, s77, 8\n"
      "s_add_u32 s72, s72, s85\n"
      "s_add_u32 s72, s72, 0x15ea000\n"
      "s_cmp_lt_u32 s76, 12\n"
      "s_cselect_b32 s76, 1, 0\n"
      "s_cmp_lt_u32 s73, 0x188\n"
      "s_cselect_b32 s20, s74, s20\n"
      "s_cselect_b32 s24, s75, s24\n"
      "s_cselect_b32 s27, s72, s27\n"
      "s_cselect_b32 s33, s76, s33\n"
      "s_cselect_b32 s63, 0xe00, s63\n"
      "s_mov_b32 s64, 0x925\n"
      "s_cselect_b32 s64, 0x2000, s64\n"
      "s_cselect_b32 s65, 8, 28\n"
      "s_mov_b32 s67, 0x1c0\n"
      "s_cselect_b32 s67, 0x80, s67\n"
      "s_cselect_b32 s68, 0x1000, 32\n"
      "s_cselect_b32 s21, 0x200, s21\n"
      "s_cselect_b32 s22, 0x800, s22\n"
      "s_cselect_b32 s23, 32, 0x70\n"
      "s_cselect_b32 s25, 0x400, s25\n"
      "s_cselect_b32 s26, 0x7000, s26\n"
      "s_mov_b32 s69, 0x2a00\n"
      "s_cselect_b32 s69, 0x80, s69\n"
      "s_mov_b32 s96, 0x5400\n"
      "s_cselect_b32 s96, 0xe00, s96\n"
      "s_cselect_b32 s42, 0x188000, s42\n"
      "s_cselect_b32 s35, 0, 0x100\n"
      "s_cmp_ge_u32 s71, 0x350\n"
      "s_cselect_b32 s42, 0, s42\n"
      "s_cselect_b32 s33, 0, s33\n"
      "s_cselect_b32 s23, 0, s23\n"
      "s_cselect_b32 s20, 0, s20\n"
      "s_cselect_b32 s24, 0, s24\n"
      "s_cselect_b32 s89, 0, s89\n"
      "s_mov_b32 s94, s42\n"
      "s_mov_b32 s90, s71\n"
      "s_lshl_b32 s70, s51, 13\n"
      "s_add_u32 s70, s70, 0xe000\n"
      "s_cmp_eq_u32 s51, 3\n"
      "s_cselect_b32 s70, 0x23000, s70\n"
      "v_add_u32 v2, 0, v238\n"
      "v_mul_u32_u24 v3, s64, v2\n"
      "v_lshrrev_b32 v3, 16, v3\n"
      "v_mul_u32_u24 v4, s65, v3\n"
      "v_sub_u32 v2, v2, v4\n"
      "v_mul_u32_u24 v3, s63, v3\n"
      "v_lshl_add_u32 v184, v2, 4, v3\n"
      "v_add_u32 v2, 64, v238\n"
      "v_mul_u32_u24 v3, s64, v2\n"
      "v_lshrrev_b32 v3, 16, v3\n"
      "v_mul_u32_u24 v4, s65, v3\n"
      "v_sub_u32 v2, v2, v4\n"
      "v_mul_u32_u24 v3, s63, v3\n"
      "v_lshl_add_u32 v185, v2, 4, v3\n"
      "v_add_u32 v2, 0x80, v238\n"
      "v_mul_u32_u24 v3, s64, v2\n"
      "v_lshrrev_b32 v3, 16, v3\n"
      "v_mul_u32_u24 v4, s65, v3\n"
      "v_sub_u32 v2, v2, v4\n"
      "v_mul_u32_u24 v3, s63, v3\n"
      "v_lshl_add_u32 v186, v2, 4, v3\n"
      "v_add_u32 v2, 0xc0, v238\n"
      "v_mul_u32_u24 v3, s64, v2\n"
      "v_lshrrev_b32 v3, 16, v3\n"
      "v_mul_u32_u24 v4, s65, v3\n"
      "v_sub_u32 v2, v2, v4\n"
      "v_mul_u32_u24 v3, s63, v3\n"
      "v_lshl_add_u32 v187, v2, 4, v3\n"
      "v_add_u32 v2, 0, v238\n"
      "v_add_u32 v2, s35, v2\n"
      "v_mul_u32_u24 v3, s64, v2\n"
      "v_lshrrev_b32 v3, 16, v3\n"
      "v_mul_u32_u24 v4, s65, v3\n"
      "v_sub_u32 v2, v2, v4\n"
      "v_mul_u32_u24 v3, s63, v3\n"
      "v_lshl_add_u32 v188, v2, 4, v3\n"
      "v_add_u32 v2, 64, v238\n"
      "v_add_u32 v2, s35, v2\n"
      "v_mul_u32_u24 v3, s64, v2\n"
      "v_lshrrev_b32 v3, 16, v3\n"
      "v_mul_u32_u24 v4, s65, v3\n"
      "v_sub_u32 v2, v2, v4\n"
      "v_mul_u32_u24 v3, s63, v3\n"
      "v_lshl_add_u32 v189, v2, 4, v3\n"
      "v_add_u32 v2, 0x80, v238\n"
      "v_add_u32 v2, s35, v2\n"
      "v_mul_u32_u24 v3, s64, v2\n"
      "v_lshrrev_b32 v3, 16, v3\n"
      "v_mul_u32_u24 v4, s65, v3\n"
      "v_sub_u32 v2, v2, v4\n"
      "v_mul_u32_u24 v3, s63, v3\n"
      "v_lshl_add_u32 v190, v2, 4, v3\n"
      "v_add_u32 v2, 0xc0, v238\n"
      "v_add_u32 v2, s35, v2\n"
      "v_mul_u32_u24 v3, s64, v2\n"
      "v_lshrrev_b32 v3, 16, v3\n"
      "v_mul_u32_u24 v4, s65, v3\n"
      "v_sub_u32 v2, v2, v4\n"
      "v_mul_u32_u24 v3, s63, v3\n"
      "v_lshl_add_u32 v191, v2, 4, v3\n"
      "v_lshlrev_b32 v224, 4, v238\n"
      "v_mul_u32_u24 v2, s66, v1\n"
      "v_bfe_u32 v3, v238, 2, 2\n"
      "v_add_u32 v2, v2, v3\n"
      "v_mul_u32_u24 v2, s67, v2\n"
      "v_bfe_u32 v3, v238, 4, 1\n"
      "v_mul_u32_u24 v3, s68, v3\n"
      "v_add_u32 v2, v2, v3\n"
      "v_and_b32 v3, 3, v238\n"
      "v_lshl_add_u32 v2, v3, 3, v2\n"
      "v_add_u32 v192, s70, v2\n"
      "v_mul_u32_u24 v2, s69, v1\n"
      "v_lshl_add_u32 v209, v0, 2, v2\n"
      "s_cmp_eq_u32 s27, 0\n"
      "s_cselect_b32 s85, 0x80000000, 0\n"
      "v_or_b32 v209, s85, v209\n"
      "s_cmp_eq_u32 s24, 0\n"
      "s_cselect_b32 s85, 0x80000000, 0\n"
      "v_or_b32 v208, s85, v224\n"
      "s_add_u32 s25, s25, s24\n"
      "s_lshl_b32 s82, s49, 2\n"
      "s_add_u32 s82, s82, s51\n"
      "s_mul_i32 s71, s82, 0x7000\n"
      "s_mul_i32 s72, s51, 0x7000\n"
      "s_cmp_ge_u32 s51, 2\n"
      "s_cselect_b32 s85, 0x7000, 0\n"
      "s_add_u32 s72, s72, s85\n"
      "v_add_u32 v2, 0, v238\n"
      "v_mul_u32_u24 v3, 0x493, v2\n"
      "v_lshrrev_b32 v3, 16, v3\n"
      "v_mul_u32_u24 v4, 56, v3\n"
      "v_sub_u32 v2, v2, v4\n"
      "v_xor_b32 v2, v2, v3\n"
      "v_mul_u32_u24 v3, 0x380, v3\n"
      "v_lshl_add_u32 v8, v2, 4, v3\n"
      "v_add_u32 v2, 64, v238\n"
      "v_mul_u32_u24 v3, 0x493, v2\n"
      "v_lshrrev_b32 v3, 16, v3\n"
      "v_mul_u32_u24 v4, 56, v3\n"
      "v_sub_u32 v2, v2, v4\n"
      "v_xor_b32 v2, v2, v3\n"
      "v_mul_u32_u24 v3, 0x380, v3\n"
      "v_lshl_add_u32 v9, v2, 4, v3\n"
      "v_add_u32 v2, 0x80, v238\n"
      "v_mul_u32_u24 v3, 0x493, v2\n"
      "v_lshrrev_b32 v3, 16, v3\n"
      "v_mul_u32_u24 v4, 56, v3\n"
      "v_sub_u32 v2, v2, v4\n"
      "v_xor_b32 v2, v2, v3\n"
      "v_mul_u32_u24 v3, 0x380, v3\n"
      "v_lshl_add_u32 v10, v2, 4, v3\n"
      "v_add_u32 v2, 0xc0, v238\n"
      "v_mul_u32_u24 v3, 0x493, v2\n"
      "v_lshrrev_b32 v3, 16, v3\n"
      "v_mul_u32_u24 v4, 56, v3\n"
      "v_sub_u32 v2, v2, v4\n"
      "v_xor_b32 v2, v2, v3\n"
      "v_mul_u32_u24 v3, 0x380, v3\n"
      "v_lshl_add_u32 v11, v2, 4, v3\n"
      "v_add_u32 v2, 0x100, v238\n"
      "v_mul_u32_u24 v3, 0x493, v2\n"
      "v_lshrrev_b32 v3, 16, v3\n"
      "v_mul_u32_u24 v4, 56, v3\n"
      "v_sub_u32 v2, v2, v4\n"
      "v_xor_b32 v2, v2, v3\n"
      "v_mul_u32_u24 v3, 0x380, v3\n"
      "v_lshl_add_u32 v12, v2, 4, v3\n"
      "v_add_u32 v2, 0x140, v238\n"
      "v_mul_u32_u24 v3, 0x493, v2\n"
      "v_lshrrev_b32 v3, 16, v3\n"
      "v_mul_u32_u24 v4, 56, v3\n"
      "v_sub_u32 v2, v2, v4\n"
      "v_xor_b32 v2, v2, v3\n"
      "v_mul_u32_u24 v3, 0x380, v3\n"
      "v_lshl_add_u32 v13, v2, 4, v3\n"
      "v_add_u32 v2, 0x180, v238\n"
      "v_mul_u32_u24 v3, 0x493, v2\n"
      "v_lshrrev_b32 v3, 16, v3\n"
      "v_mul_u32_u24 v4, 56, v3\n"
      "v_sub_u32 v2, v2, v4\n"
      "v_xor_b32 v2, v2, v3\n"
      "v_mul_u32_u24 v3, 0x380, v3\n"
      "v_lshl_add_u32 v14, v2, 4, v3\n"
      "s_waitcnt lgkmcnt(0)\n"
      "s_load_dwordx2 s[54:55], s[18:19], 0x0\n"
      "s_mov_b64 s[44:45], s[4:5]\n"
      "s_mov_b64 s[36:37], s[18:19]\n"
      "s_and_b32 s45, s45, 0xffff\n"
      "s_and_b32 s37, s37, 0xffff\n"
      "s_cmp_ge_u32 s90, 0x230\n"
      "s_cselect_b64 s[40:41], s[14:15], s[6:7]\n"
      "s_cmp_ge_u32 s90, 0x310\n"
      "s_cselect_b64 s[40:41], s[8:9], s[40:41]\n"
      "s_cmp_eq_u32 s65, 8\n"
      "s_cselect_b64 s[92:93], s[12:13], s[40:41]\n"
      "s_cselect_b64 s[40:41], s[10:11], s[40:41]\n"
      "s_and_b32 s41, s41, 0xffff\n"
      "s_and_b32 s93, s93, 0xffff\n"
      "s_add_u32 s4, s51, s49\n"
      "s_and_b32 s4, s4, 3\n"
      "s_bfe_u32 s5, s49, 0x20002\n"
      "s_cmp_eq_u32 s33, 0\n"
      "s_cbranch_scc1 .Lsk1\n"
      "s_add_u32 m0, s70, 0\n"
      "s_nop 0\n"
      "buffer_load_dwordx4 v184, s[40:43], s20 offen lds\n"
      "s_add_u32 m0, s70, 0x400\n"
      "s_nop 0\n"
      "buffer_load_dwordx4 v185, s[40:43], s20 offen lds\n"
      "s_add_u32 m0, s70, 0x800\n"
      "s_nop 0\n"
      "buffer_load_dwordx4 v186, s[40:43], s20 offen lds\n"
      "s_add_u32 m0, s70, 0xc00\n"
      "s_nop 0\n"
      "buffer_load_dwordx4 v187, s[40:43], s20 offen lds\n"
      "s_add_u32 m0, s70, 0x1000\n"
      "s_nop 0\n"
      "buffer_load_dwordx4 v188, s[92:95], s20 offen lds\n"
      "s_add_u32 m0, s70, 0x1400\n"
      "s_nop 0\n"
      "buffer_load_dwordx4 v189, s[92:95], s20 offen lds\n"
      "s_add_u32 m0, s70, 0x1800\n"
      "s_nop 0\n"
      "buffer_load_dwordx4 v190, s[92:95], s20 offen lds\n"
      "s_add_u32 m0, s70, 0x1c00\n"
      "s_nop 0\n"
      "buffer_load_dwordx4 v191, s[92:95], s20 offen lds\n"
      ".Lsk1:\n"
      "s_add_u32 m0, s72, 0\n"
      "s_nop 0\n"
      "buffer_load_dwordx4 v8, s[44:47], s71 offen lds\n"
      "s_add_u32 m0, s72, 0x400\n"
      "s_nop 0\n"
      "buffer_load_dwordx4 v9, s[44:47], s71 offen lds\n"
      "s_add_u32 m0, s72, 0x800\n"
      "s_nop 0\n"
      "buffer_load_dwordx4 v10, s[44:47], s71 offen lds\n"
      "s_add_u32 m0, s72, 0xc00\n"
      "s_nop 0\n"
      "buffer_load_dwordx4 v11, s[44:47], s71 offen lds\n"
      "s_add_u32 m0, s72, 0x1000\n"
      "s_nop 0\n"
      "buffer_load_dwordx4 v12, s[44:47], s71 offen lds\n"
      "s_add_u32 m0, s72, 0x1400\n"
      "s_nop 0\n"
      "buffer_load_dwordx4 v13, s[44:47], s71 offen lds\n"
      "s_add_u32 m0, s72, 0x1800\n"
      "s_nop 0\n"
      "buffer_load_dwordx4 v14, s[44:47], s71 offen lds\n"
      "s_and_b32 s80, s51, 1\n"
      "s_lshr_b32 s81, s51, 1\n"
      "s_lshl_b32 s84, s79, 1\n"
      "s_lshl_b32 s85, s49, 2\n"
      "s_add_u32 s84, s84, s85\n"
      "s_add_u32 s83, s84, s81\n"
      "v_lshrrev_b32 v2, 2, v238\n"
      "v_mul_u32_u24 v2, 0xa80, v2\n"
      "v_and_b32 v3, 3, v238\n"
      "v_lshl_add_u32 v2, v3, 4, v2\n"
      "s_mul_i32 s85, s50, 56\n"
      "s_and_b32 s85, s85, -16\n"
      "v_add_u32 v211, s85, v2\n"
      "s_mul_i32 s53, s51, 0x49800\n"
      "s_mul_i32 s75, s51, 0x1c00\n"
      "v_mov_b32 v24, 0\n"
      "v_mov_b32 v25, 0\n"
      "v_mov_b32 v26, 0\n"
      "v_mov_b32 v27, 0\n"
      "v_mov_b32 v28, 0xffbfffbf\n"
      "v_mov_b32 v29, 0xffbfffbf\n"
      "v_mov_b32 v30, 0xffbfffbf\n"
      "v_mov_b32 v31, 0xffbfffbf\n"
      "s_lshl_b32 s85, s50, 11\n"
      "s_lshl_b32 s86, s49, 6\n"
      "s_add_u32 s85, s85, s86\n"
      "s_lshr_b32 s86, s51, 1\n"
      "s_lshl_b32 s86, s86, 10\n"
      "s_add_u32 s85, s85, s86\n"
      "s_and_b32 s86, s51, 1\n"
      "s_lshl_b32 s86, s86, 3\n"
      "s_add_u32 s85, s85, s86\n"
      "v_add_u32 v3, s85, v224\n"
      "v_cmp_gt_u32 vcc, 4, v238\n"
      "v_mov_b32 v2, 0x80000000\n"
      "s_nop 0\n"
      "v_cndmask_b32 v239, v2, v3, vcc\n"
      "s_mov_b32 s69, 0x620000\n"
      "s_mul_i32 s85, s82, 0x8000\n"
      "s_add_u32 s69, s69, s85\n"
      "s_mul_i32 s85, s50, 0x800\n"
      "s_add_u32 s69, s69, s85\n"
      "s_mov_b32 s66, 0x420000\n"
      "s_mul_i32 s85, s50, 0x10000\n"
      "s_add_u32 s66, s66, s85\n"
      "s_mul_i32 s85, s51, 0x4000\n"
      "s_add_u32 s66, s66, s85\n"
      "s_mul_i32 s85, s49, 0x400\n"
      "s_add_u32 s66, s66, s85\n"
      "buffer_store_dwordx4 v[28:31], v224, s[36:39], s66 offen sc1\n"
      "s_add_u32 s34, s66, 0x100000\n"
      "buffer_store_dwordx4 v[28:31], v224, s[36:39], s34 offen sc1\n"
      "buffer_store_dwordx4 v[24:27], v224, s[36:39], s69 offen sc1\n"
      "buffer_store_dwordx4 v[24:27], v224, s[36:39], s69 offen offset:1024 sc1\n"
      "s_mov_b32 s34, 0x10000\n"
      "buffer_store_dwordx2 v[28:29], v239, s[36:39], s34 offen sc1\n"
      "s_mov_b32 s34, 0x18000\n"
      "buffer_store_dwordx2 v[28:29], v239, s[36:39], s34 offen sc1\n"
      "v_mul_u32_u24 v2, 0x380, v0\n"
      "v_add_u32 v2, s72, v2\n"
      "v_and_b32 v3, 7, v0\n"
      "v_add_u32 v4, 0, v1\n"
      "v_xor_b32 v4, v4, v3\n"
      "v_lshl_add_u32 v128, v4, 4, v2\n"
      "v_add_u32 v4, 2, v1\n"
      "v_xor_b32 v4, v4, v3\n"
      "v_lshl_add_u32 v129, v4, 4, v2\n"
      "v_add_u32 v4, 4, v1\n"
      "v_xor_b32 v4, v4, v3\n"
      "v_lshl_add_u32 v130, v4, 4, v2\n"
      "v_add_u32 v4, 6, v1\n"
      "v_xor_b32 v4, v4, v3\n"
      "v_lshl_add_u32 v131, v4, 4, v2\n"
      "s_mov_b32 s56, 0\n"
      "s_mov_b32 s57, -1\n"
      "s_mov_b32 s58, 0x10000000\n"
      "s_mov_b32 s59, 0x10000000\n"
      "s_sub_u32 s60, 0, s80\n"
      "s_mov_b32 s61, s60\n"
      "s_mov_b32 s97, 1\n"
      "s_lshr_b32 s85, s49, 3\n"
      "s_lshl_b32 s85, s85, 2\n"
      "s_add_u32 s71, s71, 0x1c00\n"
      "s_add_u32 m0, s72, 0x1c00\n"
      "s_nop 0\n"
      "buffer_load_dwordx4 v8, s[44:47], s71 offen lds\n"
      "s_add_u32 s85, s85, s51\n"
      "s_cmp_eq_u32 s85, 7\n"
      "s_cselect_b32 s99, -1, 0\n"
      "s_mov_b32 s98, 0\n"
      "s_mov_b32 s48, 0\n"
      "s_mov_b32 s63, 0x10000\n"
      "s_mov_b32 s77, 0x186a0\n"
      "s_mul_i32 s85, s78, 7\n"
      "s_lshl_b32 s86, s80, 2\n"
      "s_add_u32 s85, s85, s86\n"
      "s_mul_i32 s86, s83, 0x7000\n"
      "v_add_u32 v2, s85, v1\n"
      "v_add_u32 v2, 0, v2\n"
      "v_lshrrev_b32 v3, 1, v2\n"
      "v_and_b32 v2, 1, v2\n"
      "v_lshlrev_b32 v3, 10, v3\n"
      "v_lshl_add_u32 v3, v2, 9, v3\n"
      "v_lshl_add_u32 v3, v0, 4, v3\n"
      "v_add_u32 v229, s86, v3\n"
      "v_add_u32 v2, s85, v1\n"
      "v_add_u32 v2, 2, v2\n"
      "v_lshrrev_b32 v3, 1, v2\n"
      "v_and_b32 v2, 1, v2\n"
      "v_lshlrev_b32 v3, 10, v3\n"
      "s_add_u32 m0, s72, 0x2000\n"
      "s_nop 0\n"
      "buffer_load_dwordx4 v9, s[44:47], s71 offen lds\n"
      "v_lshl_add_u32 v3, v2, 9, v3\n"
      "v_lshl_add_u32 v3, v0, 4, v3\n"
      "v_add_u32 v230, s86, v3\n"
      "s_and_b64 vcc, s[56:57], s[60:61]\n"
      "v_mov_b32 v2, 0x80000000\n"
      "v_cndmask_b32 v230, v230, v2, vcc\n"
      "s_lshl_b32 s85, s83, 12\n"
      "s_lshl_b32 s86, s78, 9\n"
      "s_add_u32 s85, s85, s86\n"
      "s_lshl_b32 s86, s80, 3\n"
      "s_add_u32 s85, s85, s86\n"
      "v_lshlrev_b32 v3, 4, v0\n"
      "v_add_u32 v3, s85, v3\n"
      "v_cndmask_b32 v231, v3, v2, s[56:57]\n"
      "v_add_u32 v225, 0xe000, v224\n"
      "v_add_u32 v226, 0x1c000, v224\n"
      "s_lshl_b32 s85, s82, 12\n"
      "v_lshlrev_b32 v3, 4, v0\n"
      "v_lshl_add_u32 v3, v1, 11, v3\n"
      "v_add_u32 v232, s85, v3\n"
      "s_lshl_b32 s85, s51, 7\n"
      "s_add_u32 s85, s85, 0x27000\n"
      "v_lshlrev_b32 v3, 2, v0\n"
      "v_add_u32 v233, s85, v3\n"
      "s_add_u32 m0, s72, 0x2400\n"
      "s_nop 0\n"
      "buffer_load_dwordx4 v10, s[44:47], s71 offen lds\n"
      "v_lshlrev_b32 v3, 4, v1\n"
      "v_add_u32 v234, s85, v3\n"
      "v_lshlrev_b32 v3, 3, v0\n"
      "v_add_u32 v235, 0x27200, v3\n"
      "s_lshl_b32 s85, s80, 10\n"
      "v_add_u32 v236, s85, v224\n"
      "s_xor_b32 s85, s85, 0x400\n"
      "v_add_u32 v237, s85, v224\n"
      "v_mov_b32 v2, 0x80000000\n"
      "s_and_b32 s85, s60, 0xff000000\n"
      "s_mov_b32 s90, s85\n"
      "s_mov_b32 s91, s85\n"
      "v_cndmask_b32 v236, v236, v2, s[90:91]\n"
      "s_xor_b32 s85, s85, 0xff000000\n"
      "s_mov_b32 s90, s85\n"
      "s_mov_b32 s91, s85\n"
      "v_cndmask_b32 v237, v237, v2, s[90:91]\n"
      "s_xor_b32 s85, s51, 1\n"
      "s_mul_i32 s85, s85, 0x3000\n"
      "s_add_u32 s85, s85, 0\n"
      "v_add_u32 v249, s85, v224\n"
      "s_xor_b32 s85, s51, 2\n"
      "s_mul_i32 s85, s85, 0x3000\n"
      "s_add_u32 s85, s85, 0x1000\n"
      "s_add_u32 m0, s72, 0x2800\n"
      "s_nop 0\n"
      "buffer_load_dwordx4 v11, s[44:47], s71 offen lds\n"
      "v_add_u32 v250, s85, v224\n"
      "s_xor_b32 s85, s51, 3\n"
      "s_mul_i32 s85, s85, 0x3000\n"
      "s_add_u32 s85, s85, 0x2000\n"
      "v_add_u32 v251, s85, v224\n"
      "s_mul_i32 s85, s51, 0x3000\n"
      "v_add_u32 v252, s85, v224\n"
      "v_lshrrev_b32 v2, 2, v238\n"
      "v_and_b32 v3, 3, v238\n"
      "v_lshlrev_b32 v3, 4, v3\n"
      "v_lshl_add_u32 v3, v2, 10, v3\n"
      "s_lshl_b32 s85, s49, 6\n"
      "v_add_u32 v248, s85, v3\n"
      "s_lshl_b32 s85, s51, 12\n"
      "s_add_u32 s85, s85, 0x23000\n"
      "v_add_u32 v227, s85, v224\n"
      "s_lshl_b32 s85, s51, 10\n"
      "s_add_u32 s85, s85, 0x23000\n"
      "v_add_u32 v228, s85, v224\n"
      "v_mov_b32 v222, 0\n"
      "v_mov_b32 v223, 0\n"
      "v_mov_b32 v214, 0\n"
      "v_mov_b32 v215, 0\n"
      "s_mov_b32 s76, 0x1470000\n"
      "s_add_u32 m0, s72, 0x2c00\n"
      "s_nop 0\n"
      "buffer_load_dwordx4 v12, s[44:47], s71 offen lds\n"
      "s_mul_i32 s85, s50, 0x15000\n"
      "s_add_u32 s76, s76, s85\n"
      "s_mul_i32 s85, s51, 0x1c00\n"
      "s_add_u32 s76, s76, s85\n"
      "s_mov_b32 s62, 0x20000\n"
      "s_mul_i32 s85, s82, 0x7000\n"
      "s_add_u32 s62, s62, s85\n"
      "s_mov_b32 s66, 0x420000\n"
      "s_mul_i32 s85, s50, 0x10000\n"
      "s_add_u32 s66, s66, s85\n"
      "s_mul_i32 s85, s51, 0x4000\n"
      "s_add_u32 s66, s66, s85\n"
      "s_mul_i32 s85, s49, 0x400\n"
      "s_add_u32 s66, s66, s85\n"
      "s_mov_b32 s67, 0x420000\n"
      "s_mul_i32 s85, s50, 0x10000\n"
      "s_add_u32 s67, s67, s85\n"
      "s_mul_i32 s85, s51, 0x4000\n"
      "s_add_u32 s67, s67, s85\n"
      "s_mov_b32 s68, 0x13f0000\n"
      "s_mul_i32 s85, s78, 0x10000\n"
      "s_add_u32 s68, s68, s85\n"
      "s_mul_i32 s85, s4, 0x4000\n"
      "s_add_u32 s68, s68, s85\n"
      "s_add_u32 m0, s72, 0x3000\n"
      "s_nop 0\n"
      "buffer_load_dwordx4 v13, s[44:47], s71 offen lds\n"
      "s_mov_b32 s69, 0x620000\n"
      "s_mul_i32 s85, s82, 0x8000\n"
      "s_add_u32 s69, s69, s85\n"
      "s_mul_i32 s85, s50, 0x800\n"
      "s_add_u32 s69, s69, s85\n"
      "s_mov_b32 s73, 0x820000\n"
      "s_mul_i32 s85, s82, 0x1c000\n"
      "s_add_u32 s73, s73, s85\n"
      "s_mul_i32 s85, s50, 0x1c00\n"
      "s_add_u32 s73, s73, s85\n"
      "s_mov_b32 s74, 0x83c000\n"
      "s_mul_i32 s85, s84, 0x1c000\n"
      "s_add_u32 s74, s74, s85\n"
      "s_mul_i32 s85, s81, 0xfffe4000\n"
      "s_add_u32 s74, s74, s85\n"
      "s_mul_i32 s85, s4, 0x7000\n"
      "s_add_u32 s74, s74, s85\n"
      "v_bfe_u32 v2, v238, 2, 2\n"
      "v_lshl_add_u32 v2, v1, 3, v2\n"
      "v_lshlrev_b32 v2, 6, v2\n"
      "v_bfe_u32 v3, v238, 4, 1\n"
      "v_lshl_add_u32 v2, v3, 5, v2\n"
      "v_and_b32 v3, 3, v238\n"
      "v_lshl_add_u32 v2, v3, 3, v2\n"
      "s_add_u32 m0, s72, 0x3400\n"
      "s_nop 0\n"
      "buffer_load_dwordx4 v14, s[44:47], s71 offen lds\n"
      "s_and_b32 s85, s50, 1\n"
      "s_lshl_b32 s85, s85, 3\n"
      "s_add_u32 s85, s85, s75\n"
      "s_add_u32 s85, s85, 0xe000\n"
      "v_add_u32 v132, s85, v2\n"
      "v_add_u32 v133, 0xe000, v132\n"
      "v_add_u32 v134, s75, v225\n"
      "v_add_u32 v135, s75, v226\n"
      "s_mul_i32 s85, s51, 0x180\n"
      "s_add_u32 s85, s85, 0x23000\n"
      "v_lshlrev_b32 v2, 2, v0\n"
      "v_add_u32 v204, s85, v2\n"
      "s_min_u32 s86, s51, 2\n"
      "s_lshl_b32 s85, s86, 7\n"
      "s_add_u32 s85, s85, 0x23000\n"
      "v_add_u32 v205, s85, v2\n"
      "s_lshl_b32 s85, s86, 8\n"
      "s_add_u32 s85, s85, 0x27a00\n"
      "v_lshlrev_b32 v2, 3, v0\n"
      "v_add_u32 v206, s85, v2\n"
      "v_mov_b32 v124, 0x3f803f80\n"
      "v_mov_b32 v125, 0x3f803f80\n"
      "v_mov_b32 v126, 0x3f803f80\n"
      "v_mov_b32 v127, 0x3f803f80\n"
      "v_mov_b32 v200, 0\n"
      "v_mov_b32 v201, 0\n"
      "v_mov_b32 v202, 0\n"
      "v_mov_b32 v203, 0\n"
      "v_mov_b32 v210, 0x3f803f80\n"
      "s_cmp_eq_u32 s33, 0\n"
      "s_cbranch_scc1 .Lsk3\n"
      "v_mov_b32 v184, v192\n"
      "v_add_u32 v185, s22, v192\n"
      "v_add_u32 v186, s23, v184\n"
      "v_add_u32 v187, s23, v185\n"
      "v_add_u32 v188, s23, v186\n"
      "v_add_u32 v189, s23, v187\n"
      "v_add_u32 v190, s23, v188\n"
      "v_add_u32 v191, s23, v189\n"
      "v_add_u32 v192, s21, v184\n"
      "v_add_u32 v193, s21, v185\n"
      "v_add_u32 v194, s21, v186\n"
      "v_add_u32 v195, s21, v187\n"
      "v_add_u32 v196, s21, v188\n"
      "v_add_u32 v197, s21, v189\n"
      "v_add_u32 v198, s21, v190\n"
      "v_add_u32 v199, s21, v191\n"
      "v_mov_b32 v16, 0\n"
      "v_mov_b32 v17, 0\n"
      "v_mov_b32 v18, 0\n"
      "v_mov_b32 v19, 0\n"
      "v_mov_b32 v20, 0\n"
      "v_mov_b32 v21, 0\n"
      "v_mov_b32 v22, 0\n"
      "v_mov_b32 v23, 0\n"
      "s_waitcnt vmcnt(20)\n"
      "s_waitcnt lgkmcnt(0)\n"
      "ds_read_b64_tr_b16 v[160:161], v184\n"
      "ds_read_b64_tr_b16 v[162:163], v192\n"
      "ds_read_b64_tr_b16 v[164:165], v185\n"
      "ds_read_b64_tr_b16 v[166:167], v193\n"
      "ds_read_b64_tr_b16 v[168:169], v186\n"
      "ds_read_b64_tr_b16 v[170:171], v194\n"
      "ds_read_b64_tr_b16 v[172:173], v187\n"
      "ds_read_b64_tr_b16 v[174:175], v195\n"
      "ds_read_b64_tr_b16 v[176:177], v188\n"
      "ds_read_b64_tr_b16 v[178:179], v196\n"
      "ds_read_b64_tr_b16 v[180:181], v189\n"
      "ds_read_b64_tr_b16 v[182:183], v197\n"
      "ds_read_b64_tr_b16 v[112:113], v190\n"
      "ds_read_b64_tr_b16 v[114:115], v198\n"
      "ds_read_b64_tr_b16 v[116:117], v191\n"
      "ds_read_b64_tr_b16 v[118:119], v199\n"
      "s_cmp_eq_u32 s22, 56\n"
      "s_cselect_b32 s64, 0xf0000000, 0\n"
      "s_mov_b32 s65, s64\n"
      "s_cmp_eq_u32 s22, 64\n"
      "s_cselect_b32 s86, 0xff000000, s64\n"
      "s_mov_b32 s87, s86\n"
      "s_waitcnt lgkmcnt(15)\n"
      "v_cndmask_b32 v160, v160, 0, s[64:65]\n"
      "v_cndmask_b32 v161, v161, 0, s[64:65]\n"
      "s_or_b64 s[90:91], s[64:65], s[88:89]\n"
      "s_waitcnt lgkmcnt(14)\n"
      "v_cndmask_b32 v162, v162, 0, s[90:91]\n"
      "v_cndmask_b32 v163, v163, 0, s[90:91]\n"
      "v_dot2c_f32_bf16 v16, v210, v160\n"
      "v_dot2c_f32_bf16 v16, v210, v161\n"
      "v_dot2c_f32_bf16 v16, v210, v162\n"
      "v_dot2c_f32_bf16 v16, v210, v163\n"
      "v_accvgpr_write_b32 a64, v160\n"
      "v_accvgpr_write_b32 a65, v161\n"
      "v_accvgpr_write_b32 a66, v162\n"
      "v_accvgpr_write_b32 a67, v163\n"
      "s_waitcnt lgkmcnt(13)\n"
      "v_cndmask_b32 v164, v164, 0, s[86:87]\n"
      "v_cndmask_b32 v165, v165, 0, s[86:87]\n"
      "s_or_b64 s[90:91], s[86:87], s[88:89]\n"
      "s_waitcnt lgkmcnt(12)\n"
      "v_cndmask_b32 v166, v166, 0, s[90:91]\n"
      "v_cndmask_b32 v167, v167, 0, s[90:91]\n"
      "v_dot2c_f32_bf16 v17, v210, v164\n"
      "v_dot2c_f32_bf16 v17, v210, v165\n"
      "v_dot2c_f32_bf16 v17, v210, v166\n"
      "v_dot2c_f32_bf16 v17, v210, v167\n"
      "v_accvgpr_write_b32 a68, v164\n"
      "v_accvgpr_write_b32 a69, v165\n"
      "v_accvgpr_write_b32 a70, v166\n"
      "v_accvgpr_write_b32 a71, v167\n"
      "s_waitcnt lgkmcnt(11)\n"
      "v_cndmask_b32 v168, v168, 0, s[64:65]\n"
      "v_cndmask_b32 v169, v169, 0, s[64:65]\n"
      "s_or_b64 s[90:91], s[64:65], s[88:89]\n"
      "s_waitcnt lgkmcnt(10)\n"
      "v_cndmask_b32 v170, v170, 0, s[90:91]\n"
      "v_cndmask_b32 v171, v171, 0, s[90:91]\n"
      "v_dot2c_f32_bf16 v18, v210, v168\n"
      "v_dot2c_f32_bf16 v18, v210, v169\n"
      "v_dot2c_f32_bf16 v18, v210, v170\n"
      "v_dot2c_f32_bf16 v18, v210, v171\n"
      "v_accvgpr_write_b32 a72, v168\n"
      "v_accvgpr_write_b32 a73, v169\n"
      "v_accvgpr_write_b32 a74, v170\n"
      "v_accvgpr_write_b32 a75, v171\n"
      "s_waitcnt lgkmcnt(9)\n"
      "v_cndmask_b32 v172, v172, 0, s[86:87]\n"
      "v_cndmask_b32 v173, v173, 0, s[86:87]\n"
      "s_or_b64 s[90:91], s[86:87], s[88:89]\n"
      "s_waitcnt lgkmcnt(8)\n"
      "v_cndmask_b32 v174, v174, 0, s[90:91]\n"
      "v_cndmask_b32 v175, v175, 0, s[90:91]\n"
      "v_dot2c_f32_bf16 v19, v210, v172\n"
      "v_dot2c_f32_bf16 v19, v210, v173\n"
      "v_dot2c_f32_bf16 v19, v210, v174\n"
      "v_dot2c_f32_bf16 v19, v210, v175\n"
      "v_accvgpr_write_b32 a76, v172\n"
      "v_accvgpr_write_b32 a77, v173\n"
      "v_accvgpr_write_b32 a78, v174\n"
      "v_accvgpr_write_b32 a79, v175\n"
      "s_waitcnt lgkmcnt(7)\n"
      "v_cndmask_b32 v176, v176, 0, s[64:65]\n"
      "v_cndmask_b32 v177, v177, 0, s[64:65]\n"
      "s_or_b64 s[90:91], s[64:65], s[88:89]\n"
      "s_waitcnt lgkmcnt(6)\n"
      "v_cndmask_b32 v178, v178, 0, s[90:91]\n"
      "v_cndmask_b32 v179, v179, 0, s[90:91]\n"
      "v_dot2c_f32_bf16 v20, v210, v176\n"
      "v_dot2c_f32_bf16 v20, v210, v177\n"
      "v_dot2c_f32_bf16 v20, v210, v178\n"
      "v_dot2c_f32_bf16 v20, v210, v179\n"
      "v_accvgpr_write_b32 a80, v176\n"
      "v_accvgpr_write_b32 a81, v177\n"
      "v_accvgpr_write_b32 a82, v178\n"
      "v_accvgpr_write_b32 a83, v179\n"
      "s_waitcnt lgkmcnt(5)\n"
      "v_cndmask_b32 v180, v180, 0, s[86:87]\n"
      "v_cndmask_b32 v181, v181, 0, s[86:87]\n"
      "s_or_b64 s[90:91], s[86:87], s[88:89]\n"
      "s_waitcnt lgkmcnt(4)\n"
      "v_cndmask_b32 v182, v182, 0, s[90:91]\n"
      "v_cndmask_b32 v183, v183, 0, s[90:91]\n"
      "v_dot2c_f32_bf16 v21, v210, v180\n"
      "v_dot2c_f32_bf16 v21, v210, v181\n"
      "v_dot2c_f32_bf16 v21, v210, v182\n"
      "v_dot2c_f32_bf16 v21, v210, v183\n"
      "v_accvgpr_write_b32 a84, v180\n"
      "v_accvgpr_write_b32 a85, v181\n"
      "v_accvgpr_write_b32 a86, v182\n"
      "v_accvgpr_write_b32 a87, v183\n"
      "s_waitcnt lgkmcnt(3)\n"
      "v_cndmask_b32 v112, v112, 0, s[64:65]\n"
      "v_cndmask_b32 v113, v113, 0, s[64:65]\n"
      "s_or_b64 s[90:91], s[64:65], s[88:89]\n"
      "s_waitcnt lgkmcnt(2)\n"
      "v_cndmask_b32 v114, v114, 0, s[90:91]\n"
      "v_cndmask_b32 v115, v115, 0, s[90:91]\n"
      "v_dot2c_f32_bf16 v22, v210, v112\n"
      "v_dot2c_f32_bf16 v22, v210, v113\n"
      "v_dot2c_f32_bf16 v22, v210, v114\n"
      "v_dot2c_f32_bf16 v22, v210, v115\n"
      "v_accvgpr_write_b32 a88, v112\n"
      "v_accvgpr_write_b32 a89, v113\n"
      "v_accvgpr_write_b32 a90, v114\n"
      "v_accvgpr_write_b32 a91, v115\n"
      "s_waitcnt lgkmcnt(1)\n"
      "v_cndmask_b32 v116, v116, 0, s[86:87]\n"
      "v_cndmask_b32 v117, v117, 0, s[86:87]\n"
      "s_or_b64 s[90:91], s[86:87], s[88:89]\n"
      "s_waitcnt lgkmcnt(0)\n"
      "v_cndmask_b32 v118, v118, 0, s[90:91]\n"
      "v_cndmask_b32 v119, v119, 0, s[90:91]\n"
      "v_dot2c_f32_bf16 v23, v210, v116\n"
      "v_dot2c_f32_bf16 v23, v210, v117\n"
      "v_dot2c_f32_bf16 v23, v210, v118\n"
      "v_dot2c_f32_bf16 v23, v210, v119\n"
      "v_accvgpr_write_b32 a92, v116\n"
      "v_accvgpr_write_b32 a93, v117\n"
      "v_accvgpr_write_b32 a94, v118\n"
      "v_accvgpr_write_b32 a95, v119\n"
      "s_nop 2\n"
      "v_permlane32_swap_b32 v16, v17\n"
      "v_permlane32_swap_b32 v18, v19\n"
      "v_permlane32_swap_b32 v20, v21\n"
      "v_permlane32_swap_b32 v22, v23\n"
      "v_add_f32 v16, v16, v17\n"
      "v_add_f32 v18, v18, v19\n"
      "v_add_f32 v20, v20, v21\n"
      "v_add_f32 v22, v22, v23\n"
      "v_accvgpr_write_b32 a96, v16\n"
      "v_accvgpr_write_b32 a97, v18\n"
      "v_accvgpr_write_b32 a98, v20\n"
      "v_accvgpr_write_b32 a99, v22\n"
      "buffer_store_dwordx4 a[64:67], v224, s[36:39], s24 offen sc1\n"
      "s_add_u32 s24, s24, s26\n"
      "buffer_store_dwordx4 a[68:71], v224, s[36:39], s25 offen sc1\n"
      "s_add_u32 s25, s25, s26\n"
      "buffer_store_dwordx4 a[72:75], v224, s[36:39], s24 offen sc1\n"
      "s_add_u32 s24, s24, s26\n"
      "buffer_store_dwordx4 a[76:79], v224, s[36:39], s25 offen sc1\n"
      "s_add_u32 s25, s25, s26\n"
      "buffer_store_dwordx4 a[80:83], v224, s[36:39], s24 offen sc1\n"
      "s_add_u32 s24, s24, s26\n"
      "buffer_store_dwordx4 a[84:87], v224, s[36:39], s25 offen sc1\n"
      "s_add_u32 s25, s25, s26\n"
      "buffer_store_dwordx4 a[88:91], v224, s[36:39], s24 offen sc1\n"
      "s_add_u32 s24, s24, s26\n"
      "buffer_store_dwordx4 a[92:95], v224, s[36:39], s25 offen sc1\n"
      "s_add_u32 s25, s25, s26\n"
      "buffer_store_dword a96, v209, s[36:39], s27 offen sc1\n"
      "s_add_u32 s27, s27, s96\n"
      "buffer_store_dword a97, v209, s[36:39], s27 offen sc1\n"
      "s_add_u32 s27, s27, s96\n"
      "buffer_store_dword a98, v209, s[36:39], s27 offen sc1\n"
      "s_add_u32 s27, s27, s96\n"
      "buffer_store_dword a99, v209, s[36:39], s27 offen sc1\n"
      "s_add_u32 s27, s27, s96\n"
      "s_mov_b32 s23, 0\n"
      "v_mov_b32 v209, 0x80000000\n"
      ".Lsk3:\n"
      "s_add_u32 s71, s71, 0x1c00\n"
      "s_add_u32 m0, s72, 0x3800\n"
      "s_nop 0\n"
      "buffer_load_dwordx4 v8, s[44:47], s71 offen lds\n"
      "s_add_u32 m0, s72, 0x3c00\n"
      "s_nop 0\n"
      "buffer_load_dwordx4 v9, s[44:47], s71 offen lds\n"
      "s_add_u32 m0, s72, 0x4000\n"
      "s_nop 0\n"
      "buffer_load_dwordx4 v10, s[44:47], s71 offen lds\n"
      "s_add_u32 m0, s72, 0x4400\n"
      "s_nop 0\n"
      "buffer_load_dwordx4 v11, s[44:47], s71 offen lds\n"
      "s_lshl_b32 s85, s79, 1\n"
      "s_add_u32 s85, s85, s81\n"
      "s_cmp_ge_u32 s85, 2\n"
      "s_cselect_b32 s86, 1, 0\n"
      "s_add_u32 s85, s85, s86\n"
      "s_mul_i32 s85, s85, 0x7000\n"
      "v_mul_u32_u24 v2, 0x380, v0\n"
      "v_lshl_add_u32 v2, v1, 3, v2\n"
      "v_add_u32 v2, s85, v2\n"
      "v_and_b32 v3, 7, v0\n"
      "s_mul_i32 s85, s78, 7\n"
      "s_lshl_b32 s86, s80, 2\n"
      "s_add_u32 s85, s85, s86\n"
      "s_add_u32 s86, s85, 0\n"
      "s_min_u32 s86, s86, 55\n"
      "v_xor_b32 v4, s86, v3\n"
      "v_lshl_add_u32 v136, v4, 4, v2\n"
      "s_add_u32 s86, s85, 1\n"
      "s_min_u32 s86, s86, 55\n"
      "v_xor_b32 v4, s86, v3\n"
      "v_lshl_add_u32 v137, v4, 4, v2\n"
      "s_add_u32 s86, s85, 2\n"
      "s_min_u32 s86, s86, 55\n"
      "v_xor_b32 v4, s86, v3\n"
      "v_lshl_add_u32 v138, v4, 4, v2\n"
      "s_add_u32 s86, s85, 3\n"
      "s_min_u32 s86, s86, 55\n"
      "v_xor_b32 v4, s86, v3\n"
      "v_lshl_add_u32 v139, v4, 4, v2\n"
      "v_accvgpr_write_b32 a100, 0\n"
      "v_accvgpr_write_b32 a101, 0\n"
      "v_accvgpr_write_b32 a102, 0\n"
      "v_accvgpr_write_b32 a103, 0\n"
      "s_add_u32 s54, s54, 1\n"
      "s_addc_u32 s55, s55, 0\n"
      "s_waitcnt vmcnt(4)\n"
      "s_barrier\n"
      "v_mov_b32 v16, s54\n"
      "v_mov_b32 v17, s55\n"
      "v_cmp_eq_u32 vcc, 0, v238\n"
      "v_mov_b32 v18, 0x80000000\n"
      "s_nop 0\n"
      "v_cndmask_b32 v18, v18, 0, vcc\n"
      "s_lshl_b32 s87, s52, 3\n"
      "s_add_u32 s87, s87, 0x1000\n"
      "s_cmp_eq_u32 s51, 0\n"
      "s_cselect_b32 s86, 0, 0x80000000\n"
      "v_or_b32 v18, s86, v18\n"
      "buffer_store_dwordx2 v[16:17], v18, s[36:39], s87 offen sc1\n"
      "s_add_u32 m0, s72, 0x4800\n"
      "s_nop 0\n"
      "buffer_load_dwordx4 v12, s[44:47], s71 offen lds\n"
      "s_add_u32 m0, s72, 0x4c00\n"
      "s_nop 0\n"
      "buffer_load_dwordx4 v13, s[44:47], s71 offen lds\n"
      "s_add_u32 m0, s72, 0x5000\n"
      "s_nop 0\n"
      "buffer_load_dwordx4 v14, s[44:47], s71 offen lds\n"
      "s_add_u32 s71, s71, 0x1c00\n"
      "s_add_u32 m0, s72, 0x5400\n"
      "s_nop 0\n"
      "buffer_load_dwordx4 v8, s[44:47], s71 offen lds\n"
      "s_add_u32 m0, s72, 0x5800\n"
      "s_nop 0\n"
      "buffer_load_dwordx4 v9, s[44:47], s71 offen lds\n"
      "s_add_u32 m0, s72, 0x5c00\n"
      "s_nop 0\n"
      "buffer_load_dwordx4 v10, s[44:47], s71 offen lds\n"
      "s_add_u32 m0, s72, 0x6000\n"
      "s_nop 0\n"
      "buffer_load_dwordx4 v11, s[44:47], s71 offen lds\n"
      "s_add_u32 m0, s72, 0x6400\n"
      "s_nop 0\n"
      "buffer_load_dwordx4 v12, s[44:47], s71 offen lds\n"
      "s_add_u32 m0, s72, 0x6800\n"
      "s_nop 0\n"
      "buffer_load_dwordx4 v13, s[44:47], s71 offen lds\n"
      "s_add_u32 m0, s72, 0x6c00\n"
      "s_nop 0\n"
      "buffer_load_dwordx4 v14, s[44:47], s71 offen lds\n"
      "s_cmp_lg_u32 s33, 0\n"
      "s_cbranch_scc1 .Lsk4\n"
      "s_add_u32 m0, s70, 0\n"
      "s_nop 0\n"
      "buffer_load_dwordx4 v184, s[40:43], s20 offen lds\n"
      "s_add_u32 m0, s70, 0x400\n"
      "s_nop 0\n"
      "buffer_load_dwordx4 v185, s[40:43], s20 offen lds\n"
      "s_add_u32 m0, s70, 0x800\n"
      "s_nop 0\n"
      "buffer_load_dwordx4 v186, s[40:43], s20 offen lds\n"
      "s_add_u32 m0, s70, 0xc00\n"
      "s_nop 0\n"
      "buffer_load_dwordx4 v187, s[40:43], s20 offen lds\n"
      "s_add_u32 m0, s70, 0x1000\n"
      "s_nop 0\n"
      "buffer_load_dwordx4 v188, s[92:95], s20 offen lds\n"
      "s_add_u32 m0, s70, 0x1400\n"
      "s_nop 0\n"
      "buffer_load_dwordx4 v189, s[92:95], s20 offen lds\n"
      "s_add_u32 m0, s70, 0x1800\n"
      "s_nop 0\n"
      "buffer_load_dwordx4 v190, s[92:95], s20 offen lds\n"
      "s_add_u32 m0, s70, 0x1c00\n"
      "s_nop 0\n"
      "buffer_load_dwordx4 v191, s[92:95], s20 offen lds\n"
      ".Lsk4:\n"
      "s_mov_b32 s71, 0xf20000\n"
      "s_mul_i32 s85, s50, 0x7000\n"
      "s_add_u32 s71, s71, s85\n"
      "s_mul_i32 s85, s51, 0x1c00\n"
      "s_add_u32 s71, s71, s85\n"
      "s_mov_b32 s72, 0x1230000\n"
      "s_mul_i32 s85, s78, 0x38000\n"
      "s_add_u32 s72, s72, s85\n"
      "s_mul_i32 s85, s4, 0xe000\n"
      "s_add_u32 s72, s72, s85\n"
      "s_mov_b32 s70, 0x628000\n"
      "s_mul_i32 s85, s84, 0x8000\n"
      "s_add_u32 s70, s70, s85\n"
      "s_mul_i32 s85, s81, 0xffff8000\n"
      "s_add_u32 s70, s70, s85\n"
      "s_mul_i32 s85, s4, 0x2000\n"
      "s_add_u32 s70, s70, s85\n"
      "s_mov_b32 s92, 0x3fb8aa3b\n"
      "s_mov_b32 s93, 0x3fb8aa3b\n"
      "s_mov_b32 s94, 0x358637bd\n"
      "s_mov_b32 s95, 0x358637bd\n"
      "s_xor_b32 s6, s5, 0\n"
      "s_mul_i32 s6, s6, 0x3800\n"
      "s_add_u32 s6, s6, s72\n"
      "s_xor_b32 s7, s5, 1\n"
      "s_mul_i32 s7, s7, 0x3800\n"
      "s_add_u32 s7, s7, s72\n"
      "s_xor_b32 s8, s5, 2\n"
      "s_mul_i32 s8, s8, 0x3800\n"
      "s_add_u32 s8, s8, s72\n"
      "s_xor_b32 s9, s5, 3\n"
      "s_mul_i32 s9, s9, 0x3800\n"
      "s_add_u32 s9, s9, s72\n"
      "s_mov_b64 s[28:29], s[36:37]\n"
      "s_cmp_eq_u32 s23, 0\n"
      "s_cselect_b32 s30, 0, 0x16da000\n"
      "s_mov_b64 s[40:41], s[36:37]\n"
      "s_mov_b64 s[42:43], s[38:39]\n"
      "s_mov_b64 s[44:45], s[16:17]\n"
      "s_and_b32 s45, s45, 0xffff\n"
      "s_min_u32 s86, s51, 2\n"
      "s_mov_b32 s53, 0x15c0000\n"
      "s_mul_i32 s85, s50, 0x2a00\n"
      "s_add_u32 s53, s53, s85\n"
      "s_mul_i32 s85, s86, 0xe00\n"
      "s_add_u32 s53, s53, s85\n"
      "v_mul_u32_u24 v2, 0x700, v1\n"
      "v_lshl_add_u32 v204, v0, 2, v2\n"
      "s_waitcnt vmcnt(8)\n"
      "s_cmp_eq_u32 s33, 0\n"
      "s_cbranch_scc1 .Lsk5\n"
      "s_waitcnt vmcnt(0)\n"
      ".Lsk5:\n"
      "ds_read_b128 v[0:3], v128\n"
      "ds_read_b128 v[4:7], v129\n"
      "ds_read_b128 v[8:11], v130\n"
      "ds_read_b128 v[12:15], v131\n"
      "ds_read_b128 v[16:19], v128 offset:128\n"
      "ds_read_b128 v[20:23], v129 offset:128\n"
      "ds_read_b128 v[24:27], v130 offset:128\n"
      "ds_read_b128 v[28:31], v131 offset:128\n"
      "ds_read_b128 v[32:35], v128 offset:256\n"
      "ds_read_b128 v[36:39], v129 offset:256\n"
      "ds_read_b128 v[40:43], v130 offset:256\n"
      "ds_read_b128 v[44:47], v131 offset:256\n"
      "ds_read_b128 v[48:51], v128 offset:384\n"
      "ds_read_b128 v[52:55], v129 offset:384\n"
      "ds_read_b128 v[56:59], v130 offset:384\n"
      "ds_read_b128 v[60:63], v131 offset:384\n"
      "ds_read_b128 v[64:67], v128 offset:512\n"
      "ds_read_b128 v[68:71], v129 offset:512\n"
      "ds_read_b128 v[72:75], v130 offset:512\n"
      "ds_read_b128 v[76:79], v131 offset:512\n"
      "ds_read_b128 v[80:83], v128 offset:640\n"
      "ds_read_b128 v[84:87], v129 offset:640\n"
      "ds_read_b128 v[88:91], v130 offset:640\n"
      "ds_read_b128 v[92:95], v131 offset:640\n"
      "ds_read_b128 v[96:99], v128 offset:768\n"
      "ds_read_b128 v[100:103], v129 offset:768\n"
      "ds_read_b128 v[104:107], v130 offset:768\n"
      "ds_read_b128 v[108:111], v131 offset:768\n"
      "s_waitcnt lgkmcnt(0)\n"
      "s_barrier\n"
      "ds_read_b64 v[240:241], v136\n"
      "ds_read_b64 v[242:243], v137\n"
      "ds_read_b64 v[244:245], v138\n"
      "ds_read_b64 v[246:247], v139\n"
      "v_dot2c_f32_bf16 v200, v210, v0\n"
      "v_dot2c_f32_bf16 v201, v0, v0\n"
      "v_dot2c_f32_bf16 v202, v210, v1\n"
      "v_dot2c_f32_bf16 v203, v1, v1\n"
      "v_dot2c_f32_bf16 v200, v210, v2\n"
      "v_dot2c_f32_bf16 v201, v2, v2\n"
      "v_dot2c_f32_bf16 v202, v210, v3\n"
      "v_dot2c_f32_bf16 v203, v3, v3\n"
      "v_dot2c_f32_bf16 v200, v210, v4\n"
      "v_dot2c_f32_bf16 v201, v4, v4\n"
      "v_dot2c_f32_bf16 v202, v210, v5\n"
      "v_dot2c_f32_bf16 v203, v5, v5\n"
      "v_dot2c_f32_bf16 v200, v210, v6\n"
      "v_dot2c_f32_bf16 v201, v6, v6\n"
      "v_dot2c_f32_bf16 v202, v210, v7\n"
      "v_dot2c_f32_bf16 v203, v7, v7\n"
      "v_dot2c_f32_bf16 v200, v210, v8\n"
      "v_dot2c_f32_bf16 v201, v8, v8\n"
      "v_dot2c_f32_bf16 v202, v210, v9\n"
      "v_dot2c_f32_bf16 v203, v9, v9\n"
      "v_dot2c_f32_bf16 v200, v210, v10\n"
      "v_dot2c_f32_bf16 v201, v10, v10\n"
      "v_dot2c_f32_bf16 v202, v210, v11\n"
      "v_dot2c_f32_bf16 v203, v11, v11\n"
      "v_dot2c_f32_bf16 v200, v210, v12\n"
      "v_dot2c_f32_bf16 v201, v12, v12\n"
      "v_dot2c_f32_bf16 v202, v210, v13\n"
      "v_dot2c_f32_bf16 v203, v13, v13\n"
      "v_dot2c_f32_bf16 v200, v210, v14\n"
      "v_dot2c_f32_bf16 v201, v14, v14\n"
      "v_dot2c_f32_bf16 v202, v210, v15\n"
      "v_dot2c_f32_bf16 v203, v15, v15\n"
      "v_dot2c_f32_bf16 v200, v210, v16\n"
      "v_dot2c_f32_bf16 v201, v16, v16\n"
      "v_dot2c_f32_bf16 v202, v210, v17\n"
      "v_dot2c_f32_bf16 v203, v17, v17\n"
      "v_dot2c_f32_bf16 v200, v210, v18\n"
      "v_dot2c_f32_bf16 v201, v18, v18\n"
      "v_dot2c_f32_bf16 v202, v210, v19\n"
      "v_dot2c_f32_bf16 v203, v19, v19\n"
      "s_mov_b32 s87, 0x1000\n"
      "buffer_load_dwordx4 v[128:131], v224, s[36:39], s87 offen sc1\n"
      "buffer_load_dwordx4 v[132:135], v224, s[36:39], s87 offen offset:1024 sc1\n"
      "s_cmp_lg_u32 s33, 0\n"
      "s_cbranch_scc1 .Lsk8\n"
      "v_mov_b32 v184, v192\n"
      "v_add_u32 v185, s22, v192\n"
      "v_add_u32 v186, s23, v184\n"
      "v_add_u32 v187, s23, v185\n"
      "v_add_u32 v188, s23, v186\n"
      "v_add_u32 v189, s23, v187\n"
      "v_add_u32 v190, s23, v188\n"
      "v_add_u32 v191, s23, v189\n"
      "v_add_u32 v192, s21, v184\n"
      "v_add_u32 v193, s21, v185\n"
      "v_add_u32 v194, s21, v186\n"
      "v_add_u32 v195, s21, v187\n"
      "v_add_u32 v196, s21, v188\n"
      "v_add_u32 v197, s21, v189\n"
      "v_add_u32 v198, s21, v190\n"
      "v_add_u32 v199, s21, v191\n"
      "v_mov_b32 v136, 0\n"
      "v_mov_b32 v137, 0\n"
      "v_mov_b32 v138, 0\n"
      "v_mov_b32 v139, 0\n"
      "v_mov_b32 v140, 0\n"
      "v_mov_b32 v141, 0\n"
      "v_mov_b32 v142, 0\n"
      "v_mov_b32 v143, 0\n"
      "s_waitcnt vmcnt(2)\n"
      "s_waitcnt lgkmcnt(0)\n"
      "ds_read_b64_tr_b16 v[160:161], v184\n"
      "ds_read_b64_tr_b16 v[162:163], v192\n"
      "ds_read_b64_tr_b16 v[164:165], v185\n"
      "ds_read_b64_tr_b16 v[166:167], v193\n"
      "ds_read_b64_tr_b16 v[168:169], v186\n"
      "ds_read_b64_tr_b16 v[170:171], v194\n"
      "ds_read_b64_tr_b16 v[172:173], v187\n"
      "ds_read_b64_tr_b16 v[174:175], v195\n"
      "ds_read_b64_tr_b16 v[176:177], v188\n"
      "ds_read_b64_tr_b16 v[178:179], v196\n"
      "ds_read_b64_tr_b16 v[180:181], v189\n"
      "ds_read_b64_tr_b16 v[182:183], v197\n"
      "ds_read_b64_tr_b16 v[112:113], v190\n"
      "ds_read_b64_tr_b16 v[114:115], v198\n"
      "ds_read_b64_tr_b16 v[116:117], v191\n"
      "ds_read_b64_tr_b16 v[118:119], v199\n"
      "s_cmp_eq_u32 s22, 56\n"
      "s_cselect_b32 s64, 0xf0000000, 0\n"
      "s_mov_b32 s65, s64\n"
      "s_cmp_eq_u32 s22, 64\n"
      "s_cselect_b32 s86, 0xff000000, s64\n"
      "s_mov_b32 s87, s86\n"
      "s_waitcnt lgkmcnt(15)\n"
      "v_cndmask_b32 v160, v160, 0, s[64:65]\n"
      "v_cndmask_b32 v161, v161, 0, s[64:65]\n"
      "s_or_b64 s[90:91], s[64:65], s[88:89]\n"
      "s_waitcnt lgkmcnt(14)\n"
      "v_cndmask_b32 v162, v162, 0, s[90:91]\n"
      "v_cndmask_b32 v163, v163, 0, s[90:91]\n"
      "v_dot2c_f32_bf16 v136, v210, v160\n"
      "v_dot2c_f32_bf16 v136, v210, v161\n"
      "v_dot2c_f32_bf16 v136, v210, v162\n"
      "v_dot2c_f32_bf16 v136, v210, v163\n"
      "v_accvgpr_write_b32 a64, v160\n"
      "v_accvgpr_write_b32 a65, v161\n"
      "v_accvgpr_write_b32 a66, v162\n"
      "v_accvgpr_write_b32 a67, v163\n"
      "s_waitcnt lgkmcnt(13)\n"
      "v_cndmask_b32 v164, v164, 0, s[86:87]\n"
      "v_cndmask_b32 v165, v165, 0, s[86:87]\n"
      "s_or_b64 s[90:91], s[86:87], s[88:89]\n"
      "s_waitcnt lgkmcnt(12)\n"
      "v_cndmask_b32 v166, v166, 0, s[90:91]\n"
      "v_cndmask_b32 v167, v167, 0, s[90:91]\n"
      "v_dot2c_f32_bf16 v137, v210, v164\n"
      "v_dot2c_f32_bf16 v137, v210, v165\n"
      "v_dot2c_f32_bf16 v137, v210, v166\n"
      "v_dot2c_f32_bf16 v137, v210, v167\n"
      "v_accvgpr_write_b32 a68, v164\n"
      "v_accvgpr_write_b32 a69, v165\n"
      "v_accvgpr_write_b32 a70, v166\n"
      "v_accvgpr_write_b32 a71, v167\n"
      "s_waitcnt lgkmcnt(11)\n"
      "v_cndmask_b32 v168, v168, 0, s[64:65]\n"
      "v_cndmask_b32 v169, v169, 0, s[64:65]\n"
      "s_or_b64 s[90:91], s[64:65], s[88:89]\n"
      "s_waitcnt lgkmcnt(10)\n"
      "v_cndmask_b32 v170, v170, 0, s[90:91]\n"
      "v_cndmask_b32 v171, v171, 0, s[90:91]\n"
      "v_dot2c_f32_bf16 v138, v210, v168\n"
      "v_dot2c_f32_bf16 v138, v210, v169\n"
      "v_dot2c_f32_bf16 v138, v210, v170\n"
      "v_dot2c_f32_bf16 v138, v210, v171\n"
      "v_accvgpr_write_b32 a72, v168\n"
      "v_accvgpr_write_b32 a73, v169\n"
      "v_accvgpr_write_b32 a74, v170\n"
      "v_accvgpr_write_b32 a75, v171\n"
      "s_waitcnt lgkmcnt(9)\n"
      "v_cndmask_b32 v172, v172, 0, s[86:87]\n"
      "v_cndmask_b32 v173, v173, 0, s[86:87]\n"
      "s_or_b64 s[90:91], s[86:87], s[88:89]\n"
      "s_waitcnt lgkmcnt(8)\n"
      "v_cndmask_b32 v174, v174, 0, s[90:91]\n"
      "v_cndmask_b32 v175, v175, 0, s[90:91]\n"
      "v_dot2c_f32_bf16 v139, v210, v172\n"
      "v_dot2c_f32_bf16 v139, v210, v173\n"
      "v_dot2c_f32_bf16 v139, v210, v174\n"
      "v_dot2c_f32_bf16 v139, v210, v175\n"
      "v_accvgpr_write_b32 a76, v172\n"
      "v_accvgpr_write_b32 a77, v173\n"
      "v_accvgpr_write_b32 a78, v174\n"
      "v_accvgpr_write_b32 a79, v175\n"
      "s_waitcnt lgkmcnt(7)\n"
      "v_cndmask_b32 v176, v176, 0, s[64:65]\n"
      "v_cndmask_b32 v177, v177, 0, s[64:65]\n"
      "s_or_b64 s[90:91], s[64:65], s[88:89]\n"
      "s_waitcnt lgkmcnt(6)\n"
      "v_cndmask_b32 v178, v178, 0, s[90:91]\n"
      "v_cndmask_b32 v179, v179, 0, s[90:91]\n"
      "v_dot2c_f32_bf16 v140, v210, v176\n"
      "v_dot2c_f32_bf16 v140, v210, v177\n"
      "v_dot2c_f32_bf16 v140, v210, v178\n"
      "v_dot2c_f32_bf16 v140, v210, v179\n"
      "v_accvgpr_write_b32 a80, v176\n"
      "v_accvgpr_write_b32 a81, v177\n"
      "v_accvgpr_write_b32 a82, v178\n"
      "v_accvgpr_write_b32 a83, v179\n"
      "s_waitcnt lgkmcnt(5)\n"
      "v_cndmask_b32 v180, v180, 0, s[86:87]\n"
      "v_cndmask_b32 v181, v181, 0, s[86:87]\n"
      "s_or_b64 s[90:91], s[86:87], s[88:89]\n"
      "s_waitcnt lgkmcnt(4)\n"
      "v_cndmask_b32 v182, v182, 0, s[90:91]\n"
      "v_cndmask_b32 v183, v183, 0, s[90:91]\n"
      "v_dot2c_f32_bf16 v141, v210, v180\n"
      "v_dot2c_f32_bf16 v141, v210, v181\n"
      "v_dot2c_f32_bf16 v141, v210, v182\n"
      "v_dot2c_f32_bf16 v141, v210, v183\n"
      "v_accvgpr_write_b32 a84, v180\n"
      "v_accvgpr_write_b32 a85, v181\n"
      "v_accvgpr_write_b32 a86, v182\n"
      "v_accvgpr_write_b32 a87, v183\n"
      "s_waitcnt lgkmcnt(3)\n"
      "v_cndmask_b32 v112, v112, 0, s[64:65]\n"
      "v_cndmask_b32 v113, v113, 0, s[64:65]\n"
      "s_or_b64 s[90:91], s[64:65], s[88:89]\n"
      "s_waitcnt lgkmcnt(2)\n"
      "v_cndmask_b32 v114, v114, 0, s[90:91]\n"
      "v_cndmask_b32 v115, v115, 0, s[90:91]\n"
      "v_dot2c_f32_bf16 v142, v210, v112\n"
      "v_dot2c_f32_bf16 v142, v210, v113\n"
      "v_dot2c_f32_bf16 v142, v210, v114\n"
      "v_dot2c_f32_bf16 v142, v210, v115\n"
      "v_accvgpr_write_b32 a88, v112\n"
      "v_accvgpr_write_b32 a89, v113\n"
      "v_accvgpr_write_b32 a90, v114\n"
      "v_accvgpr_write_b32 a91, v115\n"
      "s_waitcnt lgkmcnt(1)\n"
      "v_cndmask_b32 v116, v116, 0, s[86:87]\n"
      "v_cndmask_b32 v117, v117, 0, s[86:87]\n"
      "s_or_b64 s[90:91], s[86:87], s[88:89]\n"
      "s_waitcnt lgkmcnt(0)\n"
      "v_cndmask_b32 v118, v118, 0, s[90:91]\n"
      "v_cndmask_b32 v119, v119, 0, s[90:91]\n"
      "v_dot2c_f32_bf16 v143, v210, v116\n"
      "v_dot2c_f32_bf16 v143, v210, v117\n"
      "v_dot2c_f32_bf16 v143, v210, v118\n"
      "v_dot2c_f32_bf16 v143, v210, v119\n"
      "v_accvgpr_write_b32 a92, v116\n"
      "v_accvgpr_write_b32 a93, v117\n"
      "v_accvgpr_write_b32 a94, v118\n"
      "v_accvgpr_write_b32 a95, v119\n"
      "s_nop 2\n"
      "v_permlane32_swap_b32 v136, v137\n"
      "v_permlane32_swap_b32 v138, v139\n"
      "v_permlane32_swap_b32 v140, v141\n"
      "v_permlane32_swap_b32 v142, v143\n"
      "v_add_f32 v136, v136, v137\n"
      "v_add_f32 v138, v138, v139\n"
      "v_add_f32 v140, v140, v141\n"
      "v_add_f32 v142, v142, v143\n"
      "v_accvgpr_write_b32 a96, v136\n"
      "v_accvgpr_write_b32 a97, v138\n"
      "v_accvgpr_write_b32 a98, v140\n"
      "v_accvgpr_write_b32 a99, v142\n"
      ".Lsk8:\n"
      "s_mov_b32 s88, 0x620000\n"
      "s_mul_i32 s85, s83, 0x8000\n"
      "s_add_u32 s88, s88, s85\n"
      "s_mul_i32 s85, s4, 0x2000\n"
      "s_add_u32 s88, s88, s85\n"
      "s_mov_b32 s89, 0x820000\n"
      "s_mul_i32 s85, s83, 0x1c000\n"
      "s_add_u32 s89, s89, s85\n"
      "s_mul_i32 s85, s4, 0x7000\n"
      "s_add_u32 s89, s89, s85\n"
      "s_waitcnt vmcnt(1)\n"
      "v_cmp_ne_u64 vcc, s[54:55], v[128:129]\n"
      "v_cmp_ne_u64 s[90:91], s[54:55], v[130:131]\n"
      "s_or_b64 vcc, vcc, s[90:91]\n"
      "s_waitcnt vmcnt(0)\n"
      "v_cmp_ne_u64 s[90:91], s[54:55], v[132:133]\n"
      "s_or_b64 vcc, vcc, s[90:91]\n"
      "v_cmp_ne_u64 s[90:91], s[54:55], v[134:135]\n"
      "s_or_b64 vcc, vcc, s[90:91]\n"
      "s_cbranch_vccz .Lfa7\n"
      ".Lfa6:\n"
      "s_mov_b32 s87, 0x1000\n"
      "buffer_load_dwordx4 v[128:131], v224, s[36:39], s87 offen sc1\n"
      "buffer_load_dwordx4 v[132:135], v224, s[36:39], s87 offen offset:1024 sc1\n"
      "s_waitcnt vmcnt(0)\n"
      "v_cmp_ne_u64 vcc, s[54:55], v[128:129]\n"
      "v_cmp_ne_u64 s[90:91], s[54:55], v[130:131]\n"
      "s_or_b64 vcc, vcc, s[90:91]\n"
      "v_cmp_ne_u64 s[90:91], s[54:55], v[132:133]\n"
      "s_or_b64 vcc, vcc, s[90:91]\n"
      "v_cmp_ne_u64 s[90:91], s[54:55], v[134:135]\n"
      "s_or_b64 vcc, vcc, s[90:91]\n"
      "s_cbranch_vccz .Lfa7\n"
      "s_sub_u32 s77, s77, 1\n"
      "s_cmp_gt_i32 s77, 0\n"
      "s_cbranch_scc1 .Lfa6\n"
      ".Lfa7:\n"
      "s_waitcnt lgkmcnt(0)\n"
      "s_barrier\n"
      "s_add_u32 m0, s75, 0xe000\n"
      "s_add_u32 s35, s76, 0\n"
      "buffer_load_dwordx4 v224, s[40:43], s35 offen sc1 lds\n"
      "buffer_load_dwordx4 v224, s[40:43], s35 offen offset:1024 sc1 lds\n"
      "buffer_load_dwordx4 v224, s[40:43], s35 offen offset:2048 sc1 lds\n"
      "buffer_load_dwordx4 v224, s[40:43], s35 offen offset:3072 sc1 lds\n"
      "s_add_u32 m0, s75, 0xf000\n"
      "s_add_u32 s35, s76, 0x1000\n"
      "buffer_load_dwordx4 v224, s[40:43], s35 offen sc1 lds\n"
      "buffer_load_dwordx4 v224, s[40:43], s35 offen offset:1024 sc1 lds\n"
      "v_dot2c_f32_bf16 v200, v210, v20\n"
      "v_dot2c_f32_bf16 v201, v20, v20\n"
      "v_dot2c_f32_bf16 v202, v210, v21\n"
      "v_dot2c_f32_bf16 v203, v21, v21\n"
      "v_dot2c_f32_bf16 v200, v210, v22\n"
      "v_dot2c_f32_bf16 v201, v22, v22\n"
      "v_dot2c_f32_bf16 v202, v210, v23\n"
      "v_dot2c_f32_bf16 v203, v23, v23\n"
      "buffer_load_dwordx4 v224, s[40:43], s35 offen offset:2048 sc1 lds\n"
      "v_dot2c_f32_bf16 v200, v210, v24\n"
      "v_dot2c_f32_bf16 v201, v24, v24\n"
      "v_dot2c_f32_bf16 v202, v210, v25\n"
      "v_dot2c_f32_bf16 v203, v25, v25\n"
      "v_dot2c_f32_bf16 v200, v210, v26\n"
      "v_dot2c_f32_bf16 v201, v26, v26\n"
      "v_dot2c_f32_bf16 v202, v210, v27\n"
      "v_dot2c_f32_bf16 v203, v27, v27\n"
      "s_add_u32 m0, s75, 0x15000\n"
      "s_add_u32 s35, s76, 0x7000\n"
      "buffer_load_dwordx4 v224, s[40:43], s35 offen sc1 lds\n"
      "v_dot2c_f32_bf16 v200, v210, v28\n"
      "v_dot2c_f32_bf16 v201, v28, v28\n"
      "v_dot2c_f32_bf16 v202, v210, v29\n"
      "v_dot2c_f32_bf16 v203, v29, v29\n"
      "v_dot2c_f32_bf16 v200, v210, v30\n"
      "v_dot2c_f32_bf16 v201, v30, v30\n"
      "v_dot2c_f32_bf16 v202, v210, v31\n"
      "v_dot2c_f32_bf16 v203, v31, v31\n"
      "buffer_load_dwordx4 v224, s[40:43], s35 offen offset:1024 sc1 lds\n"
      "v_dot2c_f32_bf16 v200, v210, v32\n"
      "v_dot2c_f32_bf16 v201, v32, v32\n"
      "v_dot2c_f32_bf16 v202, v210, v33\n"
      "v_dot2c_f32_bf16 v203, v33, v33\n"
      "v_dot2c_f32_bf16 v200, v210, v34\n"
      "v_dot2c_f32_bf16 v201, v34, v34\n"
      "v_dot2c_f32_bf16 v202, v210, v35\n"
      "v_dot2c_f32_bf16 v203, v35, v35\n"
      "buffer_load_dwordx4 v224, s[40:43], s35 offen offset:2048 sc1 lds\n"
      "v_dot2c_f32_bf16 v200, v210, v36\n"
      "v_dot2c_f32_bf16 v201, v36, v36\n"
      "v_dot2c_f32_bf16 v202, v210, v37\n"
      "v_dot2c_f32_bf16 v203, v37, v37\n"
      "v_dot2c_f32_bf16 v200, v210, v38\n"
      "v_dot2c_f32_bf16 v201, v38, v38\n"
      "v_dot2c_f32_bf16 v202, v210, v39\n"
      "v_dot2c_f32_bf16 v203, v39, v39\n"
      "buffer_load_dwordx4 v224, s[40:43], s35 offen offset:3072 sc1 lds\n"
      "v_dot2c_f32_bf16 v200, v210, v40\n"
      "v_dot2c_f32_bf16 v201, v40, v40\n"
      "v_dot2c_f32_bf16 v202, v210, v41\n"
      "v_dot2c_f32_bf16 v203, v41, v41\n"
      "v_dot2c_f32_bf16 v200, v210, v42\n"
      "v_dot2c_f32_bf16 v201, v42, v42\n"
      "v_dot2c_f32_bf16 v202, v210, v43\n"
      "v_dot2c_f32_bf16 v203, v43, v43\n"
      "s_add_u32 m0, s75, 0x16000\n"
      "s_add_u32 s35, s76, 0x8000\n"
      "buffer_load_dwordx4 v224, s[40:43], s35 offen sc1 lds\n"
      "v_dot2c_f32_bf16 v200, v210, v44\n"
      "v_dot2c_f32_bf16 v201, v44, v44\n"
      "v_dot2c_f32_bf16 v202, v210, v45\n"
      "v_dot2c_f32_bf16 v203, v45, v45\n"
      "v_dot2c_f32_bf16 v200, v210, v46\n"
      "v_dot2c_f32_bf16 v201, v46, v46\n"
      "v_dot2c_f32_bf16 v202, v210, v47\n"
      "v_dot2c_f32_bf16 v203, v47, v47\n"
      "buffer_load_dwordx4 v224, s[40:43], s35 offen offset:1024 sc1 lds\n"
      "v_dot2c_f32_bf16 v200, v210, v48\n"
      "v_dot2c_f32_bf16 v201, v48, v48\n"
      "v_dot2c_f32_bf16 v202, v210, v49\n"
      "v_dot2c_f32_bf16 v203, v49, v49\n"
      "v_dot2c_f32_bf16 v200, v210, v50\n"
      "v_dot2c_f32_bf16 v201, v50, v50\n"
      "v_dot2c_f32_bf16 v202, v210, v51\n"
      "v_dot2c_f32_bf16 v203, v51, v51\n"
      "buffer_load_dwordx4 v224, s[40:43], s35 offen offset:2048 sc1 lds\n"
      "v_dot2c_f32_bf16 v200, v210, v52\n"
      "v_dot2c_f32_bf16 v201, v52, v52\n"
      "v_dot2c_f32_bf16 v202, v210, v53\n"
      "v_dot2c_f32_bf16 v203, v53, v53\n"
      "v_dot2c_f32_bf16 v200, v210, v54\n"
      "v_dot2c_f32_bf16 v201, v54, v54\n"
      "v_dot2c_f32_bf16 v202, v210, v55\n"
      "v_dot2c_f32_bf16 v203, v55, v55\n"
      "buffer_load_dword v160, v204, s[36:39], s53 offen sc1\n"
      "v_dot2c_f32_bf16 v200, v210, v56\n"
      "v_dot2c_f32_bf16 v201, v56, v56\n"
      "v_dot2c_f32_bf16 v202, v210, v57\n"
      "v_dot2c_f32_bf16 v203, v57, v57\n"
      "v_dot2c_f32_bf16 v200, v210, v58\n"
      "v_dot2c_f32_bf16 v201, v58, v58\n"
      "v_dot2c_f32_bf16 v202, v210, v59\n"
      "v_dot2c_f32_bf16 v203, v59, v59\n"
      "buffer_load_dword v161, v204, s[36:39], s53 offen offset:128 sc1\n"
      "v_dot2c_f32_bf16 v200, v210, v60\n"
      "v_dot2c_f32_bf16 v201, v60, v60\n"
      "v_dot2c_f32_bf16 v202, v210, v61\n"
      "v_dot2c_f32_bf16 v203, v61, v61\n"
      "v_dot2c_f32_bf16 v200, v210, v62\n"
      "v_dot2c_f32_bf16 v201, v62, v62\n"
      "v_dot2c_f32_bf16 v202, v210, v63\n"
      "v_dot2c_f32_bf16 v203, v63, v63\n"
      "buffer_load_dword v162, v204, s[36:39], s53 offen offset:256 sc1\n"
      "v_dot2c_f32_bf16 v200, v210, v64\n"
      "v_dot2c_f32_bf16 v201, v64, v64\n"
      "v_dot2c_f32_bf16 v202, v210, v65\n"
      "v_dot2c_f32_bf16 v203, v65, v65\n"
      "v_dot2c_f32_bf16 v200, v210, v66\n"
      "v_dot2c_f32_bf16 v201, v66, v66\n"
      "v_dot2c_f32_bf16 v202, v210, v67\n"
      "v_dot2c_f32_bf16 v203, v67, v67\n"
      "buffer_load_dword v163, v204, s[36:39], s53 offen offset:384 sc1\n"
      "v_dot2c_f32_bf16 v200, v210, v68\n"
      "v_dot2c_f32_bf16 v201, v68, v68\n"
      "v_dot2c_f32_bf16 v202, v210, v69\n"
      "v_dot2c_f32_bf16 v203, v69, v69\n"
      "v_dot2c_f32_bf16 v200, v210, v70\n"
      "v_dot2c_f32_bf16 v201, v70, v70\n"
      "v_dot2c_f32_bf16 v202, v210, v71\n"
      "v_dot2c_f32_bf16 v203, v71, v71\n"
      "buffer_load_dword v164, v204, s[36:39], s53 offen offset:512 sc1\n"
      "v_dot2c_f32_bf16 v200, v210, v72\n"
      "v_dot2c_f32_bf16 v201, v72, v72\n"
      "v_dot2c_f32_bf16 v202, v210, v73\n"
      "v_dot2c_f32_bf16 v203, v73, v73\n"
      "v_dot2c_f32_bf16 v200, v210, v74\n"
      "v_dot2c_f32_bf16 v201, v74, v74\n"
      "v_dot2c_f32_bf16 v202, v210, v75\n"
      "v_dot2c_f32_bf16 v203, v75, v75\n"
      "buffer_load_dword v165, v204, s[36:39], s53 offen offset:640 sc1\n"
      "v_dot2c_f32_bf16 v200, v210, v76\n"
      "v_dot2c_f32_bf16 v201, v76, v76\n"
      "v_dot2c_f32_bf16 v202, v210, v77\n"
      "v_dot2c_f32_bf16 v203, v77, v77\n"
      "v_dot2c_f32_bf16 v200, v210, v78\n"
      "v_dot2c_f32_bf16 v201, v78, v78\n"
      "v_dot2c_f32_bf16 v202, v210, v79\n"
      "v_dot2c_f32_bf16 v203, v79, v79\n"
      "buffer_load_dword v166, v204, s[36:39], s53 offen offset:768 sc1\n"
      "v_dot2c_f32_bf16 v200, v210, v80\n"
      "v_dot2c_f32_bf16 v201, v80, v80\n"
      "v_dot2c_f32_bf16 v202, v210, v81\n"
      "v_dot2c_f32_bf16 v203, v81, v81\n"
      "v_dot2c_f32_bf16 v200, v210, v82\n"
      "v_dot2c_f32_bf16 v201, v82, v82\n"
      "v_dot2c_f32_bf16 v202, v210, v83\n"
      "v_dot2c_f32_bf16 v203, v83, v83\n"
      "buffer_load_dword v167, v204, s[36:39], s53 offen offset:896 sc1\n"
      "v_dot2c_f32_bf16 v200, v210, v84\n"
      "v_dot2c_f32_bf16 v201, v84, v84\n"
      "v_dot2c_f32_bf16 v202, v210, v85\n"
      "v_dot2c_f32_bf16 v203, v85, v85\n"
      "v_dot2c_f32_bf16 v200, v210, v86\n"
      "v_dot2c_f32_bf16 v201, v86, v86\n"
      "v_dot2c_f32_bf16 v202, v210, v87\n"
      "v_dot2c_f32_bf16 v203, v87, v87\n"
      "buffer_load_dword v168, v204, s[36:39], s53 offen offset:1024 sc1\n"
      "v_dot2c_f32_bf16 v200, v210, v88\n"
      "v_dot2c_f32_bf16 v201, v88, v88\n"
      "v_dot2c_f32_bf16 v202, v210, v89\n"
      "v_dot2c_f32_bf16 v203, v89, v89\n"
      "v_dot2c_f32_bf16 v200, v210, v90\n"
      "v_dot2c_f32_bf16 v201, v90, v90\n"
      "v_dot2c_f32_bf16 v202, v210, v91\n"
      "v_dot2c_f32_bf16 v203, v91, v91\n"
      "buffer_load_dword v169, v204, s[36:39], s53 offen offset:1152 sc1\n"
      "v_dot2c_f32_bf16 v200, v210, v92\n"
      "v_dot2c_f32_bf16 v201, v92, v92\n"
      "v_dot2c_f32_bf16 v202, v210, v93\n"
      "v_dot2c_f32_bf16 v203, v93, v93\n"
      "v_dot2c_f32_bf16 v200, v210, v94\n"
      "v_dot2c_f32_bf16 v201, v94, v94\n"
      "v_dot2c_f32_bf16 v202, v210, v95\n"
      "v_dot2c_f32_bf16 v203, v95, v95\n"
      "buffer_load_dword v170, v204, s[36:39], s53 offen offset:1280 sc1\n"
      "v_dot2c_f32_bf16 v200, v210, v96\n"
      "v_dot2c_f32_bf16 v201, v96, v96\n"
      "v_dot2c_f32_bf16 v202, v210, v97\n"
      "v_dot2c_f32_bf16 v203, v97, v97\n"
      "v_dot2c_f32_bf16 v200, v210, v98\n"
      "v_dot2c_f32_bf16 v201, v98, v98\n"
      "v_dot2c_f32_bf16 v202, v210, v99\n"
      "v_dot2c_f32_bf16 v203, v99, v99\n"
      "buffer_load_dword v171, v204, s[36:39], s53 offen offset:1408 sc1\n"
      "v_dot2c_f32_bf16 v200, v210, v100\n"
      "v_dot2c_f32_bf16 v201, v100, v100\n"
      "v_dot2c_f32_bf16 v202, v210, v101\n"
      "v_dot2c_f32_bf16 v203, v101, v101\n"
      "v_dot2c_f32_bf16 v200, v210, v102\n"
      "v_dot2c_f32_bf16 v201, v102, v102\n"
      "v_dot2c_f32_bf16 v202, v210, v103\n"
      "v_dot2c_f32_bf16 v203, v103, v103\n"
      "buffer_load_dword v172, v204, s[36:39], s53 offen offset:1536 sc1\n"
      "v_dot2c_f32_bf16 v200, v210, v104\n"
      "v_dot2c_f32_bf16 v201, v104, v104\n"
      "v_dot2c_f32_bf16 v202, v210, v105\n"
      "v_dot2c_f32_bf16 v203, v105, v105\n"
      "v_dot2c_f32_bf16 v200, v210, v106\n"
      "v_dot2c_f32_bf16 v201, v106, v106\n"
      "v_dot2c_f32_bf16 v202, v210, v107\n"
      "v_dot2c_f32_bf16 v203, v107, v107\n"
      "buffer_load_dword v173, v204, s[36:39], s53 offen offset:1664 sc1\n"
      "v_dot2c_f32_bf16 v200, v210, v108\n"
      "v_dot2c_f32_bf16 v201, v108, v108\n"
      "v_dot2c_f32_bf16 v202, v210, v109\n"
      "v_dot2c_f32_bf16 v203, v109, v109\n"
      "v_dot2c_f32_bf16 v200, v210, v110\n"
      "v_dot2c_f32_bf16 v201, v110, v110\n"
      "v_dot2c_f32_bf16 v202, v210, v111\n"
      "v_dot2c_f32_bf16 v203, v111, v111\n"
      "s_nop 2\n"
      "v_pk_add_f32 v[200:201], v[200:201], v[202:203]\n"
      "v_mov_b32 v202, v200\n"
      "v_mov_b32 v203, v201\n"
      "s_nop 0\n"
      "v_permlane32_swap_b32 v200, v202\n"
      "v_permlane32_swap_b32 v201, v203\n"
      "v_pk_add_f32 v[200:201], v[200:201], v[202:203]\n"
      "v_mul_f32 v200, 0xbb124925, v200\n"
      "v_mul_f32 v201, 0x3b124925, v201\n"
      "v_fma_f32 v201, -v200, v200, v201\n"
      "v_max_f32 v201, 0, v201\n"
      "v_add_f32 v219, 0x3727c5ac, v201\n"
      "v_rsq_f32 v218, v219\n"
      "v_cvt_pk_bf16_f32 v220, v200, v200\n"
      "v_and_b32 v202, 0xffff0000, v220\n"
      "v_sub_f32 v202, v200, v202\n"
      "v_and_b32 v221, 0xffff, v220\n"
      "v_cvt_pk_bf16_f32 v220, v200, v202\n"
      "v_cndmask_b32 v220, v220, 0, s[56:57]\n"
      "v_cndmask_b32 v221, v221, 0, s[56:57]\n"
      "ds_write_b32 v233, v218\n"
      "s_waitcnt vmcnt(14)\n"
      "s_barrier\n"
      "ds_read_b128 a[224:227], v225\n"
      "ds_read_b128 a[228:231], v225 offset:28672\n"
      "ds_read_b128 a[232:235], v225 offset:1024\n"
      "ds_read_b128 a[236:239], v225 offset:29696\n"
      "ds_read_b128 a[240:243], v225 offset:2048\n"
      "ds_read_b128 a[244:247], v225 offset:30720\n"
      "s_waitcnt lgkmcnt(4)\n"
      "v_mfma_f32_32x32x16_bf16 v[128:143], v[0:3], a[224:227], 0\n"
      "v_mfma_f32_32x32x16_bf16 v[144:159], v[0:3], a[228:231], 0\n"
      "ds_read_b128 a[248:251], v225 offset:3072\n"
      "ds_read_b128 a[252:255], v225 offset:31744\n"
      "buffer_store_dword a96, v209, s[36:39], s27 offen sc1\n"
      "s_add_u32 s27, s27, s96\n"
      "s_waitcnt lgkmcnt(4)\n"
      "v_mfma_f32_32x32x16_bf16 v[128:143], v[4:7], a[232:235], v[128:143]\n"
      "s_add_u32 m0, s75, 0x1c000\n"
      "s_add_u32 s35, s76, 0xe000\n"
      "buffer_load_dwordx4 v224, s[40:43], s35 offen sc1 lds\n"
      "v_mfma_f32_32x32x16_bf16 v[144:159], v[4:7], a[236:239], v[144:159]\n"
      "ds_read_b128 a[224:227], v225 offset:4096\n"
      "ds_read_b128 a[228:231], v225 offset:32768\n"
      "s_waitcnt lgkmcnt(4)\n"
      "v_mfma_f32_32x32x16_bf16 v[128:143], v[8:11], a[240:243], v[128:143]\n"
      "buffer_load_dwordx4 v224, s[40:43], s35 offen offset:1024 sc1 lds\n"
      "v_mfma_f32_32x32x16_bf16 v[144:159], v[8:11], a[244:247], v[144:159]\n"
      "ds_read_b128 a[232:235], v225 offset:5120\n"
      "ds_read_b128 a[236:239], v225 offset:33792\n"
      "buffer_store_dword a97, v209, s[36:39], s27 offen sc1\n"
      "s_add_u32 s27, s27, s96\n"
      "s_waitcnt lgkmcnt(4)\n"
      "v_mfma_f32_32x32x16_bf16 v[128:143], v[12:15], a[248:251], v[128:143]\n"
      "v_mfma_f32_32x32x16_bf16 v[144:159], v[12:15], a[252:255], v[144:159]\n"
      "ds_read_b128 a[240:243], v225 offset:6144\n"
      "ds_read_b128 a[244:247], v225 offset:34816\n"
      "buffer_load_dwordx4 v224, s[40:43], s35 offen offset:2048 sc1 lds\n"
      "s_waitcnt lgkmcnt(4)\n"
      "v_mfma_f32_32x32x16_bf16 v[128:143], v[16:19], a[224:227], v[128:143]\n"
      "v_mfma_f32_32x32x16_bf16 v[144:159], v[16:19], a[228:231], v[144:159]\n"
      "ds_read_b128 a[248:251], v225 offset:7168\n"
      "ds_read_b128 a[252:255], v225 offset:35840\n"
      "buffer_store_dword a98, v209, s[36:39], s27 offen sc1\n"
      "s_add_u32 s27, s27, s96\n"
      "buffer_load_dwordx4 v224, s[40:43], s35 offen offset:3072 sc1 lds\n"
      "s_waitcnt lgkmcnt(4)\n"
      "v_mfma_f32_32x32x16_bf16 v[128:143], v[20:23], a[232:235], v[128:143]\n"
      "v_mfma_f32_32x32x16_bf16 v[144:159], v[20:23], a[236:239], v[144:159]\n"
      "ds_read_b128 a[224:227], v225 offset:8192\n"
      "ds_read_b128 a[228:231], v225 offset:36864\n"
      "s_waitcnt lgkmcnt(4)\n"
      "v_mfma_f32_32x32x16_bf16 v[128:143], v[24:27], a[240:243], v[128:143]\n"
      "s_add_u32 m0, s75, 0x1d000\n"
      "s_add_u32 s35, s76, 0xf000\n"
      "buffer_load_dwordx4 v224, s[40:43], s35 offen sc1 lds\n"
      "v_mfma_f32_32x32x16_bf16 v[144:159], v[24:27], a[244:247], v[144:159]\n"
      "ds_read_b128 a[232:235], v225 offset:9216\n"
      "ds_read_b128 a[236:239], v225 offset:37888\n"
      "buffer_store_dword a99, v209, s[36:39], s27 offen sc1\n"
      "s_add_u32 s27, s27, s96\n"
      "s_waitcnt lgkmcnt(4)\n"
      "v_mfma_f32_32x32x16_bf16 v[128:143], v[28:31], a[248:251], v[128:143]\n"
      "buffer_load_dwordx4 v224, s[40:43], s35 offen offset:1024 sc1 lds\n"
      "v_mfma_f32_32x32x16_bf16 v[144:159], v[28:31], a[252:255], v[144:159]\n"
      "ds_read_b128 a[240:243], v225 offset:10240\n"
      "ds_read_b128 a[244:247], v225 offset:38912\n"
      "s_waitcnt lgkmcnt(4)\n"
      "v_mfma_f32_32x32x16_bf16 v[128:143], v[32:35], a[224:227], v[128:143]\n"
      "v_mfma_f32_32x32x16_bf16 v[144:159], v[32:35], a[228:231], v[144:159]\n"
      "ds_read_b128 a[248:251], v225 offset:11264\n"
      "ds_read_b128 a[252:255], v225 offset:39936\n"
      "buffer_store_dwordx4 a[64:67], v224, s[28:31], s24 offen sc1\n"
      "s_add_u32 s24, s24, s26\n"
      "buffer_load_dwordx4 v224, s[40:43], s35 offen offset:2048 sc1 lds\n"
      "s_waitcnt lgkmcnt(4)\n"
      "v_mfma_f32_32x32x16_bf16 v[128:143], v[36:39], a[232:235], v[128:143]\n"
      "v_mfma_f32_32x32x16_bf16 v[144:159], v[36:39], a[236:239], v[144:159]\n"
      "ds_read_b128 a[224:227], v225 offset:12288\n"
      "ds_read_b128 a[228:231], v225 offset:40960\n"
      "s_add_u32 m0, s75, 0\n"
      "s_add_u32 s35, s71, 0\n"
      "buffer_load_dwordx4 v224, s[36:39], s35 offen sc1 lds\n"
      "s_waitcnt lgkmcnt(4)\n"
      "v_mfma_f32_32x32x16_bf16 v[128:143], v[40:43], a[240:243], v[128:143]\n"
      "s_waitcnt vmcnt(25)\n"
      "v_add_f32 v160, v160, v161\n"
      "v_mfma_f32_32x32x16_bf16 v[144:159], v[40:43], a[244:247], v[144:159]\n"
      "ds_read_b128 a[232:235], v225 offset:13312\n"
      "ds_read_b128 a[236:239], v225 offset:41984\n"
      "buffer_store_dwordx4 a[68:71], v224, s[28:31], s25 offen sc1\n"
      "s_add_u32 s25, s25, s26\n"
      "s_waitcnt lgkmcnt(4)\n"
      "v_mfma_f32_32x32x16_bf16 v[128:143], v[44:47], a[248:251], v[128:143]\n"
      "buffer_load_dwordx4 v224, s[36:39], s35 offen offset:1024 sc1 lds\n"
      "s_waitcnt vmcnt(26)\n"
      "v_add_f32 v160, v160, v162\n"
      "v_mfma_f32_32x32x16_bf16 v[144:159], v[44:47], a[252:255], v[144:159]\n"
      "ds_read_b128 a[240:243], v225 offset:14336\n"
      "ds_read_b128 a[244:247], v225 offset:43008\n"
      "s_waitcnt vmcnt(25)\n"
      "v_add_f32 v160, v160, v163\n"
      "s_waitcnt lgkmcnt(4)\n"
      "v_mfma_f32_32x32x16_bf16 v[128:143], v[48:51], a[224:227], v[128:143]\n"
      "buffer_load_dwordx4 v224, s[36:39], s35 offen offset:2048 sc1 lds\n"
      "s_waitcnt vmcnt(25)\n"
      "v_add_f32 v160, v160, v164\n"
      "v_mfma_f32_32x32x16_bf16 v[144:159], v[48:51], a[228:231], v[144:159]\n"
      "ds_read_b128 a[248:251], v225 offset:15360\n"
      "ds_read_b128 a[252:255], v225 offset:44032\n"
      "buffer_store_dwordx4 a[72:75], v224, s[28:31], s24 offen sc1\n"
      "s_add_u32 s24, s24, s26\n"
      "s_waitcnt lgkmcnt(4)\n"
      "v_mfma_f32_32x32x16_bf16 v[128:143], v[52:55], a[232:235], v[128:143]\n"
      "s_waitcnt vmcnt(25)\n"
      "v_add_f32 v160, v160, v165\n"
      "v_mfma_f32_32x32x16_bf16 v[144:159], v[52:55], a[236:239], v[144:159]\n"
      "ds_read_b128 a[224:227], v225 offset:16384\n"
      "ds_read_b128 a[228:231], v225 offset:45056\n"
      "buffer_load_dwordx4 v224, s[36:39], s35 offen offset:3072 sc1 lds\n"
      "s_waitcnt vmcnt(25)\n"
      "v_add_f32 v160, v160, v166\n"
      "s_waitcnt lgkmcnt(4)\n"
      "v_mfma_f32_32x32x16_bf16 v[128:143], v[56:59], a[240:243], v[128:143]\n"
      "v_mfma_f32_32x32x16_bf16 v[144:159], v[56:59], a[244:247], v[144:159]\n"
      "ds_read_b128 a[232:235], v225 offset:17408\n"
      "ds_read_b128 a[236:239], v225 offset:46080\n"
      "buffer_store_dwordx4 a[76:79], v224, s[28:31], s25 offen sc1\n"
      "s_add_u32 s25, s25, s26\n"
      "s_add_u32 m0, s75, 0x1000\n"
      "s_add_u32 s35, s71, 0x1000\n"
      "buffer_load_dwordx4 v224, s[36:39], s35 offen sc1 lds\n"
      "s_waitcnt vmcnt(26)\n"
      "v_add_f32 v160, v160, v167\n"
      "s_waitcnt lgkmcnt(4)\n"
      "v_mfma_f32_32x32x16_bf16 v[128:143], v[60:63], a[248:251], v[128:143]\n"
      "s_waitcnt vmcnt(25)\n"
      "v_add_f32 v160, v160, v168\n"
      "v_mfma_f32_32x32x16_bf16 v[144:159], v[60:63], a[252:255], v[144:159]\n"
      "ds_read_b128 a[240:243], v225 offset:18432\n"
      "ds_read_b128 a[244:247], v225 offset:47104\n"
      "s_waitcnt lgkmcnt(4)\n"
      "v_mfma_f32_32x32x16_bf16 v[128:143], v[64:67], a[224:227], v[128:143]\n"
      "buffer_load_dwordx4 v224, s[36:39], s35 offen offset:1024 sc1 lds\n"
      "s_waitcnt vmcnt(25)\n"
      "v_add_f32 v160, v160, v169\n"
      "v_mfma_f32_32x32x16_bf16 v[144:159], v[64:67], a[228:231], v[144:159]\n"
      "ds_read_b128 a[248:251], v225 offset:19456\n"
      "ds_read_b128 a[252:255], v225 offset:48128\n"
      "buffer_store_dwordx4 a[80:83], v224, s[28:31], s24 offen sc1\n"
      "s_add_u32 s24, s24, s26\n"
      "s_waitcnt vmcnt(25)\n"
      "v_add_f32 v160, v160, v170\n"
      "s_waitcnt lgkmcnt(4)\n"
      "v_mfma_f32_32x32x16_bf16 v[128:143], v[68:71], a[232:235], v[128:143]\n"
      "buffer_load_dwordx4 v224, s[36:39], s35 offen offset:2048 sc1 lds\n"
      "s_waitcnt vmcnt(25)\n"
      "v_add_f32 v160, v160, v171\n"
      "v_mfma_f32_32x32x16_bf16 v[144:159], v[68:71], a[236:239], v[144:159]\n"
      "ds_read_b128 a[224:227], v225 offset:20480\n"
      "ds_read_b128 a[228:231], v225 offset:49152\n"
      "s_waitcnt lgkmcnt(4)\n"
      "v_mfma_f32_32x32x16_bf16 v[128:143], v[72:75], a[240:243], v[128:143]\n"
      "s_waitcnt vmcnt(24)\n"
      "v_add_f32 v160, v160, v172\n"
      "v_mfma_f32_32x32x16_bf16 v[144:159], v[72:75], a[244:247], v[144:159]\n"
      "ds_read_b128 a[232:235], v225 offset:21504\n"
      "ds_read_b128 a[236:239], v225 offset:50176\n"
      "buffer_store_dwordx4 a[84:87], v224, s[28:31], s25 offen sc1\n"
      "s_add_u32 s25, s25, s26\n"
      "s_waitcnt vmcnt(24)\n"
      "v_add_f32 v160, v160, v173\n"
      "s_waitcnt lgkmcnt(4)\n"
      "v_mfma_f32_32x32x16_bf16 v[128:143], v[76:79], a[248:251], v[128:143]\n"
      "v_mfma_f32_32x32x16_bf16 v[144:159], v[76:79], a[252:255], v[144:159]\n"
      "ds_read_b128 a[240:243], v225 offset:22528\n"
      "ds_read_b128 a[244:247], v225 offset:51200\n"
      "v_mov_b32 v161, v160\n"
      "s_waitcnt lgkmcnt(4)\n"
      "v_mfma_f32_32x32x16_bf16 v[128:143], v[80:83], a[224:227], v[128:143]\n"
      "v_permlane32_swap_b32 v160, v161\n"
      "v_mfma_f32_32x32x16_bf16 v[144:159], v[80:83], a[228:231], v[144:159]\n"
      "ds_read_b128 a[248:251], v225 offset:23552\n"
      "ds_read_b128 a[252:255], v225 offset:52224\n"
      "s_waitcnt lgkmcnt(4)\n"
      "v_mfma_f32_32x32x16_bf16 v[128:143], v[84:87], a[232:235], v[128:143]\n"
      "v_add_f32 v160, v160, v161\n"
      "v_mfma_f32_32x32x16_bf16 v[144:159], v[84:87], a[236:239], v[144:159]\n"
      "ds_read_b128 a[224:227], v225 offset:24576\n"
      "ds_read_b128 a[228:231], v225 offset:53248\n"
      "v_cvt_pk_bf16_f32 v162, v160, v160\n"
      "s_waitcnt lgkmcnt(4)\n"
      "v_mfma_f32_32x32x16_bf16 v[128:143], v[88:91], a[240:243], v[128:143]\n"
      "v_and_b32 v161, 0xffff0000, v162\n"
      "v_mfma_f32_32x32x16_bf16 v[144:159], v[88:91], a[244:247], v[144:159]\n"
      "ds_read_b128 a[232:235], v225 offset:25600\n"
      "ds_read_b128 a[236:239], v225 offset:54272\n"
      "s_waitcnt lgkmcnt(4)\n"
      "v_mfma_f32_32x32x16_bf16 v[128:143], v[92:95], a[248:251], v[128:143]\n"
      "v_sub_f32 v161, v160, v161\n"
      "v_mfma_f32_32x32x16_bf16 v[144:159], v[92:95], a[252:255], v[144:159]\n"
      "ds_read_b128 a[240:243], v225 offset:26624\n"
      "ds_read_b128 a[244:247], v225 offset:55296\n"
      "v_cvt_pk_bf16_f32 v163, v161, 0\n"
      "s_waitcnt lgkmcnt(4)\n"
      "v_mfma_f32_32x32x16_bf16 v[128:143], v[96:99], a[224:227], v[128:143]\n"
      "v_mfma_f32_32x32x16_bf16 v[144:159], v[96:99], a[228:231], v[144:159]\n"
      "ds_read_b128 a[248:251], v225 offset:27648\n"
      "ds_read_b128 a[252:255], v225 offset:56320\n"
      "ds_write_b64 v206, v[162:163]\n"
      "s_waitcnt lgkmcnt(5)\n"
      "v_mfma_f32_32x32x16_bf16 v[128:143], v[100:103], a[232:235], v[128:143]\n"
      "v_mfma_f32_32x32x16_bf16 v[144:159], v[100:103], a[236:239], v[144:159]\n"
      "s_waitcnt lgkmcnt(3)\n"
      "v_mfma_f32_32x32x16_bf16 v[128:143], v[104:107], a[240:243], v[128:143]\n"
      "v_mfma_f32_32x32x16_bf16 v[144:159], v[104:107], a[244:247], v[144:159]\n"
      "s_waitcnt lgkmcnt(1)\n"
      "v_mfma_f32_32x32x16_bf16 v[128:143], v[108:111], a[248:251], v[128:143]\n"
      "v_mfma_f32_32x32x16_bf16 v[144:159], v[108:111], a[252:255], v[144:159]\n"
      "s_waitcnt vmcnt(12)\n"
      "s_waitcnt lgkmcnt(0)\n"
      "s_barrier\n"
      "ds_read_b64 v[212:213], v235 offset:2048\n"
      "v_accvgpr_write_b32 a226, 0\n"
      "v_accvgpr_write_b32 a227, 0\n"
      "ds_read_b64 a[224:225], v235 offset:2304\n"
      "ds_read_b128 v[184:187], v234\n"
      "ds_read_b128 v[188:191], v234 offset:32\n"
      "ds_read_b128 v[192:195], v234 offset:64\n"
      "ds_read_b128 v[196:199], v234 offset:96\n"
      "s_nop 1\n"
      "s_branch .Lg1join\n"
      ".Lloop:\n"
      "ds_read_b128 a[224:227], v225\n"
      "ds_read_b128 a[228:231], v225 offset:28672\n"
      "ds_read_b128 a[232:235], v225 offset:1024\n"
      "ds_read_b128 a[236:239], v225 offset:29696\n"
      "ds_read_b128 a[240:243], v225 offset:2048\n"
      "ds_read_b128 a[244:247], v225 offset:30720\n"
      "ds_read_b128 a[248:251], v225 offset:3072\n"
      "ds_read_b128 a[252:255], v225 offset:31744\n"
      "; G1\n"
      "s_add_u32 s78, s62, 0x1c0000\n"
      "s_add_u32 s79, s62, 0x1c1000\n"
      "s_add_u32 s80, s62, 0x1c2000\n"
      "s_add_u32 s81, s62, 0x1c3000\n"
      "s_add_u32 s82, s62, 0x1c4000\n"
      "s_add_u32 s83, s62, 0x1c5000\n"
      "s_add_u32 s84, s62, 0x1c6000\n"
      "s_mov_b32 s85, 0x3e0000\n"
      "v_and_b32 v216, 15, v238\n"
      "v_lshrrev_b32 v217, 1, v216\n"
      "v_mul_u32_u24 v217, 7, v217\n"
      "v_and_b32 v216, 1, v216\n"
      "v_lshl_add_u32 v216, v216, 2, v217\n"
      "v_add_u32 v216, 2, v216\n"
      "v_lshlrev_b32 v216, 9, v216\n"
      ".Ldl16:\n"
      "s_sleep 1\n"
      "s_memtime s[90:91]\n"
      "s_waitcnt lgkmcnt(0)\n"
      "s_sub_u32 s90, s90, s96\n"
      "s_cmp_lt_u32 s90, 0x1f4\n"
      "s_cbranch_scc1 .Ldl16\n"
      ".Lhd13:\n"
      "buffer_load_dword v217, v216, s[36:39], s78 offen offset:496 sc1\n"
      "buffer_load_dwordx4 v[0:3], v224, s[36:39], s78 offen sc1\n"
      "buffer_load_dwordx4 v[4:7], v224, s[36:39], s78 offen offset:1024 sc1\n"
      "buffer_load_dwordx4 v[8:11], v224, s[36:39], s78 offen offset:2048 sc1\n"
      "buffer_load_dwordx4 v[12:15], v224, s[36:39], s78 offen offset:3072 sc1\n"
      "buffer_load_dwordx4 v[16:19], v224, s[36:39], s79 offen sc1\n"
      "buffer_load_dwordx4 v[20:23], v224, s[36:39], s79 offen offset:1024 sc1\n"
      "buffer_load_dwordx4 v[24:27], v224, s[36:39], s79 offen offset:2048 sc1\n"
      "buffer_load_dwordx4 v[28:31], v224, s[36:39], s79 offen offset:3072 sc1\n"
      "s_waitcnt vmcnt(8)\n"
      "v_bitop3_b32 v217, v217, v217, 1 bitop3:0x2a\n"
      "v_cmp_ne_u32 vcc, 0, v217\n"
      "s_cbranch_vccz .Lco15\n"
      "s_sub_u32 s77, s77, 1\n"
      "s_cmp_gt_i32 s77, 0\n"
      "s_cbranch_scc1 .Lhd13\n"
      ".Lco15:\n"
      "s_waitcnt vmcnt(6)\n"
      "v_bitop3_b32 v217, v0, v1, v2 bitop3:0x80\n"
      "v_bitop3_b32 v217, v217, v3, v4 bitop3:0x80\n"
      "v_bitop3_b32 v217, v217, v5, v6 bitop3:0x80\n"
      "v_bitop3_b32 v217, v217, v7, 1 bitop3:0x2a\n"
      "v_cmp_ne_u32 vcc, 0, v217\n"
      "s_cbranch_vccnz .Lst21\n"
      ".Lco22:\n"
      "v_mfma_f32_32x32x16_bf16 v[128:143], v[0:3], a[224:227], 0\n"
      "ds_read_b128 v[160:163], v225 offset:4096\n"
      "buffer_load_dwordx4 v[32:35], v224, s[36:39], s80 offen sc1\n"
      "s_waitcnt vmcnt(5)\n"
      "v_bitop3_b32 v217, v8, v9, v10 bitop3:0x80\n"
      "v_bitop3_b32 v217, v217, v11, v12 bitop3:0x80\n"
      "v_mfma_f32_32x32x16_bf16 v[144:159], v[0:3], a[228:231], 0\n"
      "ds_read_b128 v[164:167], v225 offset:32768\n"
      "buffer_load_dwordx4 v[36:39], v224, s[36:39], s80 offen offset:1024 sc1\n"
      "v_bitop3_b32 v217, v217, v13, v14 bitop3:0x80\n"
      "v_bitop3_b32 v217, v217, v15, 1 bitop3:0x2a\n"
      "v_cmp_ne_u32 vcc, 0, v217\n"
      "v_mfma_f32_32x32x16_bf16 v[128:143], v[4:7], a[232:235], v[128:143]\n"
      "ds_read_b128 v[168:171], v225 offset:5120\n"
      "buffer_load_dwordx4 v[40:43], v224, s[36:39], s80 offen offset:2048 sc1\n"
      "s_cbranch_vccnz .Lst25\n"
      ".Lco26:\n"
      "v_mfma_f32_32x32x16_bf16 v[144:159], v[4:7], a[236:239], v[144:159]\n"
      "ds_read_b128 v[172:175], v225 offset:33792\n"
      "buffer_load_dwordx4 v[44:47], v224, s[36:39], s80 offen offset:3072 sc1\n"
      "v_mfma_f32_32x32x16_bf16 v[128:143], v[8:11], a[240:243], v[128:143]\n"
      "ds_read_b128 v[176:179], v225 offset:6144\n"
      "buffer_load_dwordx4 v[184:187], v232, s[36:39], s85 offen sc1\n"
      "s_waitcnt vmcnt(7)\n"
      "v_bitop3_b32 v217, v16, v17, v18 bitop3:0x80\n"
      "v_bitop3_b32 v217, v217, v19, v20 bitop3:0x80\n"
      "v_mfma_f32_32x32x16_bf16 v[144:159], v[8:11], a[244:247], v[144:159]\n"
      "ds_read_b128 v[180:183], v225 offset:34816\n"
      "buffer_load_dwordx4 v[188:191], v232, s[36:39], s85 offen offset:512 sc1\n"
      "v_bitop3_b32 v217, v217, v21, v22 bitop3:0x80\n"
      "v_bitop3_b32 v217, v217, v23, 1 bitop3:0x2a\n"
      "v_cmp_ne_u32 vcc, 0, v217\n"
      "v_mfma_f32_32x32x16_bf16 v[128:143], v[12:15], a[248:251], v[128:143]\n"
      "ds_read_b128 a[224:227], v225 offset:7168\n"
      "buffer_load_dwordx4 v[192:195], v232, s[36:39], s85 offen offset:1024 sc1\n"
      "s_cbranch_vccnz .Lst29\n"
      ".Lco30:\n"
      "v_mfma_f32_32x32x16_bf16 v[144:159], v[12:15], a[252:255], v[144:159]\n"
      "ds_read_b128 a[228:231], v225 offset:35840\n"
      "buffer_load_dwordx4 v[196:199], v232, s[36:39], s85 offen offset:1536 sc1\n"
      "s_waitcnt lgkmcnt(6)\n"
      "v_mfma_f32_32x32x16_bf16 v[128:143], v[16:19], v[160:163], v[128:143]\n"
      "ds_read_b128 a[232:235], v225 offset:8192\n"
      "buffer_load_dwordx4 v[48:51], v224, s[36:39], s81 offen sc1\n"
      "s_waitcnt vmcnt(9)\n"
      "v_bitop3_b32 v217, v24, v25, v26 bitop3:0x80\n"
      "v_bitop3_b32 v217, v217, v27, v28 bitop3:0x80\n"
      "v_mfma_f32_32x32x16_bf16 v[144:159], v[16:19], v[164:167], v[144:159]\n"
      "ds_read_b128 a[236:239], v225 offset:36864\n"
      "buffer_load_dwordx4 v[52:55], v224, s[36:39], s81 offen offset:1024 sc1\n"
      "v_bitop3_b32 v217, v217, v29, v30 bitop3:0x80\n"
      "v_bitop3_b32 v217, v217, v31, 1 bitop3:0x2a\n"
      "v_cmp_ne_u32 vcc, 0, v217\n"
      "s_waitcnt lgkmcnt(6)\n"
      "v_mfma_f32_32x32x16_bf16 v[128:143], v[20:23], v[168:171], v[128:143]\n"
      "ds_read_b128 a[240:243], v225 offset:9216\n"
      "buffer_load_dwordx4 v[56:59], v224, s[36:39], s81 offen offset:2048 sc1\n"
      "s_cbranch_vccnz .Lst33\n"
      ".Lco34:\n"
      "v_mfma_f32_32x32x16_bf16 v[144:159], v[20:23], v[172:175], v[144:159]\n"
      "ds_read_b128 a[244:247], v225 offset:37888\n"
      "buffer_load_dwordx4 v[60:63], v224, s[36:39], s81 offen offset:3072 sc1\n"
      "s_waitcnt lgkmcnt(6)\n"
      "v_mfma_f32_32x32x16_bf16 v[128:143], v[24:27], v[176:179], v[128:143]\n"
      "ds_read_b128 a[248:251], v225 offset:10240\n"
      "buffer_load_dwordx4 v[64:67], v224, s[36:39], s82 offen sc1\n"
      "s_waitcnt vmcnt(11)\n"
      "v_bitop3_b32 v217, v32, v33, v34 bitop3:0x80\n"
      "v_bitop3_b32 v217, v217, v35, v36 bitop3:0x80\n"
      "v_mfma_f32_32x32x16_bf16 v[144:159], v[24:27], v[180:183], v[144:159]\n"
      "ds_read_b128 a[252:255], v225 offset:38912\n"
      "buffer_load_dwordx4 v[68:71], v224, s[36:39], s82 offen offset:1024 sc1\n"
      "v_bitop3_b32 v217, v217, v37, v38 bitop3:0x80\n"
      "v_bitop3_b32 v217, v217, v39, 1 bitop3:0x2a\n"
      "v_cmp_ne_u32 vcc, 0, v217\n"
      "s_waitcnt lgkmcnt(6)\n"
      "v_mfma_f32_32x32x16_bf16 v[128:143], v[28:31], a[224:227], v[128:143]\n"
      "ds_read_b128 v[160:163], v225 offset:11264\n"
      "s_cbranch_vccnz .Lst37\n"
      ".Lco38:\n"
      "v_mfma_f32_32x32x16_bf16 v[144:159], v[28:31], a[228:231], v[144:159]\n"
      "ds_read_b128 v[164:167], v225 offset:39936\n"
      "buffer_load_dwordx4 v[72:75], v224, s[36:39], s82 offen offset:2048 sc1\n"
      "s_waitcnt lgkmcnt(6)\n"
      "v_mfma_f32_32x32x16_bf16 v[128:143], v[32:35], a[232:235], v[128:143]\n"
      "ds_read_b128 v[168:171], v225 offset:12288\n"
      "s_waitcnt vmcnt(11)\n"
      "v_bitop3_b32 v217, v40, v41, v42 bitop3:0x80\n"
      "v_bitop3_b32 v217, v217, v43, v44 bitop3:0x80\n"
      "v_mfma_f32_32x32x16_bf16 v[144:159], v[32:35], a[236:239], v[144:159]\n"
      "ds_read_b128 v[172:175], v225 offset:40960\n"
      "buffer_load_dwordx4 v[76:79], v224, s[36:39], s82 offen offset:3072 sc1\n"
      "v_bitop3_b32 v217, v217, v45, v46 bitop3:0x80\n"
      "v_bitop3_b32 v217, v217, v47, 1 bitop3:0x2a\n"
      "v_cmp_ne_u32 vcc, 0, v217\n"
      "s_waitcnt lgkmcnt(6)\n"
      "v_mfma_f32_32x32x16_bf16 v[128:143], v[36:39], a[240:243], v[128:143]\n"
      "ds_read_b128 v[176:179], v225 offset:13312\n"
      "s_cbranch_vccnz .Lst41\n"
      ".Lco42:\n"
      "v_mfma_f32_32x32x16_bf16 v[144:159], v[36:39], a[244:247], v[144:159]\n"
      "ds_read_b128 v[180:183], v225 offset:41984\n"
      "buffer_load_dwordx4 v[80:83], v224, s[36:39], s83 offen sc1\n"
      "s_waitcnt lgkmcnt(6)\n"
      "v_mfma_f32_32x32x16_bf16 v[128:143], v[40:43], a[248:251], v[128:143]\n"
      "ds_read_b128 a[224:227], v225 offset:14336\n"
      "s_waitcnt vmcnt(7)\n"
      "v_bitop3_b32 v217, v48, v49, v50 bitop3:0x80\n"
      "v_bitop3_b32 v217, v217, v51, v52 bitop3:0x80\n"
      "v_mfma_f32_32x32x16_bf16 v[144:159], v[40:43], a[252:255], v[144:159]\n"
      "ds_read_b128 a[228:231], v225 offset:43008\n"
      "buffer_load_dwordx4 v[84:87], v224, s[36:39], s83 offen offset:1024 sc1\n"
      "v_bitop3_b32 v217, v217, v53, v54 bitop3:0x80\n"
      "v_bitop3_b32 v217, v217, v55, 1 bitop3:0x2a\n"
      "v_cmp_ne_u32 vcc, 0, v217\n"
      "s_waitcnt lgkmcnt(6)\n"
      "v_mfma_f32_32x32x16_bf16 v[128:143], v[44:47], v[160:163], v[128:143]\n"
      "ds_read_b128 a[232:235], v225 offset:15360\n"
      "s_cbranch_vccnz .Lst45\n"
      ".Lco46:\n"
      "v_mfma_f32_32x32x16_bf16 v[144:159], v[44:47], v[164:167], v[144:159]\n"
      "ds_read_b128 a[236:239], v225 offset:44032\n"
      "buffer_load_dwordx4 v[88:91], v224, s[36:39], s83 offen offset:2048 sc1\n"
      "s_waitcnt lgkmcnt(6)\n"
      "v_mfma_f32_32x32x16_bf16 v[128:143], v[48:51], v[168:171], v[128:143]\n"
      "ds_read_b128 a[240:243], v225 offset:16384\n"
      "s_waitcnt vmcnt(7)\n"
      "v_bitop3_b32 v217, v56, v57, v58 bitop3:0x80\n"
      "v_bitop3_b32 v217, v217, v59, v60 bitop3:0x80\n"
      "v_mfma_f32_32x32x16_bf16 v[144:159], v[48:51], v[172:175], v[144:159]\n"
      "ds_read_b128 a[244:247], v225 offset:45056\n"
      "buffer_load_dwordx4 v[92:95], v224, s[36:39], s83 offen offset:3072 sc1\n"
      "v_bitop3_b32 v217, v217, v61, v62 bitop3:0x80\n"
      "v_bitop3_b32 v217, v217, v63, 1 bitop3:0x2a\n"
      "v_cmp_ne_u32 vcc, 0, v217\n"
      "s_waitcnt lgkmcnt(6)\n"
      "v_mfma_f32_32x32x16_bf16 v[128:143], v[52:55], v[176:179], v[128:143]\n"
      "ds_read_b128 a[248:251], v225 offset:17408\n"
      "s_cbranch_vccnz .Lst49\n"
      ".Lco50:\n"
      "v_mfma_f32_32x32x16_bf16 v[144:159], v[52:55], v[180:183], v[144:159]\n"
      "ds_read_b128 a[252:255], v225 offset:46080\n"
      "buffer_load_dwordx4 v[96:99], v224, s[36:39], s84 offen sc1\n"
      "s_waitcnt lgkmcnt(6)\n"
      "v_mfma_f32_32x32x16_bf16 v[128:143], v[56:59], a[224:227], v[128:143]\n"
      "ds_read_b128 v[160:163], v225 offset:18432\n"
      "s_waitcnt vmcnt(7)\n"
      "v_bitop3_b32 v217, v64, v65, v66 bitop3:0x80\n"
      "v_bitop3_b32 v217, v217, v67, v68 bitop3:0x80\n"
      "v_mfma_f32_32x32x16_bf16 v[144:159], v[56:59], a[228:231], v[144:159]\n"
      "ds_read_b128 v[164:167], v225 offset:47104\n"
      "buffer_load_dwordx4 v[100:103], v224, s[36:39], s84 offen offset:1024 sc1\n"
      "v_bitop3_b32 v217, v217, v69, v70 bitop3:0x80\n"
      "v_bitop3_b32 v217, v217, v71, 1 bitop3:0x2a\n"
      "v_cmp_ne_u32 vcc, 0, v217\n"
      "s_waitcnt lgkmcnt(6)\n"
      "v_mfma_f32_32x32x16_bf16 v[128:143], v[60:63], a[232:235], v[128:143]\n"
      "ds_read_b128 v[168:171], v225 offset:19456\n"
      "s_cbranch_vccnz .Lst53\n"
      ".Lco54:\n"
      "v_mfma_f32_32x32x16_bf16 v[144:159], v[60:63], a[236:239], v[144:159]\n"
      "ds_read_b128 v[172:175], v225 offset:48128\n"
      "buffer_load_dwordx4 v[104:107], v224, s[36:39], s84 offen offset:2048 sc1\n"
      "v_bitop3_b32 v200, v184, v185, v186 bitop3:0x80\n"
      "v_bitop3_b32 v200, v200, v187, v188 bitop3:0x80\n"
      "v_bitop3_b32 v200, v200, v189, v190 bitop3:0x80\n"
      "v_bitop3_b32 v200, v200, v191, v192 bitop3:0x80\n"
      "v_bitop3_b32 v200, v200, v193, v194 bitop3:0x80\n"
      "v_bitop3_b32 v200, v200, v195, v196 bitop3:0x80\n"
      "v_bitop3_b32 v200, v200, v197, v198 bitop3:0x80\n"
      "v_bitop3_b32 v200, v200, v199, 1 bitop3:0x2a\n"
      "v_cmp_ne_u32 vcc, 0, v200\n"
      "s_cbranch_vccnz .Lst9\n"
      ".Lco10:\n"
      "s_waitcnt lgkmcnt(6)\n"
      "v_mfma_f32_32x32x16_bf16 v[128:143], v[64:67], a[240:243], v[128:143]\n"
      "ds_read_b128 v[176:179], v225 offset:20480\n"
      "s_waitcnt vmcnt(7)\n"
      "v_bitop3_b32 v217, v72, v73, v74 bitop3:0x80\n"
      "v_bitop3_b32 v217, v217, v75, v76 bitop3:0x80\n"
      "v_mfma_f32_32x32x16_bf16 v[144:159], v[64:67], a[244:247], v[144:159]\n"
      "ds_read_b128 v[180:183], v225 offset:49152\n"
      "buffer_load_dwordx4 v[108:111], v224, s[36:39], s84 offen offset:3072 sc1\n"
      "v_bitop3_b32 v217, v217, v77, v78 bitop3:0x80\n"
      "v_bitop3_b32 v217, v217, v79, 1 bitop3:0x2a\n"
      "v_cmp_ne_u32 vcc, 0, v217\n"
      "s_waitcnt lgkmcnt(6)\n"
      "v_mfma_f32_32x32x16_bf16 v[128:143], v[68:71], a[248:251], v[128:143]\n"
      "ds_read_b128 a[224:227], v225 offset:21504\n"
      "s_cbranch_vccnz .Lst57\n"
      ".Lco58:\n"
      "v_mfma_f32_32x32x16_bf16 v[144:159], v[68:71], a[252:255], v[144:159]\n"
      "ds_read_b128 a[228:231], v225 offset:50176\n"
      "s_add_u32 m0, s75, 0\n"
      "s_add_u32 s35, s71, 0\n"
      "buffer_load_dwordx4 v224, s[36:39], s35 offen sc1 lds\n"
      "v_pk_add_f32 v[184:185], v[184:185], v[186:187]\n"
      "v_pk_add_f32 v[188:189], v[188:189], v[190:191]\n"
      "v_pk_add_f32 v[192:193], v[192:193], v[194:195]\n"
      "v_pk_add_f32 v[196:197], v[196:197], v[198:199]\n"
      "v_pk_add_f32 v[184:185], v[184:185], v[188:189]\n"
      "v_pk_add_f32 v[192:193], v[192:193], v[196:197]\n"
      "v_pk_add_f32 v[200:201], v[184:185], v[192:193]\n"
      "v_mov_b32 v202, v200\n"
      "v_mov_b32 v203, v201\n"
      "s_waitcnt lgkmcnt(6)\n"
      "v_mfma_f32_32x32x16_bf16 v[128:143], v[72:75], v[160:163], v[128:143]\n"
      "ds_read_b128 a[232:235], v225 offset:22528\n"
      "s_waitcnt vmcnt(7)\n"
      "v_bitop3_b32 v217, v80, v81, v82 bitop3:0x80\n"
      "v_bitop3_b32 v217, v217, v83, v84 bitop3:0x80\n"
      "v_mfma_f32_32x32x16_bf16 v[144:159], v[72:75], v[164:167], v[144:159]\n"
      "ds_read_b128 a[236:239], v225 offset:51200\n"
      "buffer_load_dwordx4 v224, s[36:39], s35 offen offset:1024 sc1 lds\n"
      "v_bitop3_b32 v217, v217, v85, v86 bitop3:0x80\n"
      "v_bitop3_b32 v217, v217, v87, 1 bitop3:0x2a\n"
      "v_cmp_ne_u32 vcc, 0, v217\n"
      "s_waitcnt lgkmcnt(6)\n"
      "v_mfma_f32_32x32x16_bf16 v[128:143], v[76:79], v[168:171], v[128:143]\n"
      "ds_read_b128 a[240:243], v225 offset:23552\n"
      "s_cbranch_vccnz .Lst61\n"
      ".Lco62:\n"
      "v_mfma_f32_32x32x16_bf16 v[144:159], v[76:79], v[172:175], v[144:159]\n"
      "ds_read_b128 a[244:247], v225 offset:52224\n"
      "buffer_load_dwordx4 v224, s[36:39], s35 offen offset:2048 sc1 lds\n"
      "v_permlane32_swap_b32 v200, v202\n"
      "v_permlane32_swap_b32 v201, v203\n"
      "v_pk_add_f32 v[200:201], v[200:201], v[202:203]\n"
      "v_mul_f32 v200, 0xbb124925, v200\n"
      "v_mul_f32 v201, 0x3b124925, v201\n"
      "v_fma_f32 v201, -v200, v200, v201\n"
      "v_max_f32 v201, 0, v201\n"
      "v_add_f32 v219, 0x3727c5ac, v201\n"
      "v_rsq_f32 v218, v219\n"
      "s_waitcnt lgkmcnt(6)\n"
      "v_mfma_f32_32x32x16_bf16 v[128:143], v[80:83], v[176:179], v[128:143]\n"
      "ds_read_b128 a[248:251], v225 offset:24576\n"
      "s_waitcnt vmcnt(7)\n"
      "v_bitop3_b32 v217, v88, v89, v90 bitop3:0x80\n"
      "v_bitop3_b32 v217, v217, v91, v92 bitop3:0x80\n"
      "v_mfma_f32_32x32x16_bf16 v[144:159], v[80:83], v[180:183], v[144:159]\n"
      "ds_read_b128 a[252:255], v225 offset:53248\n"
      "buffer_load_dwordx4 v224, s[36:39], s35 offen offset:3072 sc1 lds\n"
      "v_bitop3_b32 v217, v217, v93, v94 bitop3:0x80\n"
      "v_bitop3_b32 v217, v217, v95, 1 bitop3:0x2a\n"
      "v_cmp_ne_u32 vcc, 0, v217\n"
      "s_waitcnt lgkmcnt(6)\n"
      "v_mfma_f32_32x32x16_bf16 v[128:143], v[84:87], a[224:227], v[128:143]\n"
      "ds_read_b128 v[160:163], v225 offset:25600\n"
      "s_cbranch_vccnz .Lst65\n"
      ".Lco66:\n"
      "v_mfma_f32_32x32x16_bf16 v[144:159], v[84:87], a[228:231], v[144:159]\n"
      "ds_read_b128 v[164:167], v225 offset:54272\n"
      "s_add_u32 m0, s75, 0x1000\n"
      "s_add_u32 s35, s71, 0x1000\n"
      "buffer_load_dwordx4 v224, s[36:39], s35 offen sc1 lds\n"
      "v_cvt_pk_bf16_f32 v220, v200, v200\n"
      "v_and_b32 v202, 0xffff0000, v220\n"
      "v_sub_f32 v202, v200, v202\n"
      "v_and_b32 v221, 0xffff, v220\n"
      "v_cvt_pk_bf16_f32 v220, v200, v202\n"
      "v_cndmask_b32 v220, v220, 0, s[56:57]\n"
      "v_cndmask_b32 v221, v221, 0, s[56:57]\n"
      "ds_write_b32 v233, v218\n"
      "ds_read_b64 v[212:213], v235 offset:2048\n"
      "s_waitcnt lgkmcnt(8)\n"
      "v_mfma_f32_32x32x16_bf16 v[128:143], v[88:91], a[232:235], v[128:143]\n"
      "ds_read_b128 v[168:171], v225 offset:26624\n"
      "s_waitcnt vmcnt(7)\n"
      "v_bitop3_b32 v217, v96, v97, v98 bitop3:0x80\n"
      "v_bitop3_b32 v217, v217, v99, v100 bitop3:0x80\n"
      "v_mfma_f32_32x32x16_bf16 v[144:159], v[88:91], a[236:239], v[144:159]\n"
      "ds_read_b128 v[172:175], v225 offset:55296\n"
      "buffer_load_dwordx4 v224, s[36:39], s35 offen offset:1024 sc1 lds\n"
      "v_bitop3_b32 v217, v217, v101, v102 bitop3:0x80\n"
      "v_bitop3_b32 v217, v217, v103, 1 bitop3:0x2a\n"
      "v_cmp_ne_u32 vcc, 0, v217\n"
      "s_waitcnt lgkmcnt(8)\n"
      "v_mfma_f32_32x32x16_bf16 v[128:143], v[92:95], a[240:243], v[128:143]\n"
      "ds_read_b128 v[176:179], v225 offset:27648\n"
      "s_cbranch_vccnz .Lst69\n"
      ".Lco70:\n"
      "v_mfma_f32_32x32x16_bf16 v[144:159], v[92:95], a[244:247], v[144:159]\n"
      "ds_read_b128 v[180:183], v225 offset:56320\n"
      "buffer_load_dwordx4 v224, s[36:39], s35 offen offset:2048 sc1 lds\n"
      "ds_read_b128 v[184:187], v234\n"
      "ds_read_b128 v[188:191], v234 offset:32\n"
      "ds_read_b128 v[192:195], v234 offset:64\n"
      "ds_read_b128 v[196:199], v234 offset:96\n"
      "s_waitcnt lgkmcnt(12)\n"
      "v_mfma_f32_32x32x16_bf16 v[128:143], v[96:99], a[248:251], v[128:143]\n"
      "s_waitcnt vmcnt(7)\n"
      "v_bitop3_b32 v217, v104, v105, v106 bitop3:0x80\n"
      "v_bitop3_b32 v217, v217, v107, v108 bitop3:0x80\n"
      "v_mfma_f32_32x32x16_bf16 v[144:159], v[96:99], a[252:255], v[144:159]\n"
      "s_add_u32 s86, s68, 0x3000\n"
      "buffer_load_dwordx4 v[200:203], v236, s[36:39], s86 offen sc1\n"
      "v_bitop3_b32 v217, v217, v109, v110 bitop3:0x80\n"
      "v_bitop3_b32 v217, v217, v111, 1 bitop3:0x2a\n"
      "v_cmp_ne_u32 vcc, 0, v217\n"
      "s_waitcnt lgkmcnt(10)\n"
      "v_mfma_f32_32x32x16_bf16 v[128:143], v[100:103], v[160:163], v[128:143]\n"
      "s_add_u32 s86, s68, 0x3000\n"
      "buffer_load_dwordx4 v[204:207], v237, s[36:39], s86 offen sc1\n"
      "s_cbranch_vccnz .Lst73\n"
      ".Lco74:\n"
      "v_mfma_f32_32x32x16_bf16 v[144:159], v[100:103], v[164:167], v[144:159]\n"
      "s_add_u32 s86, s68, 0x3800\n"
      "buffer_load_dwordx4 v[208:211], v236, s[36:39], s86 offen sc1\n"
      "v_accvgpr_write_b32 a226, 0\n"
      "v_accvgpr_write_b32 a227, 0\n"
      "ds_read_b64 a[224:225], v235 offset:2304\n"
      "s_waitcnt lgkmcnt(7)\n"
      "v_mfma_f32_32x32x16_bf16 v[128:143], v[104:107], v[168:171], v[128:143]\n"
      "v_mfma_f32_32x32x16_bf16 v[144:159], v[104:107], v[172:175], v[144:159]\n"
      "s_waitcnt lgkmcnt(5)\n"
      "v_mfma_f32_32x32x16_bf16 v[128:143], v[108:111], v[176:179], v[128:143]\n"
      "v_mfma_f32_32x32x16_bf16 v[144:159], v[108:111], v[180:183], v[144:159]\n"
      "s_nop 1\n"
      ".Lg1join:\n"
      "s_waitcnt lgkmcnt(0)\n"
      "v_mfma_f32_32x32x16_bf16 v[128:143], v[220:223], v[212:215], v[128:143]\n"
      "v_mfma_f32_32x32x16_bf16 v[144:159], v[220:223], a[224:227], v[144:159]\n"
      "ds_read_b64 v[212:213], v235 offset:2560\n"
      "s_nop 9\n"
      "v_pk_fma_f32 v[128:129], v[128:129], v[184:185], s[94:95]\n"
      "v_pk_fma_f32 v[130:131], v[130:131], v[186:187], s[94:95]\n"
      "v_pk_fma_f32 v[132:133], v[132:133], v[188:189], s[94:95]\n"
      "v_pk_fma_f32 v[134:135], v[134:135], v[190:191], s[94:95]\n"
      "v_pk_fma_f32 v[136:137], v[136:137], v[192:193], s[94:95]\n"
      "v_pk_fma_f32 v[138:139], v[138:139], v[194:195], s[94:95]\n"
      "v_pk_fma_f32 v[140:141], v[140:141], v[196:197], s[94:95]\n"
      "v_pk_fma_f32 v[142:143], v[142:143], v[198:199], s[94:95]\n"
      "v_pk_mul_f32 v[112:113], v[128:129], s[92:93]\n"
      "v_pk_mul_f32 v[114:115], v[130:131], s[92:93]\n"
      "v_pk_mul_f32 v[116:117], v[132:133], s[92:93]\n"
      "s_cmp_lg_u32 s48, 0\n"
      "s_cbranch_scc1 .Lsk77\n"
      "s_add_u32 s86, s68, 0x3000\n"
      "s_waitcnt vmcnt(2)\n"
      "buffer_load_dwordx4 v[200:203], v236, s[36:39], s86 offen sc1\n"
      "s_add_u32 s86, s68, 0x3000\n"
      "s_waitcnt vmcnt(2)\n"
      "buffer_load_dwordx4 v[204:207], v237, s[36:39], s86 offen sc1\n"
      "s_add_u32 s86, s68, 0x3800\n"
      "s_waitcnt vmcnt(2)\n"
      "buffer_load_dwordx4 v[208:211], v236, s[36:39], s86 offen sc1\n"
      ".Lsk77:\n"
      "v_pk_mul_f32 v[118:119], v[134:135], s[92:93]\n"
      "v_pk_mul_f32 v[120:121], v[136:137], s[92:93]\n"
      "v_pk_mul_f32 v[122:123], v[138:139], s[92:93]\n"
      "v_pk_mul_f32 v[124:125], v[140:141], s[92:93]\n"
      "v_pk_mul_f32 v[126:127], v[142:143], s[92:93]\n"
      "v_pk_mul_f32 v[144:145], v[144:145], v[184:185]\n"
      "v_pk_mul_f32 v[146:147], v[146:147], v[186:187]\n"
      "v_pk_mul_f32 v[148:149], v[148:149], v[188:189]\n"
      "v_pk_mul_f32 v[150:151], v[150:151], v[190:191]\n"
      "v_pk_mul_f32 v[152:153], v[152:153], v[192:193]\n"
      "v_pk_mul_f32 v[154:155], v[154:155], v[194:195]\n"
      "buffer_store_dwordx4 a[88:91], v224, s[28:31], s24 offen sc1\n"
      "s_add_u32 s24, s24, s26\n"
      "v_pk_mul_f32 v[156:157], v[156:157], v[196:197]\n"
      "v_pk_mul_f32 v[158:159], v[158:159], v[198:199]\n"
      "v_exp_f32 v112, v112 clamp\n"
      "v_max_f32 v128, s94, v128\n"
      "v_exp_f32 v113, v113 clamp\n"
      "v_max_f32 v129, s94, v129\n"
      "v_exp_f32 v114, v114 clamp\n"
      "v_max_f32 v130, s94, v130\n"
      "v_exp_f32 v115, v115 clamp\n"
      "v_max_f32 v131, s94, v131\n"
      "v_exp_f32 v116, v116 clamp\n"
      "buffer_store_dwordx4 a[92:95], v224, s[28:31], s25 offen sc1\n"
      "s_add_u32 s25, s25, s26\n"
      "v_max_f32 v132, s94, v132\n"
      "v_exp_f32 v117, v117 clamp\n"
      "v_max_f32 v133, s94, v133\n"
      "v_exp_f32 v118, v118 clamp\n"
      "v_max_f32 v134, s94, v134\n"
      "v_exp_f32 v119, v119 clamp\n"
      "v_max_f32 v135, s94, v135\n"
      "v_exp_f32 v120, v120 clamp\n"
      "v_max_f32 v136, s94, v136\n"
      "v_exp_f32 v121, v121 clamp\n"
      "v_max_f32 v137, s94, v137\n"
      "s_add_u32 s86, s68, 0x3800\n"
      "buffer_load_dwordx4 v[180:183], v237, s[36:39], s86 offen sc1\n"
      "v_exp_f32 v122, v122 clamp\n"
      "v_max_f32 v138, s94, v138\n"
      "v_exp_f32 v123, v123 clamp\n"
      "v_max_f32 v139, s94, v139\n"
      "v_exp_f32 v124, v124 clamp\n"
      "v_max_f32 v140, s94, v140\n"
      "v_exp_f32 v125, v125 clamp\n"
      "v_max_f32 v141, s94, v141\n"
      "v_exp_f32 v126, v126 clamp\n"
      "v_max_f32 v142, s94, v142\n"
      "v_exp_f32 v127, v127 clamp\n"
      "s_add_u32 s86, s68, 0\n"
      "buffer_load_dwordx4 a[228:231], v237, s[36:39], s86 offen sc1\n"
      "v_max_f32 v143, s94, v143\n"
      "v_pk_add_f32 v[128:129], v[128:129], v[112:113]\n"
      "v_pk_add_f32 v[130:131], v[130:131], v[114:115]\n"
      "v_pk_add_f32 v[132:133], v[132:133], v[116:117]\n"
      "v_pk_add_f32 v[134:135], v[134:135], v[118:119]\n"
      "v_pk_add_f32 v[136:137], v[136:137], v[120:121]\n"
      "v_pk_add_f32 v[138:139], v[138:139], v[122:123]\n"
      "v_pk_add_f32 v[140:141], v[140:141], v[124:125]\n"
      "v_pk_add_f32 v[142:143], v[142:143], v[126:127]\n"
      "v_mov_b32 v217, 0x3f803f80\n"
      "v_cvt_pk_bf16_f32 v112, v128, v129\n"
      "s_mov_b32 s34, 0x20000\n"
      "buffer_store_dwordx4 a[100:103], v229, s[40:43], s34 offen sc1\n"
      "v_cvt_pk_bf16_f32 v113, v130, v131\n"
      "v_cvt_pk_bf16_f32 v114, v132, v133\n"
      "v_cvt_pk_bf16_f32 v115, v134, v135\n"
      "v_cvt_pk_bf16_f32 v116, v136, v137\n"
      "v_cvt_pk_bf16_f32 v117, v138, v139\n"
      "v_cvt_pk_bf16_f32 v118, v140, v141\n"
      "v_cvt_pk_bf16_f32 v119, v142, v143\n"
      "v_cvt_pk_bf16_f32 v120, v144, v145\n"
      "v_cvt_pk_bf16_f32 v121, v146, v147\n"
      "v_cvt_pk_bf16_f32 v122, v148, v149\n"
      "v_cvt_pk_bf16_f32 v123, v150, v151\n"
      "s_mov_b32 s34, 0x20000\n"
      "buffer_store_dwordx4 a[100:103], v230, s[40:43], s34 offen sc1\n"
      "v_cvt_pk_bf16_f32 v124, v152, v153\n"
      "v_cvt_pk_bf16_f32 v125, v154, v155\n"
      "v_cvt_pk_bf16_f32 v126, v156, v157\n"
      "v_cvt_pk_bf16_f32 v127, v158, v159\n"
      "v_cndmask_b32 v120, v120, v217, s[58:59]\n"
      "v_cndmask_b32 v121, v121, v217, s[58:59]\n"
      "v_cndmask_b32 v122, v122, v217, s[58:59]\n"
      "v_cndmask_b32 v123, v123, v217, s[58:59]\n"
      "v_cndmask_b32 v124, v124, v217, s[58:59]\n"
      "v_cndmask_b32 v125, v125, v217, s[58:59]\n"
      "v_cndmask_b32 v126, v126, v217, s[58:59]\n"
      "s_add_u32 s86, s68, 0x800\n"
      "buffer_load_dwordx4 a[232:235], v236, s[36:39], s86 offen sc1\n"
      "v_cndmask_b32 v127, v127, v217, s[58:59]\n"
      "v_mfma_f32_32x32x16_bf16 v[128:143], v[112:115], v[120:123], 0\n"
      "s_nop 0\n"
      "v_mfma_f32_32x32x16_bf16 v[128:143], v[116:119], v[124:127], v[128:143]\n"
      "s_add_u32 s86, s68, 0x800\n"
      "buffer_load_dwordx4 a[236:239], v237, s[36:39], s86 offen sc1\n"
      "ds_read_b128 v[160:163], v226\n"
      "ds_read_b128 v[164:167], v226 offset:1024\n"
      "ds_read_b128 v[168:171], v226 offset:2048\n"
      "ds_read_b128 v[172:175], v226 offset:3072\n"
      "s_nop 5\n"
      "ds_write_b128 v227, v[128:131]\n"
      "s_mov_b32 s34, 0x3a0000\n"
      "buffer_store_dwordx2 a[100:101], v231, s[40:43], s34 offen sc1\n"
      "ds_write_b128 v227, v[132:135] offset:1024\n"
      "ds_write_b128 v227, v[136:139] offset:2048\n"
      "ds_write_b128 v227, v[140:143] offset:3072\n"
      "s_waitcnt lgkmcnt(0)\n"
      "s_barrier\n"
      "ds_read_b128 v[144:147], v228\n"
      "ds_read_b128 v[148:151], v228 offset:4096\n"
      "ds_read_b128 v[152:155], v228 offset:8192\n"
      "ds_read_b128 v[156:159], v228 offset:12288\n"
      "s_waitcnt lgkmcnt(2)\n"
      "v_pk_add_f32 v[144:145], v[144:145], v[148:149]\n"
      "v_pk_add_f32 v[146:147], v[146:147], v[150:151]\n"
      "s_waitcnt lgkmcnt(1)\n"
      "v_pk_add_f32 v[144:145], v[144:145], v[152:153]\n"
      "v_pk_add_f32 v[146:147], v[146:147], v[154:155]\n"
      "s_waitcnt lgkmcnt(0)\n"
      "v_pk_add_f32 v[144:145], v[144:145], v[156:157]\n"
      "v_pk_add_f32 v[146:147], v[146:147], v[158:159]\n"
      "buffer_store_dwordx4 v[144:147], v224, s[36:39], s66 offen sc1\n"
      "s_memtime s[90:91]\n"
      "s_waitcnt lgkmcnt(0)\n"
      "s_mov_b32 s96, s90\n"
      "v_mfma_f32_32x32x16_bf16 v[112:127], v[160:163], v[0:3], 0\n"
      "ds_read_b128 v[176:179], v226 offset:4096\n"
      "s_add_u32 s86, s68, 0x1000\n"
      "buffer_load_dwordx4 a[240:243], v236, s[36:39], s86 offen sc1\n"
      "v_mfma_f32_32x32x16_bf16 v[112:127], v[164:167], v[4:7], v[112:127]\n"
      "ds_read_b128 v[160:163], v226 offset:5120\n"
      "v_mfma_f32_32x32x16_bf16 v[112:127], v[168:171], v[8:11], v[112:127]\n"
      "ds_read_b128 v[164:167], v226 offset:6144\n"
      "v_mfma_f32_32x32x16_bf16 v[112:127], v[172:175], v[12:15], v[112:127]\n"
      "ds_read_b128 v[168:171], v226 offset:7168\n"
      "s_waitcnt lgkmcnt(3)\n"
      "v_mfma_f32_32x32x16_bf16 v[112:127], v[176:179], v[16:19], v[112:127]\n"
      "ds_read_b128 v[172:175], v226 offset:8192\n"
      "s_add_u32 s86, s68, 0x1000\n"
      "buffer_load_dwordx4 a[244:247], v237, s[36:39], s86 offen sc1\n"
      "s_waitcnt lgkmcnt(3)\n"
      "v_mfma_f32_32x32x16_bf16 v[112:127], v[160:163], v[20:23], v[112:127]\n"
      "ds_read_b128 v[176:179], v226 offset:9216\n"
      "s_waitcnt lgkmcnt(3)\n"
      "v_mfma_f32_32x32x16_bf16 v[112:127], v[164:167], v[24:27], v[112:127]\n"
      "ds_read_b128 v[160:163], v226 offset:10240\n"
      "s_waitcnt lgkmcnt(3)\n"
      "v_mfma_f32_32x32x16_bf16 v[112:127], v[168:171], v[28:31], v[112:127]\n"
      "ds_read_b128 v[164:167], v226 offset:11264\n"
      "s_waitcnt lgkmcnt(3)\n"
      "v_mfma_f32_32x32x16_bf16 v[112:127], v[172:175], v[32:35], v[112:127]\n"
      "ds_read_b128 v[168:171], v226 offset:12288\n"
      "s_add_u32 s86, s68, 0x1800\n"
      "buffer_load_dwordx4 a[248:251], v236, s[36:39], s86 offen sc1\n"
      "s_waitcnt lgkmcnt(3)\n"
      "v_mfma_f32_32x32x16_bf16 v[112:127], v[176:179], v[36:39], v[112:127]\n"
      "ds_read_b128 v[172:175], v226 offset:13312\n"
      "s_waitcnt lgkmcnt(3)\n"
      "v_mfma_f32_32x32x16_bf16 v[112:127], v[160:163], v[40:43], v[112:127]\n"
      "ds_read_b128 v[176:179], v226 offset:14336\n"
      "s_waitcnt lgkmcnt(3)\n"
      "v_mfma_f32_32x32x16_bf16 v[112:127], v[164:167], v[44:47], v[112:127]\n"
      "ds_read_b128 v[160:163], v226 offset:15360\n"
      "s_waitcnt lgkmcnt(3)\n"
      "v_mfma_f32_32x32x16_bf16 v[112:127], v[168:171], v[48:51], v[112:127]\n"
      "ds_read_b128 v[164:167], v226 offset:16384\n"
      "s_add_u32 s86, s68, 0x1800\n"
      "buffer_load_dwordx4 a[252:255], v237, s[36:39], s86 offen sc1\n"
      "s_waitcnt lgkmcnt(3)\n"
      "v_mfma_f32_32x32x16_bf16 v[112:127], v[172:175], v[52:55], v[112:127]\n"
      "ds_read_b128 v[168:171], v226 offset:17408\n"
      "s_waitcnt lgkmcnt(3)\n"
      "v_mfma_f32_32x32x16_bf16 v[112:127], v[176:179], v[56:59], v[112:127]\n"
      "ds_read_b128 v[172:175], v226 offset:18432\n"
      "s_waitcnt lgkmcnt(3)\n"
      "v_mfma_f32_32x32x16_bf16 v[112:127], v[160:163], v[60:63], v[112:127]\n"
      "ds_read_b128 v[176:179], v226 offset:19456\n"
      "s_waitcnt lgkmcnt(3)\n"
      "v_mfma_f32_32x32x16_bf16 v[112:127], v[164:167], v[64:67], v[112:127]\n"
      "ds_read_b128 v[160:163], v226 offset:20480\n"
      "s_add_u32 s86, s68, 0\n"
      "buffer_load_dwordx4 a[224:227], v236, s[36:39], s86 offen sc1\n"
      "s_waitcnt lgkmcnt(3)\n"
      "v_mfma_f32_32x32x16_bf16 v[112:127], v[168:171], v[68:71], v[112:127]\n"
      "ds_read_b128 v[164:167], v226 offset:21504\n"
      "s_waitcnt lgkmcnt(3)\n"
      "v_mfma_f32_32x32x16_bf16 v[112:127], v[172:175], v[72:75], v[112:127]\n"
      "ds_read_b128 v[168:171], v226 offset:22528\n"
      "s_waitcnt lgkmcnt(3)\n"
      "v_mfma_f32_32x32x16_bf16 v[112:127], v[176:179], v[76:79], v[112:127]\n"
      "ds_read_b128 v[172:175], v226 offset:23552\n"
      "s_waitcnt lgkmcnt(3)\n"
      "v_mfma_f32_32x32x16_bf16 v[112:127], v[160:163], v[80:83], v[112:127]\n"
      "ds_read_b128 v[176:179], v226 offset:24576\n"
      "s_add_u32 s86, s68, 0x2000\n"
      "buffer_load_dwordx4 v[184:187], v236, s[36:39], s86 offen sc1\n"
      "s_waitcnt lgkmcnt(3)\n"
      "v_mfma_f32_32x32x16_bf16 v[112:127], v[164:167], v[84:87], v[112:127]\n"
      "ds_read_b128 v[160:163], v226 offset:25600\n"
      "s_waitcnt lgkmcnt(3)\n"
      "v_mfma_f32_32x32x16_bf16 v[112:127], v[168:171], v[88:91], v[112:127]\n"
      "ds_read_b128 v[164:167], v226 offset:26624\n"
      "s_waitcnt lgkmcnt(3)\n"
      "v_mfma_f32_32x32x16_bf16 v[112:127], v[172:175], v[92:95], v[112:127]\n"
      "ds_read_b128 v[168:171], v226 offset:27648\n"
      "s_waitcnt lgkmcnt(3)\n"
      "v_mfma_f32_32x32x16_bf16 v[112:127], v[176:179], v[96:99], v[112:127]\n"
      "s_add_u32 s86, s68, 0x2000\n"
      "buffer_load_dwordx4 v[188:191], v237, s[36:39], s86 offen sc1\n"
      "s_waitcnt lgkmcnt(2)\n"
      "v_mfma_f32_32x32x16_bf16 v[112:127], v[160:163], v[100:103], v[112:127]\n"
      "s_waitcnt lgkmcnt(1)\n"
      "v_mfma_f32_32x32x16_bf16 v[112:127], v[164:167], v[104:107], v[112:127]\n"
      "s_waitcnt lgkmcnt(0)\n"
      "v_mfma_f32_32x32x16_bf16 v[112:127], v[168:171], v[108:111], v[112:127]\n"
      "v_mfma_f32_32x32x16_bf16 v[112:127], v[212:215], v[220:223], v[112:127]\n"
      "s_cmp_lg_u32 s48, 0\n"
      "s_cbranch_scc1 .Lsk78\n"
      "s_or_b32 s87, s52, s51\n"
      "s_cmp_lg_u32 s87, 0\n"
      "s_cbranch_scc1 .Lsk19\n"
      "v_mov_b32 v128, s54\n"
      "v_mov_b32 v129, s55\n"
      "v_cmp_eq_u32 vcc, 0, v238\n"
      "v_mov_b32 v130, 0x80000000\n"
      "s_nop 0\n"
      "v_cndmask_b32 v130, v130, 0, vcc\n"
      "buffer_store_dwordx2 v[128:129], v130, s[36:39], 0 offen sc1\n"
      "s_nop 1\n"
      ".Lsk19:\n"
      ".Lsk78:\n"
      "buffer_load_dwordx4 v[128:131], v248, s[36:39], s67 offen sc1\n"
      "s_nop 8\n"
      "v_pk_fma_f32 v[112:113], v[112:113], v[218:219], s[94:95] op_sel_hi:[1,0,1]\n"
      "v_pk_fma_f32 v[114:115], v[114:115], v[218:219], s[94:95] op_sel_hi:[1,0,1]\n"
      "v_pk_fma_f32 v[116:117], v[116:117], v[218:219], s[94:95] op_sel_hi:[1,0,1]\n"
      "v_pk_fma_f32 v[118:119], v[118:119], v[218:219], s[94:95] op_sel_hi:[1,0,1]\n"
      "v_pk_fma_f32 v[120:121], v[120:121], v[218:219], s[94:95] op_sel_hi:[1,0,1]\n"
      "v_pk_fma_f32 v[122:123], v[122:123], v[218:219], s[94:95] op_sel_hi:[1,0,1]\n"
      "v_pk_fma_f32 v[124:125], v[124:125], v[218:219], s[94:95] op_sel_hi:[1,0,1]\n"
      "v_pk_fma_f32 v[126:127], v[126:127], v[218:219], s[94:95] op_sel_hi:[1,0,1]\n"
      "v_pk_mul_f32 v[144:145], v[112:113], s[92:93]\n"
      "v_pk_mul_f32 v[146:147], v[114:115], s[92:93]\n"
      "v_pk_mul_f32 v[148:149], v[116:117], s[92:93]\n"
      "v_pk_mul_f32 v[150:151], v[118:119], s[92:93]\n"
      "v_pk_mul_f32 v[152:153], v[120:121], s[92:93]\n"
      "v_pk_mul_f32 v[154:155], v[122:123], s[92:93]\n"
      "v_pk_mul_f32 v[156:157], v[124:125], s[92:93]\n"
      "v_pk_mul_f32 v[158:159], v[126:127], s[92:93]\n"
      "v_exp_f32 v144, v144 clamp\n"
      "v_max_f32 v112, s94, v112\n"
      "v_exp_f32 v145, v145 clamp\n"
      "v_max_f32 v113, s94, v113\n"
      "s_add_u32 s86, s68, 0x2800\n"
      "buffer_load_dwordx4 v[192:195], v236, s[36:39], s86 offen sc1\n"
      "v_exp_f32 v146, v146 clamp\n"
      "v_max_f32 v114, s94, v114\n"
      "v_exp_f32 v147, v147 clamp\n"
      "v_max_f32 v115, s94, v115\n"
      "v_exp_f32 v148, v148 clamp\n"
      "v_max_f32 v116, s94, v116\n"
      "v_exp_f32 v149, v149 clamp\n"
      "v_max_f32 v117, s94, v117\n"
      "v_exp_f32 v150, v150 clamp\n"
      "v_max_f32 v118, s94, v118\n"
      "v_exp_f32 v151, v151 clamp\n"
      "v_max_f32 v119, s94, v119\n"
      "v_exp_f32 v152, v152 clamp\n"
      "v_max_f32 v120, s94, v120\n"
      "v_exp_f32 v153, v153 clamp\n"
      "v_max_f32 v121, s94, v121\n"
      "v_exp_f32 v154, v154 clamp\n"
      "v_max_f32 v122, s94, v122\n"
      "v_exp_f32 v155, v155 clamp\n"
      "v_max_f32 v123, s94, v123\n"
      "s_add_u32 s86, s68, 0x2800\n"
      "buffer_load_dwordx4 v[196:199], v237, s[36:39], s86 offen sc1\n"
      "v_exp_f32 v156, v156 clamp\n"
      "v_max_f32 v124, s94, v124\n"
      "v_exp_f32 v157, v157 clamp\n"
      "v_max_f32 v125, s94, v125\n"
      "v_exp_f32 v158, v158 clamp\n"
      "v_max_f32 v126, s94, v126\n"
      "v_exp_f32 v159, v159 clamp\n"
      "v_max_f32 v127, s94, v127\n"
      "v_pk_add_f32 v[112:113], v[112:113], v[144:145]\n"
      "v_pk_add_f32 v[114:115], v[114:115], v[146:147]\n"
      "v_pk_add_f32 v[116:117], v[116:117], v[148:149]\n"
      "v_pk_add_f32 v[118:119], v[118:119], v[150:151]\n"
      "v_pk_add_f32 v[120:121], v[120:121], v[152:153]\n"
      "v_pk_add_f32 v[122:123], v[122:123], v[154:155]\n"
      "v_pk_add_f32 v[124:125], v[124:125], v[156:157]\n"
      "v_pk_add_f32 v[126:127], v[126:127], v[158:159]\n"
      "v_cvt_pk_bf16_f32 v136, v112, v113\n"
      "v_cvt_pk_bf16_f32 v137, v114, v115\n"
      "v_cvt_pk_bf16_f32 v138, v116, v117\n"
      "v_cvt_pk_bf16_f32 v139, v118, v119\n"
      "v_cvt_pk_bf16_f32 v140, v120, v121\n"
      "v_cvt_pk_bf16_f32 v141, v122, v123\n"
      "v_cvt_pk_bf16_f32 v142, v124, v125\n"
      "v_cvt_pk_bf16_f32 v143, v126, v127\n"
      "v_cndmask_b32 v142, v142, 0, s[56:57]\n"
      "v_cndmask_b32 v143, v143, 0, s[56:57]\n"
      "v_and_or_b32 v136, v136, -2, s97\n"
      "v_and_or_b32 v137, v137, -2, s97\n"
      "v_and_or_b32 v138, v138, -2, s97\n"
      "v_and_or_b32 v139, v139, -2, s97\n"
      "v_and_or_b32 v140, v140, -2, s97\n"
      "v_and_or_b32 v141, v141, -2, s97\n"
      "v_and_or_b32 v142, v142, -2, s97\n"
      "v_and_or_b32 v143, v143, -2, s97\n"
      "buffer_store_dwordx4 v[136:139], v224, s[36:39], s69 offen sc1\n"
      "buffer_store_dwordx4 v[140:143], v224, s[36:39], s69 offen offset:1024 sc1\n"
      "s_waitcnt vmcnt(4)\n"
      "v_cmp_u_f32 vcc, v128, v129\n"
      "v_cmp_u_f32 s[86:87], v130, v131\n"
      "s_or_b64 vcc, vcc, s[86:87]\n"
      "s_cbranch_vccnz .Lst79\n"
      ".Lco80:\n"
      "s_mov_b32 s86, s99\n"
      "s_mov_b32 s87, s99\n"
      "v_add_f32_dpp v128, v128, v128 row_ror:4 row_mask:0xf bank_mask:0xf\n"
      "v_add_f32_dpp v129, v129, v129 row_ror:4 row_mask:0xf bank_mask:0xf\n"
      "v_add_f32_dpp v130, v130, v130 row_ror:4 row_mask:0xf bank_mask:0xf\n"
      "v_add_f32_dpp v131, v131, v131 row_ror:4 row_mask:0xf bank_mask:0xf\n"
      "v_add_f32_dpp v128, v128, v128 row_ror:8 row_mask:0xf bank_mask:0xf\n"
      "v_add_f32_dpp v129, v129, v129 row_ror:8 row_mask:0xf bank_mask:0xf\n"
      "v_add_f32_dpp v130, v130, v130 row_ror:8 row_mask:0xf bank_mask:0xf\n"
      "v_add_f32_dpp v131, v131, v131 row_ror:8 row_mask:0xf bank_mask:0xf\n"
      "v_mov_b32 v132, v128\n"
      "v_mov_b32 v133, v129\n"
      "v_mov_b32 v134, v130\n"
      "v_mov_b32 v135, v131\n"
      "v_permlane16_swap_b32 v128, v132\n"
      "v_permlane16_swap_b32 v129, v133\n"
      "v_permlane16_swap_b32 v130, v134\n"
      "v_permlane16_swap_b32 v131, v135\n"
      "v_pk_add_f32 v[128:129], v[128:129], v[132:133]\n"
      "v_pk_add_f32 v[130:131], v[130:131], v[134:135]\n"
      "v_mov_b32 v132, v128\n"
      "v_mov_b32 v133, v129\n"
      "v_mov_b32 v134, v130\n"
      "v_mov_b32 v135, v131\n"
      "v_permlane32_swap_b32 v128, v132\n"
      "v_permlane32_swap_b32 v129, v133\n"
      "v_permlane32_swap_b32 v130, v134\n"
      "v_permlane32_swap_b32 v131, v135\n"
      "v_pk_add_f32 v[128:129], v[128:129], v[132:133]\n"
      "v_pk_add_f32 v[130:131], v[130:131], v[134:135]\n"
      "v_cvt_pk_bf16_f32 v128, v128, v129\n"
      "v_cvt_pk_bf16_f32 v129, v130, v131\n"
      "v_cndmask_b32 v128, v128, 0, s[86:87]\n"
      "v_cndmask_b32 v129, v129, 0, s[86:87]\n"
      "buffer_store_dwordx2 v[128:129], v239, s[36:39], s63 offen sc1\n"
      "s_memtime s[90:91]\n"
      "s_waitcnt lgkmcnt(0)\n"
      "s_mov_b32 s96, s90\n"
      "s_cmp_lg_u32 s48, 0\n"
      "s_cbranch_scc1 .Lsk82\n"
      "s_barrier\n"
      "v_mov_b32 v168, s54\n"
      "v_mov_b32 v169, s55\n"
      "v_cmp_eq_u32 vcc, 0, v238\n"
      "v_mov_b32 v170, 0x80000000\n"
      "s_nop 0\n"
      "v_cndmask_b32 v170, v170, 0, vcc\n"
      "s_lshl_b32 s87, s52, 3\n"
      "s_add_u32 s87, s87, 0x1800\n"
      "s_cmp_eq_u32 s51, 0\n"
      "s_cselect_b32 s86, 0, 0x80000000\n"
      "v_or_b32 v170, s86, v170\n"
      "buffer_store_dwordx2 v[168:169], v170, s[36:39], s87 offen sc1\n"
      "s_nop 1\n"
      ".Lsk82:\n"
      "; G2\n"
      "v_add_u32 v218, 0xe000, v252\n"
      "s_add_u32 s80, s70, 0x1000\n"
      "s_add_u32 s81, s88, 0x1000\n"
      "s_min_u32 s82, s70, s88\n"
      "s_lshl_b32 s83, s4, 13\n"
      "s_add_u32 s83, s83, s63\n"
      "s_add_u32 s84, s83, 0x1000\n"
      "v_and_b32 v216, 3, v238\n"
      "v_lshlrev_b32 v216, 11, v216\n"
      "v_bfe_u32 v217, v238, 2, 1\n"
      "v_lshl_add_u32 v216, v217, 15, v216\n"
      ".Lhd86:\n"
      "buffer_load_dword v217, v216, s[36:39], s82 offen offset:1520 sc1\n"
      "buffer_load_dwordx4 v[0:3], v224, s[36:39], s70 offen sc1\n"
      "buffer_load_dwordx4 v[4:7], v224, s[36:39], s70 offen offset:1024 sc1\n"
      "buffer_load_dwordx4 v[8:11], v224, s[36:39], s70 offen offset:2048 sc1\n"
      "buffer_load_dwordx4 v[12:15], v224, s[36:39], s70 offen offset:3072 sc1\n"
      "buffer_load_dwordx4 v[16:19], v224, s[36:39], s80 offen sc1\n"
      "buffer_load_dwordx4 v[20:23], v224, s[36:39], s80 offen offset:1024 sc1\n"
      "buffer_load_dwordx4 v[24:27], v224, s[36:39], s80 offen offset:2048 sc1\n"
      "buffer_load_dwordx4 v[28:31], v224, s[36:39], s80 offen offset:3072 sc1\n"
      "buffer_load_dwordx4 v[32:35], v224, s[36:39], s88 offen sc1\n"
      "buffer_load_dwordx4 v[36:39], v224, s[36:39], s88 offen offset:1024 sc1\n"
      "buffer_load_dwordx4 v[40:43], v224, s[36:39], s88 offen offset:2048 sc1\n"
      "buffer_load_dwordx4 v[44:47], v224, s[36:39], s88 offen offset:3072 sc1\n"
      "buffer_load_dwordx4 v[48:51], v224, s[36:39], s81 offen sc1\n"
      "buffer_load_dwordx4 v[52:55], v224, s[36:39], s81 offen offset:1024 sc1\n"
      "buffer_load_dwordx4 v[56:59], v224, s[36:39], s81 offen offset:2048 sc1\n"
      "buffer_load_dwordx4 v[60:63], v224, s[36:39], s81 offen offset:3072 sc1\n"
      "s_waitcnt vmcnt(16)\n"
      "v_bitop3_b32 v217, v217, s97, 1 bitop3:0x28\n"
      "v_cmp_ne_u32 vcc, 0, v217\n"
      "s_cbranch_vccz .Lco88\n"
      "s_sub_u32 s77, s77, 1\n"
      "s_cmp_gt_i32 s77, 0\n"
      "s_cbranch_scc1 .Lhd86\n"
      ".Lco88:\n"
      "buffer_load_dwordx4 v[128:131], v224, s[36:39], s83 offen sc1\n"
      "buffer_load_dwordx4 v[132:135], v224, s[36:39], s83 offen offset:1024 sc1\n"
      "buffer_load_dwordx4 v[136:139], v224, s[36:39], s83 offen offset:2048 sc1\n"
      "buffer_load_dwordx4 v[140:143], v224, s[36:39], s83 offen offset:3072 sc1\n"
      "buffer_load_dwordx4 v[144:147], v224, s[36:39], s84 offen sc1\n"
      "buffer_load_dwordx4 v[148:151], v224, s[36:39], s84 offen offset:1024 sc1\n"
      "buffer_load_dwordx4 v[152:155], v224, s[36:39], s84 offen offset:2048 sc1\n"
      "buffer_load_dwordx4 v[156:159], v224, s[36:39], s84 offen offset:3072 sc1\n"
      "s_barrier\n"
      "s_add_u32 s34, s73, 0\n"
      "buffer_store_dwordx4 a[100:103], v224, s[40:43], s34 offen sc1\n"
      "s_waitcnt vmcnt(23)\n"
      "v_bitop3_b32 v217, v0, v1, s97 bitop3:0x7e\n"
      "v_bitop3_b32 v219, v2, v3, s97 bitop3:0x7e\n"
      "v_bitop3_b32 v176, v4, v5, s97 bitop3:0x7e\n"
      "v_or3_b32 v217, v217, v219, v176\n"
      "v_bitop3_b32 v219, v6, v7, s97 bitop3:0x7e\n"
      "v_bitop3_b32 v217, v217, v219, 1 bitop3:0xa8\n"
      "v_cmp_ne_u32 vcc, 0, v217\n"
      "s_cbranch_vccnz .Lst89\n"
      ".Lco90:\n"
      "s_waitcnt vmcnt(21)\n"
      "v_bitop3_b32 v217, v8, v9, s97 bitop3:0x7e\n"
      "v_bitop3_b32 v219, v10, v11, s97 bitop3:0x7e\n"
      "v_bitop3_b32 v176, v12, v13, s97 bitop3:0x7e\n"
      "v_or3_b32 v217, v217, v219, v176\n"
      "v_bitop3_b32 v219, v14, v15, s97 bitop3:0x7e\n"
      "v_bitop3_b32 v217, v217, v219, 1 bitop3:0xa8\n"
      "v_cmp_ne_u32 vcc, 0, v217\n"
      "s_cbranch_vccnz .Lst93\n"
      ".Lco94:\n"
      "s_waitcnt vmcnt(19)\n"
      "v_bitop3_b32 v217, v16, v17, s97 bitop3:0x7e\n"
      "v_bitop3_b32 v219, v18, v19, s97 bitop3:0x7e\n"
      "v_bitop3_b32 v176, v20, v21, s97 bitop3:0x7e\n"
      "v_or3_b32 v217, v217, v219, v176\n"
      "v_bitop3_b32 v219, v22, v23, s97 bitop3:0x7e\n"
      "v_bitop3_b32 v217, v217, v219, 1 bitop3:0xa8\n"
      "v_cmp_ne_u32 vcc, 0, v217\n"
      "s_cbranch_vccnz .Lst97\n"
      ".Lco98:\n"
      "s_waitcnt vmcnt(17)\n"
      "v_bitop3_b32 v217, v24, v25, s97 bitop3:0x7e\n"
      "v_bitop3_b32 v219, v26, v27, s97 bitop3:0x7e\n"
      "v_bitop3_b32 v176, v28, v29, s97 bitop3:0x7e\n"
      "v_or3_b32 v217, v217, v219, v176\n"
      "v_bitop3_b32 v219, v30, v31, s97 bitop3:0x7e\n"
      "v_bitop3_b32 v217, v217, v219, 1 bitop3:0xa8\n"
      "v_cmp_ne_u32 vcc, 0, v217\n"
      "s_cbranch_vccnz .Lst101\n"
      ".Lco102:\n"
      "s_waitcnt vmcnt(15)\n"
      "v_bitop3_b32 v217, v32, v33, s97 bitop3:0x7e\n"
      "v_bitop3_b32 v219, v34, v35, s97 bitop3:0x7e\n"
      "v_bitop3_b32 v176, v36, v37, s97 bitop3:0x7e\n"
      "v_or3_b32 v217, v217, v219, v176\n"
      "v_bitop3_b32 v219, v38, v39, s97 bitop3:0x7e\n"
      "v_bitop3_b32 v217, v217, v219, 1 bitop3:0xa8\n"
      "v_cmp_ne_u32 vcc, 0, v217\n"
      "s_cbranch_vccnz .Lst105\n"
      ".Lco106:\n"
      "s_waitcnt vmcnt(13)\n"
      "v_bitop3_b32 v217, v40, v41, s97 bitop3:0x7e\n"
      "v_bitop3_b32 v219, v42, v43, s97 bitop3:0x7e\n"
      "v_bitop3_b32 v176, v44, v45, s97 bitop3:0x7e\n"
      "v_or3_b32 v217, v217, v219, v176\n"
      "v_bitop3_b32 v219, v46, v47, s97 bitop3:0x7e\n"
      "v_bitop3_b32 v217, v217, v219, 1 bitop3:0xa8\n"
      "v_cmp_ne_u32 vcc, 0, v217\n"
      "s_cbranch_vccnz .Lst109\n"
      ".Lco110:\n"
      "s_waitcnt vmcnt(11)\n"
      "v_bitop3_b32 v217, v48, v49, s97 bitop3:0x7e\n"
      "v_bitop3_b32 v219, v50, v51, s97 bitop3:0x7e\n"
      "v_bitop3_b32 v176, v52, v53, s97 bitop3:0x7e\n"
      "v_or3_b32 v217, v217, v219, v176\n"
      "v_bitop3_b32 v219, v54, v55, s97 bitop3:0x7e\n"
      "v_bitop3_b32 v217, v217, v219, 1 bitop3:0xa8\n"
      "v_cmp_ne_u32 vcc, 0, v217\n"
      "s_cbranch_vccnz .Lst113\n"
      ".Lco114:\n"
      "s_waitcnt vmcnt(9)\n"
      "v_bitop3_b32 v217, v56, v57, s97 bitop3:0x7e\n"
      "v_bitop3_b32 v219, v58, v59, s97 bitop3:0x7e\n"
      "v_bitop3_b32 v176, v60, v61, s97 bitop3:0x7e\n"
      "v_or3_b32 v217, v217, v219, v176\n"
      "v_bitop3_b32 v219, v62, v63, s97 bitop3:0x7e\n"
      "v_bitop3_b32 v217, v217, v219, 1 bitop3:0xa8\n"
      "v_cmp_ne_u32 vcc, 0, v217\n"
      "s_cbranch_vccnz .Lst117\n"
      ".Lco118:\n"
      "s_waitcnt vmcnt(8)\n"
      "v_max3_u32 v178, v128, v129, v130\n"
      "s_waitcnt vmcnt(7)\n"
      "v_max3_u32 v178, v178, v131, v132\n"
      "v_max3_u32 v178, v178, v133, v134\n"
      "s_waitcnt vmcnt(6)\n"
      "v_max3_u32 v178, v178, v135, v136\n"
      "v_max3_u32 v178, v178, v137, v138\n"
      "s_waitcnt vmcnt(5)\n"
      "v_max3_u32 v178, v178, v139, v140\n"
      "v_max3_u32 v178, v178, v141, v142\n"
      "s_waitcnt vmcnt(4)\n"
      "v_max3_u32 v178, v178, v143, v144\n"
      "v_max3_u32 v178, v178, v145, v146\n"
      "s_waitcnt vmcnt(3)\n"
      "v_max3_u32 v178, v178, v147, v148\n"
      "v_max3_u32 v178, v178, v149, v150\n"
      "s_waitcnt vmcnt(2)\n"
      "v_max3_u32 v178, v178, v151, v152\n"
      "v_max3_u32 v178, v178, v153, v154\n"
      "s_waitcnt vmcnt(1)\n"
      "v_max3_u32 v178, v178, v155, v156\n"
      "v_max3_u32 v178, v178, v157, v158\n"
      "v_max_u32 v178, v178, v159\n"
      "v_cmp_eq_u32 vcc, 0xffbfffbf, v178\n"
      "s_cbranch_vccnz .Lst121\n"
      ".Lco122:\n"
      "s_cmp_lg_u32 s48, 0\n"
      "s_cbranch_scc1 .Lsk124\n"
      "s_mov_b32 s87, 0x1800\n"
      "buffer_load_dwordx4 v[160:163], v224, s[36:39], s87 offen sc1\n"
      "buffer_load_dwordx4 v[164:167], v224, s[36:39], s87 offen offset:1024 sc1\n"
      ".Lsk124:\n"
      "v_mfma_f32_32x32x16_bf16 v[64:79], v[128:131], v[0:3], 0\n"
      "v_mfma_f32_32x32x16_bf16 v[64:79], v[132:135], v[4:7], v[64:79]\n"
      "v_mfma_f32_32x32x16_bf16 v[80:95], v[136:139], v[8:11], 0\n"
      "v_mfma_f32_32x32x16_bf16 v[80:95], v[140:143], v[12:15], v[80:95]\n"
      "s_nop 9\n"
      "v_mov_b32 v178, v76\n"
      "v_mov_b32 v179, v76\n"
      "s_nop 1\n"
      "v_permlane32_swap_b32 v178, v179\n"
      "v_add_f32 v179, s94, v179\n"
      "v_rcp_f32 v178, v179\n"
      "s_nop 0\n"
      "v_cndmask_b32 v179, v178, 0, s[56:57]\n"
      "v_pk_mul_f32 v[64:65], v[64:65], v[178:179] op_sel_hi:[1,0]\n"
      "v_pk_mul_f32 v[66:67], v[66:67], v[178:179] op_sel_hi:[1,0]\n"
      "v_pk_mul_f32 v[68:69], v[68:69], v[178:179] op_sel_hi:[1,0]\n"
      "v_pk_mul_f32 v[70:71], v[70:71], v[178:179] op_sel_hi:[1,0]\n"
      "v_pk_mul_f32 v[72:73], v[72:73], v[178:179] op_sel_hi:[1,0]\n"
      "v_pk_mul_f32 v[74:75], v[74:75], v[178:179] op_sel_hi:[1,0]\n"
      "v_pk_mul_f32 v[76:77], v[76:77], v[178:179] op_sel:[0,1]\n"
      "v_pk_mul_f32 v[78:79], v[78:79], v[178:179] op_sel:[0,1]\n"
      "v_cvt_pk_bf16_f32 v0, v64, v65\n"
      "v_cvt_pk_bf16_f32 v1, v66, v67\n"
      "v_cvt_pk_bf16_f32 v2, v68, v69\n"
      "v_cvt_pk_bf16_f32 v3, v70, v71\n"
      "v_cvt_pk_bf16_f32 v4, v72, v73\n"
      "v_cvt_pk_bf16_f32 v5, v74, v75\n"
      "v_cvt_pk_bf16_f32 v6, v76, v77\n"
      "v_cvt_pk_bf16_f32 v7, v78, v79\n"
      "v_mfma_f32_32x32x16_bf16 v[96:111], a[224:227], v[0:3], 0\n"
      "v_mov_b32 v178, v92\n"
      "v_mov_b32 v179, v92\n"
      "s_nop 1\n"
      "v_permlane32_swap_b32 v178, v179\n"
      "v_add_f32 v179, s94, v179\n"
      "v_mfma_f32_32x32x16_bf16 v[112:127], a[228:231], v[0:3], 0\n"
      "v_rcp_f32 v178, v179\n"
      "s_nop 0\n"
      "v_cndmask_b32 v179, v178, 0, s[56:57]\n"
      "v_pk_mul_f32 v[80:81], v[80:81], v[178:179] op_sel_hi:[1,0]\n"
      "v_mfma_f32_32x32x16_bf16 v[64:79], v[144:147], v[16:19], 0\n"
      "s_add_u32 s34, s73, 0x400\n"
      "buffer_store_dwordx4 a[100:103], v224, s[40:43], s34 offen sc1\n"
      "v_pk_mul_f32 v[82:83], v[82:83], v[178:179] op_sel_hi:[1,0]\n"
      "v_pk_mul_f32 v[84:85], v[84:85], v[178:179] op_sel_hi:[1,0]\n"
      "v_pk_mul_f32 v[86:87], v[86:87], v[178:179] op_sel_hi:[1,0]\n"
      "v_pk_mul_f32 v[88:89], v[88:89], v[178:179] op_sel_hi:[1,0]\n"
      "v_mfma_f32_32x32x16_bf16 v[64:79], v[148:151], v[20:23], v[64:79]\n"
      "s_add_u32 m0, s75, 0x7000\n"
      "s_add_u32 s35, s71, 0x70000\n"
      "buffer_load_dwordx4 v224, s[36:39], s35 offen sc1 lds\n"
      "v_pk_mul_f32 v[90:91], v[90:91], v[178:179] op_sel_hi:[1,0]\n"
      "v_pk_mul_f32 v[92:93], v[92:93], v[178:179] op_sel:[0,1]\n"
      "v_pk_mul_f32 v[94:95], v[94:95], v[178:179] op_sel:[0,1]\n"
      "v_mfma_f32_32x32x16_bf16 v[96:111], a[232:235], v[4:7], v[96:111]\n"
      "v_cvt_pk_bf16_f32 v8, v80, v81\n"
      "v_cvt_pk_bf16_f32 v9, v82, v83\n"
      "v_cvt_pk_bf16_f32 v10, v84, v85\n"
      "v_cvt_pk_bf16_f32 v11, v86, v87\n"
      "v_mfma_f32_32x32x16_bf16 v[112:127], a[236:239], v[4:7], v[112:127]\n"
      "v_cvt_pk_bf16_f32 v12, v88, v89\n"
      "v_cvt_pk_bf16_f32 v13, v90, v91\n"
      "v_cvt_pk_bf16_f32 v14, v92, v93\n"
      "v_cvt_pk_bf16_f32 v15, v94, v95\n"
      "v_mfma_f32_32x32x16_bf16 v[96:111], a[240:243], v[8:11], v[96:111]\n"
      "buffer_load_dwordx4 v224, s[36:39], s35 offen offset:1024 sc1 lds\n"
      "v_mov_b32 v178, v76\n"
      "v_mov_b32 v179, v76\n"
      "s_nop 1\n"
      "v_permlane32_swap_b32 v178, v179\n"
      "v_add_f32 v179, s94, v179\n"
      "v_mfma_f32_32x32x16_bf16 v[112:127], a[244:247], v[8:11], v[112:127]\n"
      "v_rcp_f32 v178, v179\n"
      "s_nop 0\n"
      "v_cndmask_b32 v179, v178, 0, s[56:57]\n"
      "v_pk_mul_f32 v[64:65], v[64:65], v[178:179] op_sel_hi:[1,0]\n"
      "v_mfma_f32_32x32x16_bf16 v[80:95], v[152:155], v[24:27], 0\n"
      "s_add_u32 s34, s73, 0x800\n"
      "buffer_store_dwordx4 a[100:103], v224, s[40:43], s34 offen sc1\n"
      "v_pk_mul_f32 v[66:67], v[66:67], v[178:179] op_sel_hi:[1,0]\n"
      "v_pk_mul_f32 v[68:69], v[68:69], v[178:179] op_sel_hi:[1,0]\n"
      "v_pk_mul_f32 v[70:71], v[70:71], v[178:179] op_sel_hi:[1,0]\n"
      "v_pk_mul_f32 v[72:73], v[72:73], v[178:179] op_sel_hi:[1,0]\n"
      "v_mfma_f32_32x32x16_bf16 v[80:95], v[156:159], v[28:31], v[80:95]\n"
      "buffer_load_dwordx4 v224, s[36:39], s35 offen offset:2048 sc1 lds\n"
      "v_pk_mul_f32 v[74:75], v[74:75], v[178:179] op_sel_hi:[1,0]\n"
      "v_pk_mul_f32 v[76:77], v[76:77], v[178:179] op_sel:[0,1]\n"
      "v_pk_mul_f32 v[78:79], v[78:79], v[178:179] op_sel:[0,1]\n"
      "v_mfma_f32_32x32x16_bf16 v[96:111], a[248:251], v[12:15], v[96:111]\n"
      "v_cvt_pk_bf16_f32 v16, v64, v65\n"
      "v_cvt_pk_bf16_f32 v17, v66, v67\n"
      "v_cvt_pk_bf16_f32 v18, v68, v69\n"
      "v_cvt_pk_bf16_f32 v19, v70, v71\n"
      "v_mfma_f32_32x32x16_bf16 v[112:127], a[252:255], v[12:15], v[112:127]\n"
      "s_cmp_lg_u32 s48, 0\n"
      "s_cbranch_scc1 .Lsk125\n"
      "s_waitcnt vmcnt(6)\n"
      "v_cmp_ne_u64 vcc, s[54:55], v[160:161]\n"
      "v_cmp_ne_u64 s[86:87], s[54:55], v[162:163]\n"
      "s_or_b64 vcc, vcc, s[86:87]\n"
      "s_waitcnt vmcnt(5)\n"
      "v_cmp_ne_u64 s[86:87], s[54:55], v[164:165]\n"
      "s_or_b64 vcc, vcc, s[86:87]\n"
      "v_cmp_ne_u64 s[86:87], s[54:55], v[166:167]\n"
      "s_or_b64 vcc, vcc, s[86:87]\n"
      "s_cbranch_vccnz .Lfl83\n"
      ".Lne84:\n"
      ".Lsk125:\n"
      "v_cvt_pk_bf16_f32 v20, v72, v73\n"
      "v_cvt_pk_bf16_f32 v21, v74, v75\n"
      "v_cvt_pk_bf16_f32 v22, v76, v77\n"
      "v_cvt_pk_bf16_f32 v23, v78, v79\n"
      "v_mfma_f32_32x32x16_bf16 v[96:111], v[184:187], v[16:19], v[96:111]\n"
      "buffer_load_dwordx4 v224, s[36:39], s35 offen offset:3072 sc1 lds\n"
      "v_mov_b32 v178, v92\n"
      "v_mov_b32 v179, v92\n"
      "s_nop 1\n"
      "v_permlane32_swap_b32 v178, v179\n"
      "v_add_f32 v179, s94, v179\n"
      "v_mfma_f32_32x32x16_bf16 v[112:127], v[188:191], v[16:19], v[112:127]\n"
      "s_cmp_lg_u32 s48, 0\n"
      "s_cbranch_scc1 .Lsk126\n"
      "s_add_u32 s33, s6, 0\n"
      "buffer_load_dwordx4 a[0:3], v236, s[36:39], s33 offen sc1\n"
      "s_add_u32 s33, s6, 0\n"
      "buffer_load_dwordx4 a[4:7], v237, s[36:39], s33 offen sc1\n"
      ".Lsk126:\n"
      "v_rcp_f32 v178, v179\n"
      "s_nop 0\n"
      "v_cndmask_b32 v179, v178, 0, s[56:57]\n"
      "v_pk_mul_f32 v[80:81], v[80:81], v[178:179] op_sel_hi:[1,0]\n"
      "v_mfma_f32_32x32x16_bf16 v[160:175], v[128:131], v[32:35], 0\n"
      "s_add_u32 s34, s73, 0xc00\n"
      "buffer_store_dwordx4 a[100:103], v224, s[40:43], s34 offen sc1\n"
      "v_pk_mul_f32 v[82:83], v[82:83], v[178:179] op_sel_hi:[1,0]\n"
      "v_pk_mul_f32 v[84:85], v[84:85], v[178:179] op_sel_hi:[1,0]\n"
      "v_pk_mul_f32 v[86:87], v[86:87], v[178:179] op_sel_hi:[1,0]\n"
      "v_pk_mul_f32 v[88:89], v[88:89], v[178:179] op_sel_hi:[1,0]\n"
      "v_mfma_f32_32x32x16_bf16 v[160:175], v[132:135], v[36:39], v[160:175]\n"
      "s_add_u32 m0, s75, 0x8000\n"
      "s_add_u32 s35, s71, 0x71000\n"
      "buffer_load_dwordx4 v224, s[36:39], s35 offen sc1 lds\n"
      "v_pk_mul_f32 v[90:91], v[90:91], v[178:179] op_sel_hi:[1,0]\n"
      "v_pk_mul_f32 v[92:93], v[92:93], v[178:179] op_sel:[0,1]\n"
      "v_pk_mul_f32 v[94:95], v[94:95], v[178:179] op_sel:[0,1]\n"
      "v_mfma_f32_32x32x16_bf16 v[96:111], v[192:195], v[20:23], v[96:111]\n"
      "v_cvt_pk_bf16_f32 v24, v80, v81\n"
      "v_cvt_pk_bf16_f32 v25, v82, v83\n"
      "v_cvt_pk_bf16_f32 v26, v84, v85\n"
      "v_cvt_pk_bf16_f32 v27, v86, v87\n"
      "v_mfma_f32_32x32x16_bf16 v[112:127], v[196:199], v[20:23], v[112:127]\n"
      "v_cvt_pk_bf16_f32 v28, v88, v89\n"
      "v_cvt_pk_bf16_f32 v29, v90, v91\n"
      "v_cvt_pk_bf16_f32 v30, v92, v93\n"
      "v_cvt_pk_bf16_f32 v31, v94, v95\n"
      "v_mfma_f32_32x32x16_bf16 v[96:111], v[200:203], v[24:27], v[96:111]\n"
      "buffer_load_dwordx4 v224, s[36:39], s35 offen offset:1024 sc1 lds\n"
      "v_lshlrev_b32 v64, 16, v240\n"
      "v_and_b32 v65, 0xffff0000, v240\n"
      "v_lshlrev_b32 v66, 16, v241\n"
      "v_and_b32 v67, 0xffff0000, v241\n"
      "v_lshlrev_b32 v68, 16, v242\n"
      "v_and_b32 v69, 0xffff0000, v242\n"
      "v_lshlrev_b32 v70, 16, v243\n"
      "v_mfma_f32_32x32x16_bf16 v[112:127], v[204:207], v[24:27], v[112:127]\n"
      "v_and_b32 v71, 0xffff0000, v243\n"
      "v_lshlrev_b32 v72, 16, v244\n"
      "v_and_b32 v73, 0xffff0000, v244\n"
      "v_lshlrev_b32 v74, 16, v245\n"
      "v_and_b32 v75, 0xffff0000, v245\n"
      "v_lshlrev_b32 v76, 16, v246\n"
      "v_mfma_f32_32x32x16_bf16 v[0:15], v[136:139], v[40:43], 0\n"
      "s_add_u32 s34, s73, 0x1000\n"
      "buffer_store_dwordx4 a[100:103], v224, s[40:43], s34 offen sc1\n"
      "v_and_b32 v77, 0xffff0000, v246\n"
      "v_lshlrev_b32 v78, 16, v247\n"
      "v_and_b32 v79, 0xffff0000, v247\n"
      "v_mov_b32 v178, v172\n"
      "v_mov_b32 v179, v172\n"
      "s_nop 1\n"
      "v_permlane32_swap_b32 v178, v179\n"
      "v_add_f32 v179, s94, v179\n"
      "v_mfma_f32_32x32x16_bf16 v[0:15], v[140:143], v[44:47], v[0:15]\n"
      "buffer_load_dwordx4 v224, s[36:39], s35 offen offset:2048 sc1 lds\n"
      "v_rcp_f32 v178, v179\n"
      "s_nop 0\n"
      "v_cndmask_b32 v179, v178, 0, s[56:57]\n"
      "v_pk_mul_f32 v[160:161], v[160:161], v[178:179] op_sel_hi:[1,0]\n"
      "v_pk_mul_f32 v[162:163], v[162:163], v[178:179] op_sel_hi:[1,0]\n"
      "v_pk_mul_f32 v[164:165], v[164:165], v[178:179] op_sel_hi:[1,0]\n"
      "v_mfma_f32_32x32x16_bf16 v[96:111], v[208:211], v[28:31], v[96:111]\n"
      "v_pk_mul_f32 v[166:167], v[166:167], v[178:179] op_sel_hi:[1,0]\n"
      "v_pk_mul_f32 v[168:169], v[168:169], v[178:179] op_sel_hi:[1,0]\n"
      "v_pk_mul_f32 v[170:171], v[170:171], v[178:179] op_sel_hi:[1,0]\n"
      "v_pk_mul_f32 v[172:173], v[172:173], v[178:179] op_sel:[0,1]\n"
      "v_pk_mul_f32 v[174:175], v[174:175], v[178:179] op_sel:[0,1]\n"
      "v_cvt_pk_bf16_f32 v32, v160, v161\n"
      "v_cvt_pk_bf16_f32 v33, v162, v163\n"
      "v_mfma_f32_32x32x16_bf16 v[112:127], v[180:183], v[28:31], v[112:127]\n"
      "v_cvt_pk_bf16_f32 v34, v164, v165\n"
      "v_cvt_pk_bf16_f32 v35, v166, v167\n"
      "v_cvt_pk_bf16_f32 v36, v168, v169\n"
      "v_cvt_pk_bf16_f32 v37, v170, v171\n"
      "v_cvt_pk_bf16_f32 v38, v172, v173\n"
      "v_cvt_pk_bf16_f32 v39, v174, v175\n"
      "v_mfma_f32_32x32x16_bf16 v[64:79], a[224:227], v[32:35], v[64:79]\n"
      "s_add_u32 m0, s75, 0x1c000\n"
      "s_add_u32 s35, s71, 0xe0000\n"
      "buffer_load_dwordx4 v224, s[36:39], s35 offen sc1 lds\n"
      "v_mov_b32 v178, v12\n"
      "v_mov_b32 v179, v12\n"
      "s_nop 1\n"
      "v_permlane32_swap_b32 v178, v179\n"
      "v_add_f32 v179, s94, v179\n"
      "v_mfma_f32_32x32x16_bf16 v[80:95], a[228:231], v[32:35], 0\n"
      "ds_write_b128 v250, v[96:99] offset:57344\n"
      "s_cmp_lg_u32 s48, 0\n"
      "s_cbranch_scc1 .Lsk127\n"
      "s_add_u32 s33, s6, 0x800\n"
      "buffer_load_dwordx4 a[8:11], v236, s[36:39], s33 offen sc1\n"
      "s_add_u32 s33, s6, 0x800\n"
      "buffer_load_dwordx4 a[12:15], v237, s[36:39], s33 offen sc1\n"
      ".Lsk127:\n"
      "v_rcp_f32 v178, v179\n"
      "s_nop 0\n"
      "v_cndmask_b32 v179, v178, 0, s[56:57]\n"
      "v_pk_mul_f32 v[0:1], v[0:1], v[178:179] op_sel_hi:[1,0]\n"
      "v_mfma_f32_32x32x16_bf16 v[160:175], v[144:147], v[48:51], 0\n"
      "s_add_u32 s34, s73, 0x1400\n"
      "buffer_store_dwordx4 a[100:103], v224, s[40:43], s34 offen sc1\n"
      "v_pk_mul_f32 v[2:3], v[2:3], v[178:179] op_sel_hi:[1,0]\n"
      "v_pk_mul_f32 v[4:5], v[4:5], v[178:179] op_sel_hi:[1,0]\n"
      "v_pk_mul_f32 v[6:7], v[6:7], v[178:179] op_sel_hi:[1,0]\n"
      "v_pk_mul_f32 v[8:9], v[8:9], v[178:179] op_sel_hi:[1,0]\n"
      "v_mfma_f32_32x32x16_bf16 v[160:175], v[148:151], v[52:55], v[160:175]\n"
      "ds_write_b128 v250, v[100:103] offset:58368\n"
      "buffer_load_dwordx4 v224, s[36:39], s35 offen offset:1024 sc1 lds\n"
      "v_pk_mul_f32 v[10:11], v[10:11], v[178:179] op_sel_hi:[1,0]\n"
      "v_pk_mul_f32 v[12:13], v[12:13], v[178:179] op_sel:[0,1]\n"
      "v_pk_mul_f32 v[14:15], v[14:15], v[178:179] op_sel:[0,1]\n"
      "v_mfma_f32_32x32x16_bf16 v[64:79], a[232:235], v[36:39], v[64:79]\n"
      "v_cvt_pk_bf16_f32 v40, v0, v1\n"
      "v_cvt_pk_bf16_f32 v41, v2, v3\n"
      "v_cvt_pk_bf16_f32 v42, v4, v5\n"
      "v_cvt_pk_bf16_f32 v43, v6, v7\n"
      "v_mfma_f32_32x32x16_bf16 v[80:95], a[236:239], v[36:39], v[80:95]\n"
      "v_cvt_pk_bf16_f32 v44, v8, v9\n"
      "v_cvt_pk_bf16_f32 v45, v10, v11\n"
      "v_cvt_pk_bf16_f32 v46, v12, v13\n"
      "v_cvt_pk_bf16_f32 v47, v14, v15\n"
      "v_mfma_f32_32x32x16_bf16 v[64:79], a[240:243], v[40:43], v[64:79]\n"
      "buffer_load_dwordx4 v224, s[36:39], s35 offen offset:2048 sc1 lds\n"
      "v_mov_b32 v178, v172\n"
      "v_mov_b32 v179, v172\n"
      "s_nop 1\n"
      "v_permlane32_swap_b32 v178, v179\n"
      "v_add_f32 v179, s94, v179\n"
      "v_mfma_f32_32x32x16_bf16 v[80:95], a[244:247], v[40:43], v[80:95]\n"
      "ds_write_b128 v250, v[104:107] offset:59392\n"
      "v_rcp_f32 v178, v179\n"
      "s_nop 0\n"
      "v_cndmask_b32 v179, v178, 0, s[56:57]\n"
      "v_pk_mul_f32 v[160:161], v[160:161], v[178:179] op_sel_hi:[1,0]\n"
      "v_mfma_f32_32x32x16_bf16 v[0:15], v[152:155], v[56:59], 0\n"
      "s_add_u32 s34, s73, 0x1800\n"
      "buffer_store_dwordx4 a[100:103], v224, s[40:43], s34 offen sc1\n"
      "v_pk_mul_f32 v[162:163], v[162:163], v[178:179] op_sel_hi:[1,0]\n"
      "v_pk_mul_f32 v[164:165], v[164:165], v[178:179] op_sel_hi:[1,0]\n"
      "v_pk_mul_f32 v[166:167], v[166:167], v[178:179] op_sel_hi:[1,0]\n"
      "v_pk_mul_f32 v[168:169], v[168:169], v[178:179] op_sel_hi:[1,0]\n"
      "v_mfma_f32_32x32x16_bf16 v[0:15], v[156:159], v[60:63], v[0:15]\n"
      "ds_write_b128 v250, v[108:111] offset:60416\n"
      "buffer_load_dwordx4 v224, s[36:39], s35 offen offset:3072 sc1 lds\n"
      "v_pk_mul_f32 v[170:171], v[170:171], v[178:179] op_sel_hi:[1,0]\n"
      "v_pk_mul_f32 v[172:173], v[172:173], v[178:179] op_sel:[0,1]\n"
      "v_pk_mul_f32 v[174:175], v[174:175], v[178:179] op_sel:[0,1]\n"
      "v_mfma_f32_32x32x16_bf16 v[64:79], a[248:251], v[44:47], v[64:79]\n"
      "v_cvt_pk_bf16_f32 v48, v160, v161\n"
      "v_cvt_pk_bf16_f32 v49, v162, v163\n"
      "v_cvt_pk_bf16_f32 v50, v164, v165\n"
      "v_cvt_pk_bf16_f32 v51, v166, v167\n"
      "v_mfma_f32_32x32x16_bf16 v[80:95], a[252:255], v[44:47], v[80:95]\n"
      "v_cvt_pk_bf16_f32 v52, v168, v169\n"
      "v_cvt_pk_bf16_f32 v53, v170, v171\n"
      "v_cvt_pk_bf16_f32 v54, v172, v173\n"
      "v_cvt_pk_bf16_f32 v55, v174, v175\n"
      "v_mfma_f32_32x32x16_bf16 v[64:79], v[184:187], v[48:51], v[64:79]\n"
      "s_add_u32 m0, s75, 0x1d000\n"
      "s_add_u32 s35, s71, 0xe1000\n"
      "buffer_load_dwordx4 v224, s[36:39], s35 offen sc1 lds\n"
      "v_mov_b32 v178, v12\n"
      "v_mov_b32 v179, v12\n"
      "s_nop 1\n"
      "v_permlane32_swap_b32 v178, v179\n"
      "v_add_f32 v179, s94, v179\n"
      "v_rcp_f32 v178, v179\n"
      "v_mfma_f32_32x32x16_bf16 v[80:95], v[188:191], v[48:51], v[80:95]\n"
      "ds_write_b128 v251, v[112:115] offset:57344\n"
      "s_cmp_lg_u32 s48, 0\n"
      "s_cbranch_scc1 .Lsk128\n"
      "s_add_u32 s33, s6, 0x1000\n"
      "buffer_load_dwordx4 a[16:19], v236, s[36:39], s33 offen sc1\n"
      "s_add_u32 s33, s6, 0x1000\n"
      "buffer_load_dwordx4 a[20:23], v237, s[36:39], s33 offen sc1\n"
      ".Lsk128:\n"
      "v_cndmask_b32 v179, v178, 0, s[56:57]\n"
      "v_pk_mul_f32 v[0:1], v[0:1], v[178:179] op_sel_hi:[1,0]\n"
      "v_pk_mul_f32 v[2:3], v[2:3], v[178:179] op_sel_hi:[1,0]\n"
      "v_pk_mul_f32 v[4:5], v[4:5], v[178:179] op_sel_hi:[1,0]\n"
      "v_pk_mul_f32 v[6:7], v[6:7], v[178:179] op_sel_hi:[1,0]\n"
      "v_pk_mul_f32 v[8:9], v[8:9], v[178:179] op_sel_hi:[1,0]\n"
      "v_mfma_f32_32x32x16_bf16 v[64:79], v[192:195], v[52:55], v[64:79]\n"
      "v_pk_mul_f32 v[10:11], v[10:11], v[178:179] op_sel_hi:[1,0]\n"
      "v_pk_mul_f32 v[12:13], v[12:13], v[178:179] op_sel:[0,1]\n"
      "v_pk_mul_f32 v[14:15], v[14:15], v[178:179] op_sel:[0,1]\n"
      "v_cvt_pk_bf16_f32 v56, v0, v1\n"
      "v_cvt_pk_bf16_f32 v57, v2, v3\n"
      "v_mfma_f32_32x32x16_bf16 v[80:95], v[196:199], v[52:55], v[80:95]\n"
      "ds_write_b128 v251, v[116:119] offset:58368\n"
      "buffer_load_dwordx4 v224, s[36:39], s35 offen offset:1024 sc1 lds\n"
      "v_cvt_pk_bf16_f32 v58, v4, v5\n"
      "v_cvt_pk_bf16_f32 v59, v6, v7\n"
      "v_cvt_pk_bf16_f32 v60, v8, v9\n"
      "v_cvt_pk_bf16_f32 v61, v10, v11\n"
      "v_cvt_pk_bf16_f32 v62, v12, v13\n"
      "v_cvt_pk_bf16_f32 v63, v14, v15\n"
      "v_mfma_f32_32x32x16_bf16 v[80:95], v[204:207], v[56:59], v[80:95]\n"
      "buffer_load_dwordx4 v224, s[36:39], s35 offen offset:2048 sc1 lds\n"
      "v_mfma_f32_32x32x16_bf16 v[80:95], v[180:183], v[60:63], v[80:95]\n"
      "ds_write_b128 v251, v[120:123] offset:59392\n"
      "v_mfma_f32_32x32x16_bf16 v[64:79], v[200:203], v[56:59], v[64:79]\n"
      "v_mfma_f32_32x32x16_bf16 v[64:79], v[208:211], v[60:63], v[64:79]\n"
      "s_nop 8\n"
      "ds_write_b128 v249, v[80:83] offset:57344\n"
      "ds_write_b128 v249, v[84:87] offset:58368\n"
      "ds_write_b128 v249, v[88:91] offset:59392\n"
      "ds_write_b128 v249, v[92:95] offset:60416\n"
      "ds_write_b128 v251, v[124:127] offset:60416\n"
      "s_cmp_lg_u32 s48, 0\n"
      "s_cbranch_scc1 .Lsk129\n"
      "s_add_u32 s33, s6, 0x1800\n"
      "buffer_load_dwordx4 a[24:27], v236, s[36:39], s33 offen sc1\n"
      "s_add_u32 s33, s6, 0x1800\n"
      "buffer_load_dwordx4 a[28:31], v237, s[36:39], s33 offen sc1\n"
      "s_add_u32 s33, s6, 0x2000\n"
      "buffer_load_dwordx4 a[32:35], v236, s[36:39], s33 offen sc1\n"
      "s_add_u32 s33, s6, 0x2000\n"
      "buffer_load_dwordx4 a[36:39], v237, s[36:39], s33 offen sc1\n"
      ".Lsk129:\n"
      "s_mov_b32 s78, 0x20000\n"
      "s_mov_b32 s79, 0x3a0000\n"
      "s_mov_b32 s80, 0x7060302\n"
      "s_xor_b32 s81, s97, 1\n"
      "s_waitcnt vmcnt(4)\n"
      "s_waitcnt lgkmcnt(0)\n"
      "s_barrier\n"
      "ds_read_b128 v[16:19], v218 offset:4096\n"
      "ds_read_b128 v[20:23], v218 offset:5120\n"
      "ds_read_b128 v[24:27], v218 offset:6144\n"
      "ds_read_b128 v[28:31], v218 offset:7168\n"
      "ds_read_b128 v[32:35], v218 offset:8192\n"
      "ds_read_b128 v[36:39], v218 offset:9216\n"
      "ds_read_b128 v[40:43], v218 offset:10240\n"
      "ds_read_b128 v[44:47], v218 offset:11264\n"
      "ds_read_b128 v[0:3], v218\n"
      "ds_read_b128 v[4:7], v218 offset:1024\n"
      "ds_read_b128 v[8:11], v218 offset:2048\n"
      "ds_read_b128 v[12:15], v218 offset:3072\n"
      "s_waitcnt lgkmcnt(11)\n"
      "v_pk_add_f32 v[64:65], v[64:65], v[16:17]\n"
      "v_pk_add_f32 v[66:67], v[66:67], v[18:19]\n"
      "s_waitcnt lgkmcnt(10)\n"
      "v_pk_add_f32 v[68:69], v[68:69], v[20:21]\n"
      "v_pk_add_f32 v[70:71], v[70:71], v[22:23]\n"
      "s_waitcnt lgkmcnt(9)\n"
      "v_pk_add_f32 v[72:73], v[72:73], v[24:25]\n"
      "v_pk_add_f32 v[74:75], v[74:75], v[26:27]\n"
      "s_waitcnt lgkmcnt(8)\n"
      "v_pk_add_f32 v[76:77], v[76:77], v[28:29]\n"
      "v_pk_add_f32 v[78:79], v[78:79], v[30:31]\n"
      "s_waitcnt lgkmcnt(7)\n"
      "v_pk_add_f32 v[64:65], v[64:65], v[32:33]\n"
      "v_pk_add_f32 v[66:67], v[66:67], v[34:35]\n"
      "s_waitcnt lgkmcnt(6)\n"
      "v_pk_add_f32 v[68:69], v[68:69], v[36:37]\n"
      "v_pk_add_f32 v[70:71], v[70:71], v[38:39]\n"
      "s_waitcnt lgkmcnt(5)\n"
      "v_pk_add_f32 v[72:73], v[72:73], v[40:41]\n"
      "v_pk_add_f32 v[74:75], v[74:75], v[42:43]\n"
      "s_waitcnt lgkmcnt(4)\n"
      "v_pk_add_f32 v[76:77], v[76:77], v[44:45]\n"
      "v_pk_add_f32 v[78:79], v[78:79], v[46:47]\n"
      "s_waitcnt lgkmcnt(3)\n"
      "v_pk_add_f32 v[64:65], v[64:65], v[0:1]\n"
      "v_pk_add_f32 v[66:67], v[66:67], v[2:3]\n"
      "s_waitcnt lgkmcnt(2)\n"
      "v_pk_add_f32 v[68:69], v[68:69], v[4:5]\n"
      "v_pk_add_f32 v[70:71], v[70:71], v[6:7]\n"
      "s_waitcnt lgkmcnt(1)\n"
      "v_pk_add_f32 v[72:73], v[72:73], v[8:9]\n"
      "v_pk_add_f32 v[74:75], v[74:75], v[10:11]\n"
      "s_waitcnt lgkmcnt(0)\n"
      "v_pk_add_f32 v[76:77], v[76:77], v[12:13]\n"
      "v_pk_add_f32 v[78:79], v[78:79], v[14:15]\n"
      "v_cvt_pk_bf16_f32 v240, v64, v65\n"
      "v_cvt_pk_bf16_f32 v241, v66, v67\n"
      "v_cvt_pk_bf16_f32 v242, v68, v69\n"
      "v_cvt_pk_bf16_f32 v243, v70, v71\n"
      "v_cvt_pk_bf16_f32 v244, v72, v73\n"
      "v_cvt_pk_bf16_f32 v245, v74, v75\n"
      "v_cvt_pk_bf16_f32 v246, v76, v77\n"
      "v_cvt_pk_bf16_f32 v247, v78, v79\n"
      "v_perm_b32 v48, v240, v64, s80\n"
      "v_perm_b32 v49, v241, v66, s80\n"
      "v_perm_b32 v50, v242, v68, s80\n"
      "v_perm_b32 v51, v243, v70, s80\n"
      "v_add_u32 v48, s81, v48\n"
      "v_add_u32 v49, s81, v49\n"
      "v_add_u32 v50, s81, v50\n"
      "v_add_u32 v51, s81, v51\n"
      "v_and_or_b32 v48, v48, -2, s97\n"
      "v_and_or_b32 v49, v49, -2, s97\n"
      "v_and_or_b32 v50, v50, -2, s97\n"
      "v_and_or_b32 v51, v51, -2, s97\n"
      "s_nop 0\n"
      "v_permlane32_swap_b32 v48, v50\n"
      "v_permlane32_swap_b32 v49, v51\n"
      "buffer_store_dwordx4 v[48:51], v229, s[36:39], s78 offen sc1\n"
      "v_perm_b32 v52, v244, v72, s80\n"
      "v_perm_b32 v53, v245, v74, s80\n"
      "v_perm_b32 v54, v246, v76, s80\n"
      "v_perm_b32 v55, v247, v78, s80\n"
      "v_add_u32 v52, s81, v52\n"
      "v_add_u32 v53, s81, v53\n"
      "v_add_u32 v54, s81, v54\n"
      "v_add_u32 v55, s81, v55\n"
      "v_and_or_b32 v52, v52, -2, s97\n"
      "v_and_or_b32 v53, v53, -2, s97\n"
      "v_and_or_b32 v54, v54, -2, s97\n"
      "v_and_or_b32 v55, v55, -2, s97\n"
      "s_nop 0\n"
      "v_permlane32_swap_b32 v52, v54\n"
      "v_permlane32_swap_b32 v53, v55\n"
      "buffer_store_dwordx4 v[52:55], v230, s[36:39], s78 offen sc1\n"
      "s_memtime s[90:91]\n"
      "s_waitcnt lgkmcnt(0)\n"
      "s_mov_b32 s96, s90\n"
      "v_cndmask_b32 v76, v76, 0, s[60:61]\n"
      "v_cndmask_b32 v77, v77, 0, s[60:61]\n"
      "v_cndmask_b32 v78, v78, 0, s[60:61]\n"
      "v_cndmask_b32 v79, v79, 0, s[60:61]\n"
      "v_pk_add_f32 v[0:1], v[64:65], v[66:67]\n"
      "v_pk_mul_f32 v[2:3], v[64:65], v[64:65]\n"
      "v_pk_fma_f32 v[2:3], v[66:67], v[66:67], v[2:3]\n"
      "v_pk_add_f32 v[0:1], v[0:1], v[68:69]\n"
      "v_pk_fma_f32 v[2:3], v[68:69], v[68:69], v[2:3]\n"
      "v_pk_add_f32 v[0:1], v[0:1], v[70:71]\n"
      "v_pk_fma_f32 v[2:3], v[70:71], v[70:71], v[2:3]\n"
      "v_pk_add_f32 v[0:1], v[0:1], v[72:73]\n"
      "v_pk_fma_f32 v[2:3], v[72:73], v[72:73], v[2:3]\n"
      "v_pk_add_f32 v[0:1], v[0:1], v[74:75]\n"
      "v_pk_fma_f32 v[2:3], v[74:75], v[74:75], v[2:3]\n"
      "v_pk_add_f32 v[0:1], v[0:1], v[76:77]\n"
      "v_pk_fma_f32 v[2:3], v[76:77], v[76:77], v[2:3]\n"
      "v_pk_add_f32 v[0:1], v[0:1], v[78:79]\n"
      "v_pk_fma_f32 v[2:3], v[78:79], v[78:79], v[2:3]\n"
      "v_add_f32 v6, v0, v1\n"
      "v_add_f32 v7, v2, v3\n"
      "v_mov_b32 v4, v6\n"
      "v_mov_b32 v5, v7\n"
      "s_nop 0\n"
      "v_permlane32_swap_b32 v6, v4\n"
      "v_permlane32_swap_b32 v7, v5\n"
      "v_pk_add_f32 v[6:7], v[6:7], v[4:5]\n"
      "v_and_or_b32 v6, v6, -2, s97\n"
      "v_and_or_b32 v7, v7, -2, s97\n"
      "buffer_store_dwordx2 v[6:7], v231, s[36:39], s79 offen sc1\n"
      "s_barrier\n"
      "; G3\n"
      "ds_read_b128 a[224:227], v224\n"
      "ds_read_b128 a[228:231], v224 offset:28672\n"
      "ds_read_b128 a[232:235], v224 offset:1024\n"
      "ds_read_b128 a[236:239], v224 offset:29696\n"
      "ds_read_b128 a[240:243], v224 offset:2048\n"
      "ds_read_b128 a[244:247], v224 offset:30720\n"
      "ds_read_b128 a[248:251], v224 offset:3072\n"
      "ds_read_b128 a[252:255], v224 offset:31744\n"
      "ds_read_b128 v[160:163], v224 offset:4096\n"
      "ds_read_b128 v[164:167], v224 offset:32768\n"
      "s_add_u32 s78, s62, 0\n"
      "s_add_u32 s79, s62, 0x1000\n"
      "s_add_u32 s80, s62, 0x2000\n"
      "s_add_u32 s81, s62, 0x3000\n"
      "s_add_u32 s82, s62, 0x4000\n"
      "s_add_u32 s83, s62, 0x5000\n"
      "s_add_u32 s84, s62, 0x6000\n"
      "s_mov_b32 s85, 0x3a0000\n"
      "s_cmp_lg_u32 s48, 0\n"
      "s_cbranch_scc1 .Lsk130\n"
      "s_lshl_b32 s87, s51, 5\n"
      "s_add_u32 s87, s87, s50\n"
      "s_mul_i32 s87, s87, 0xe00\n"
      "s_add_u32 s87, s87, 0x15ea000\n"
      "v_lshrrev_b32 v112, 5, v238\n"
      "v_mul_u32_u24 v113, 0xe000, v112\n"
      "v_and_b32 v112, 31, v238\n"
      "v_lshl_add_u32 v113, v112, 2, v113\n"
      "buffer_load_dword v144, v113, s[36:39], s87 offen sc1\n"
      "buffer_load_dword v145, v113, s[36:39], s87 offen offset:128 sc1\n"
      "buffer_load_dword v146, v113, s[36:39], s87 offen offset:256 sc1\n"
      "buffer_load_dword v147, v113, s[36:39], s87 offen offset:384 sc1\n"
      "buffer_load_dword v148, v113, s[36:39], s87 offen offset:512 sc1\n"
      "buffer_load_dword v149, v113, s[36:39], s87 offen offset:640 sc1\n"
      "buffer_load_dword v150, v113, s[36:39], s87 offen offset:768 sc1\n"
      "buffer_load_dword v151, v113, s[36:39], s87 offen offset:896 sc1\n"
      "buffer_load_dword v152, v113, s[36:39], s87 offen offset:1024 sc1\n"
      "buffer_load_dword v153, v113, s[36:39], s87 offen offset:1152 sc1\n"
      "buffer_load_dword v154, v113, s[36:39], s87 offen offset:1280 sc1\n"
      "buffer_load_dword v155, v113, s[36:39], s87 offen offset:1408 sc1\n"
      "buffer_load_dword v156, v113, s[36:39], s87 offen offset:1536 sc1\n"
      "buffer_load_dword v157, v113, s[36:39], s87 offen offset:1664 sc1\n"
      "buffer_load_dword v158, v113, s[36:39], s87 offen offset:1792 sc1\n"
      "buffer_load_dword v159, v113, s[36:39], s87 offen offset:1920 sc1\n"
      "buffer_load_dword v200, v113, s[36:39], s87 offen offset:2048 sc1\n"
      "buffer_load_dword v201, v113, s[36:39], s87 offen offset:2176 sc1\n"
      "buffer_load_dword v202, v113, s[36:39], s87 offen offset:2304 sc1\n"
      "buffer_load_dword v203, v113, s[36:39], s87 offen offset:2432 sc1\n"
      "buffer_load_dword v204, v113, s[36:39], s87 offen offset:2560 sc1\n"
      "buffer_load_dword v205, v113, s[36:39], s87 offen offset:2688 sc1\n"
      "buffer_load_dword v206, v113, s[36:39], s87 offen offset:2816 sc1\n"
      "buffer_load_dword v207, v113, s[36:39], s87 offen offset:2944 sc1\n"
      "buffer_load_dword v208, v113, s[36:39], s87 offen offset:3072 sc1\n"
      "buffer_load_dword v209, v113, s[36:39], s87 offen offset:3200 sc1\n"
      "buffer_load_dword v210, v113, s[36:39], s87 offen offset:3328 sc1\n"
      "buffer_load_dword v219, v113, s[36:39], s87 offen offset:3456 sc1\n"
      "s_add_u32 s33, s6, 0x2800\n"
      "buffer_load_dwordx4 a[40:43], v236, s[36:39], s33 offen sc1\n"
      "s_add_u32 s33, s6, 0x2800\n"
      "buffer_load_dwordx4 a[44:47], v237, s[36:39], s33 offen sc1\n"
      "s_add_u32 s33, s6, 0x3000\n"
      "buffer_load_dwordx4 a[48:51], v236, s[36:39], s33 offen sc1\n"
      "s_add_u32 s33, s6, 0x3000\n"
      "buffer_load_dwordx4 a[52:55], v237, s[36:39], s33 offen sc1\n"
      "s_add_u32 s33, s7, 0\n"
      "buffer_load_dwordx4 a[56:59], v236, s[36:39], s33 offen sc1\n"
      "s_add_u32 s33, s7, 0\n"
      "buffer_load_dwordx4 a[60:63], v237, s[36:39], s33 offen sc1\n"
      "s_add_u32 s33, s7, 0x3000\n"
      "buffer_load_dwordx4 a[104:107], v236, s[36:39], s33 offen sc1\n"
      "s_add_u32 s33, s7, 0x3000\n"
      "buffer_load_dwordx4 a[108:111], v237, s[36:39], s33 offen sc1\n"
      ".Lsk130:\n"
      "v_and_b32 v216, 15, v238\n"
      "v_lshrrev_b32 v217, 1, v216\n"
      "v_mul_u32_u24 v217, 7, v217\n"
      "v_and_b32 v216, 1, v216\n"
      "v_lshl_add_u32 v216, v216, 2, v217\n"
      "v_add_u32 v216, 2, v216\n"
      "v_lshlrev_b32 v216, 9, v216\n"
      ".Ldl138:\n"
      "s_sleep 1\n"
      "s_memtime s[90:91]\n"
      "s_waitcnt lgkmcnt(0)\n"
      "s_sub_u32 s90, s90, s96\n"
      "s_cmp_lt_u32 s90, 0x1f4\n"
      "s_cbranch_scc1 .Ldl138\n"
      ".Lhd135:\n"
      "buffer_load_dword v217, v216, s[36:39], s78 offen offset:496 sc1\n"
      "buffer_load_dwordx4 v[0:3], v224, s[36:39], s78 offen sc1\n"
      "buffer_load_dwordx4 v[4:7], v224, s[36:39], s78 offen offset:1024 sc1\n"
      "buffer_load_dwordx4 v[8:11], v224, s[36:39], s78 offen offset:2048 sc1\n"
      "buffer_load_dwordx4 v[12:15], v224, s[36:39], s78 offen offset:3072 sc1\n"
      "buffer_load_dwordx4 v[16:19], v224, s[36:39], s79 offen sc1\n"
      "buffer_load_dwordx4 v[20:23], v224, s[36:39], s79 offen offset:1024 sc1\n"
      "buffer_load_dwordx4 v[24:27], v224, s[36:39], s79 offen offset:2048 sc1\n"
      "buffer_load_dwordx4 v[28:31], v224, s[36:39], s79 offen offset:3072 sc1\n"
      "s_waitcnt vmcnt(8)\n"
      "v_bitop3_b32 v217, v217, s97, 1 bitop3:0x28\n"
      "v_cmp_ne_u32 vcc, 0, v217\n"
      "s_cbranch_vccz .Lco137\n"
      "s_sub_u32 s77, s77, 1\n"
      "s_cmp_gt_i32 s77, 0\n"
      "s_cbranch_scc1 .Lhd135\n"
      ".Lco137:\n"
      "s_waitcnt vmcnt(6)\n"
      "v_bitop3_b32 v217, v0, v1, s97 bitop3:0x7e\n"
      "v_bitop3_b32 v254, v2, v3, s97 bitop3:0x7e\n"
      "v_bitop3_b32 v211, v4, v5, s97 bitop3:0x7e\n"
      "v_or3_b32 v217, v217, v254, v211\n"
      "v_bitop3_b32 v254, v6, v7, s97 bitop3:0x7e\n"
      "v_bitop3_b32 v217, v217, v254, 1 bitop3:0xa8\n"
      "v_cmp_ne_u32 vcc, 0, v217\n"
      "s_cbranch_vccnz .Lst139\n"
      ".Lco140:\n"
      "v_mfma_f32_32x32x16_bf16 v[128:143], a[224:227], v[0:3], 0\n"
      "ds_read_b128 v[168:171], v224 offset:5120\n"
      "buffer_load_dwordx4 v[32:35], v224, s[36:39], s80 offen sc1\n"
      "s_waitcnt vmcnt(5)\n"
      "v_bitop3_b32 v217, v8, v9, s97 bitop3:0x7e\n"
      "v_bitop3_b32 v254, v10, v11, s97 bitop3:0x7e\n"
      "v_bitop3_b32 v211, v12, v13, s97 bitop3:0x7e\n"
      "v_mfma_f32_32x32x16_bf16 v[112:127], a[228:231], v[0:3], 0\n"
      "ds_read_b128 v[172:175], v224 offset:33792\n"
      "buffer_load_dwordx4 v[36:39], v224, s[36:39], s80 offen offset:1024 sc1\n"
      "v_or3_b32 v217, v217, v254, v211\n"
      "v_bitop3_b32 v254, v14, v15, s97 bitop3:0x7e\n"
      "v_bitop3_b32 v217, v217, v254, 1 bitop3:0xa8\n"
      "v_cmp_ne_u32 vcc, 0, v217\n"
      "v_mfma_f32_32x32x16_bf16 v[128:143], a[232:235], v[4:7], v[128:143]\n"
      "ds_read_b128 v[176:179], v224 offset:6144\n"
      "buffer_load_dwordx4 v[40:43], v224, s[36:39], s80 offen offset:2048 sc1\n"
      "s_cbranch_vccnz .Lst143\n"
      ".Lco144:\n"
      "v_mfma_f32_32x32x16_bf16 v[112:127], a[236:239], v[4:7], v[112:127]\n"
      "ds_read_b128 v[180:183], v224 offset:34816\n"
      "buffer_load_dwordx4 v[44:47], v224, s[36:39], s80 offen offset:3072 sc1\n"
      "v_mfma_f32_32x32x16_bf16 v[128:143], a[240:243], v[8:11], v[128:143]\n"
      "ds_read_b128 a[224:227], v224 offset:7168\n"
      "buffer_load_dwordx4 v[184:187], v232, s[36:39], s85 offen sc1\n"
      "s_waitcnt vmcnt(7)\n"
      "v_bitop3_b32 v217, v16, v17, s97 bitop3:0x7e\n"
      "v_bitop3_b32 v254, v18, v19, s97 bitop3:0x7e\n"
      "v_bitop3_b32 v211, v20, v21, s97 bitop3:0x7e\n"
      "v_mfma_f32_32x32x16_bf16 v[112:127], a[244:247], v[8:11], v[112:127]\n"
      "ds_read_b128 a[228:231], v224 offset:35840\n"
      "buffer_load_dwordx4 v[188:191], v232, s[36:39], s85 offen offset:512 sc1\n"
      "v_or3_b32 v217, v217, v254, v211\n"
      "v_bitop3_b32 v254, v22, v23, s97 bitop3:0x7e\n"
      "v_bitop3_b32 v217, v217, v254, 1 bitop3:0xa8\n"
      "v_cmp_ne_u32 vcc, 0, v217\n"
      "v_mfma_f32_32x32x16_bf16 v[128:143], a[248:251], v[12:15], v[128:143]\n"
      "ds_read_b128 a[232:235], v224 offset:8192\n"
      "buffer_load_dwordx4 v[192:195], v232, s[36:39], s85 offen offset:1024 sc1\n"
      "s_cbranch_vccnz .Lst147\n"
      ".Lco148:\n"
      "v_mfma_f32_32x32x16_bf16 v[112:127], a[252:255], v[12:15], v[112:127]\n"
      "ds_read_b128 a[236:239], v224 offset:36864\n"
      "buffer_load_dwordx4 v[196:199], v232, s[36:39], s85 offen offset:1536 sc1\n"
      "v_mfma_f32_32x32x16_bf16 v[128:143], v[160:163], v[16:19], v[128:143]\n"
      "ds_read_b128 a[240:243], v224 offset:9216\n"
      "buffer_load_dwordx4 v[48:51], v224, s[36:39], s81 offen sc1\n"
      "s_waitcnt vmcnt(9)\n"
      "v_bitop3_b32 v217, v24, v25, s97 bitop3:0x7e\n"
      "v_bitop3_b32 v254, v26, v27, s97 bitop3:0x7e\n"
      "v_bitop3_b32 v211, v28, v29, s97 bitop3:0x7e\n"
      "v_mfma_f32_32x32x16_bf16 v[112:127], v[164:167], v[16:19], v[112:127]\n"
      "ds_read_b128 a[244:247], v224 offset:37888\n"
      "buffer_load_dwordx4 v[52:55], v224, s[36:39], s81 offen offset:1024 sc1\n"
      "v_or3_b32 v217, v217, v254, v211\n"
      "v_bitop3_b32 v254, v30, v31, s97 bitop3:0x7e\n"
      "v_bitop3_b32 v217, v217, v254, 1 bitop3:0xa8\n"
      "v_cmp_ne_u32 vcc, 0, v217\n"
      "s_waitcnt lgkmcnt(8)\n"
      "v_mfma_f32_32x32x16_bf16 v[128:143], v[168:171], v[20:23], v[128:143]\n"
      "ds_read_b128 a[248:251], v224 offset:10240\n"
      "buffer_load_dwordx4 v[56:59], v224, s[36:39], s81 offen offset:2048 sc1\n"
      "s_cbranch_vccnz .Lst151\n"
      ".Lco152:\n"
      "v_mfma_f32_32x32x16_bf16 v[112:127], v[172:175], v[20:23], v[112:127]\n"
      "ds_read_b128 a[252:255], v224 offset:38912\n"
      "buffer_load_dwordx4 v[60:63], v224, s[36:39], s81 offen offset:3072 sc1\n"
      "s_waitcnt lgkmcnt(8)\n"
      "v_mfma_f32_32x32x16_bf16 v[128:143], v[176:179], v[24:27], v[128:143]\n"
      "ds_read_b128 v[160:163], v224 offset:11264\n"
      "s_waitcnt vmcnt(10)\n"
      "v_bitop3_b32 v217, v32, v33, s97 bitop3:0x7e\n"
      "v_bitop3_b32 v254, v34, v35, s97 bitop3:0x7e\n"
      "v_bitop3_b32 v211, v36, v37, s97 bitop3:0x7e\n"
      "v_mfma_f32_32x32x16_bf16 v[112:127], v[180:183], v[24:27], v[112:127]\n"
      "ds_read_b128 v[164:167], v224 offset:39936\n"
      "buffer_load_dwordx4 v[64:67], v224, s[36:39], s82 offen sc1\n"
      "v_or3_b32 v217, v217, v254, v211\n"
      "v_bitop3_b32 v254, v38, v39, s97 bitop3:0x7e\n"
      "v_bitop3_b32 v217, v217, v254, 1 bitop3:0xa8\n"
      "v_cmp_ne_u32 vcc, 0, v217\n"
      "s_waitcnt lgkmcnt(8)\n"
      "v_mfma_f32_32x32x16_bf16 v[128:143], a[224:227], v[28:31], v[128:143]\n"
      "ds_read_b128 v[168:171], v224 offset:12288\n"
      "s_cbranch_vccnz .Lst155\n"
      ".Lco156:\n"
      "v_mfma_f32_32x32x16_bf16 v[112:127], a[228:231], v[28:31], v[112:127]\n"
      "ds_read_b128 v[172:175], v224 offset:40960\n"
      "buffer_load_dwordx4 v[68:71], v224, s[36:39], s82 offen offset:1024 sc1\n"
      "s_waitcnt lgkmcnt(8)\n"
      "v_mfma_f32_32x32x16_bf16 v[128:143], a[232:235], v[32:35], v[128:143]\n"
      "ds_read_b128 v[176:179], v224 offset:13312\n"
      "s_waitcnt vmcnt(10)\n"
      "v_bitop3_b32 v217, v40, v41, s97 bitop3:0x7e\n"
      "v_bitop3_b32 v254, v42, v43, s97 bitop3:0x7e\n"
      "v_bitop3_b32 v211, v44, v45, s97 bitop3:0x7e\n"
      "v_mfma_f32_32x32x16_bf16 v[112:127], a[236:239], v[32:35], v[112:127]\n"
      "ds_read_b128 v[180:183], v224 offset:41984\n"
      "buffer_load_dwordx4 v[72:75], v224, s[36:39], s82 offen offset:2048 sc1\n"
      "v_or3_b32 v217, v217, v254, v211\n"
      "v_bitop3_b32 v254, v46, v47, s97 bitop3:0x7e\n"
      "v_bitop3_b32 v217, v217, v254, 1 bitop3:0xa8\n"
      "v_cmp_ne_u32 vcc, 0, v217\n"
      "s_waitcnt lgkmcnt(8)\n"
      "v_mfma_f32_32x32x16_bf16 v[128:143], a[240:243], v[36:39], v[128:143]\n"
      "ds_read_b128 a[224:227], v224 offset:14336\n"
      "s_cbranch_vccnz .Lst159\n"
      ".Lco160:\n"
      "v_mfma_f32_32x32x16_bf16 v[112:127], a[244:247], v[36:39], v[112:127]\n"
      "ds_read_b128 a[228:231], v224 offset:43008\n"
      "buffer_load_dwordx4 v[76:79], v224, s[36:39], s82 offen offset:3072 sc1\n"
      "s_waitcnt lgkmcnt(8)\n"
      "v_mfma_f32_32x32x16_bf16 v[128:143], a[248:251], v[40:43], v[128:143]\n"
      "ds_read_b128 a[232:235], v224 offset:15360\n"
      "s_waitcnt vmcnt(6)\n"
      "v_bitop3_b32 v217, v48, v49, s97 bitop3:0x7e\n"
      "v_bitop3_b32 v254, v50, v51, s97 bitop3:0x7e\n"
      "v_bitop3_b32 v211, v52, v53, s97 bitop3:0x7e\n"
      "v_mfma_f32_32x32x16_bf16 v[112:127], a[252:255], v[40:43], v[112:127]\n"
      "ds_read_b128 a[236:239], v224 offset:44032\n"
      "buffer_load_dwordx4 v[80:83], v224, s[36:39], s83 offen sc1\n"
      "v_or3_b32 v217, v217, v254, v211\n"
      "v_bitop3_b32 v254, v54, v55, s97 bitop3:0x7e\n"
      "v_bitop3_b32 v217, v217, v254, 1 bitop3:0xa8\n"
      "v_cmp_ne_u32 vcc, 0, v217\n"
      "s_waitcnt lgkmcnt(8)\n"
      "v_mfma_f32_32x32x16_bf16 v[128:143], v[160:163], v[44:47], v[128:143]\n"
      "ds_read_b128 a[240:243], v224 offset:16384\n"
      "s_cbranch_vccnz .Lst164\n"
      ".Lco165:\n"
      "v_mfma_f32_32x32x16_bf16 v[112:127], v[164:167], v[44:47], v[112:127]\n"
      "ds_read_b128 a[244:247], v224 offset:45056\n"
      "buffer_load_dwordx4 v[84:87], v224, s[36:39], s83 offen offset:1024 sc1\n"
      "s_cmp_lg_u32 s48, 0\n"
      "s_cbranch_scc1 .Lsk163\n"
      "v_pk_add_f32 v[144:145], v[144:145], v[146:147]\n"
      "v_pk_add_f32 v[144:145], v[144:145], v[148:149]\n"
      "v_pk_add_f32 v[144:145], v[144:145], v[150:151]\n"
      "v_pk_add_f32 v[144:145], v[144:145], v[152:153]\n"
      "v_pk_add_f32 v[144:145], v[144:145], v[154:155]\n"
      "v_pk_add_f32 v[144:145], v[144:145], v[156:157]\n"
      "v_pk_add_f32 v[144:145], v[144:145], v[158:159]\n"
      "v_pk_add_f32 v[144:145], v[144:145], v[200:201]\n"
      "v_pk_add_f32 v[144:145], v[144:145], v[202:203]\n"
      "v_pk_add_f32 v[144:145], v[144:145], v[204:205]\n"
      "v_pk_add_f32 v[144:145], v[144:145], v[206:207]\n"
      "v_pk_add_f32 v[144:145], v[144:145], v[208:209]\n"
      "v_add_f32 v144, v144, v210\n"
      "v_add_f32 v145, v145, v219\n"
      "v_add_f32 v144, v144, v145\n"
      "v_cvt_pk_bf16_f32 v146, v144, v144\n"
      "v_and_b32 v145, 0xffff0000, v146\n"
      "v_sub_f32 v145, v144, v145\n"
      "v_cvt_pk_bf16_f32 v147, v145, 0\n"
      "s_lshl_b32 s87, s51, 9\n"
      "s_add_u32 s87, s87, 0x27200\n"
      "v_lshlrev_b32 v148, 3, v238\n"
      "v_add_u32 v148, s87, v148\n"
      "ds_write_b64 v148, v[146:147]\n"
      "s_waitcnt lgkmcnt(0)\n"
      ".Lsk163:\n"
      "s_waitcnt lgkmcnt(8)\n"
      "v_mfma_f32_32x32x16_bf16 v[128:143], v[168:171], v[48:51], v[128:143]\n"
      "ds_read_b128 a[248:251], v224 offset:17408\n"
      "s_waitcnt vmcnt(6)\n"
      "v_bitop3_b32 v217, v56, v57, s97 bitop3:0x7e\n"
      "v_bitop3_b32 v254, v58, v59, s97 bitop3:0x7e\n"
      "v_bitop3_b32 v211, v60, v61, s97 bitop3:0x7e\n"
      "v_mfma_f32_32x32x16_bf16 v[112:127], v[172:175], v[48:51], v[112:127]\n"
      "ds_read_b128 a[252:255], v224 offset:46080\n"
      "buffer_load_dwordx4 v[88:91], v224, s[36:39], s83 offen offset:2048 sc1\n"
      "v_or3_b32 v217, v217, v254, v211\n"
      "v_bitop3_b32 v254, v62, v63, s97 bitop3:0x7e\n"
      "v_bitop3_b32 v217, v217, v254, 1 bitop3:0xa8\n"
      "v_cmp_ne_u32 vcc, 0, v217\n"
      "s_waitcnt lgkmcnt(8)\n"
      "v_mfma_f32_32x32x16_bf16 v[128:143], v[176:179], v[52:55], v[128:143]\n"
      "ds_read_b128 v[160:163], v224 offset:18432\n"
      "s_cbranch_vccnz .Lst168\n"
      ".Lco169:\n"
      "v_mfma_f32_32x32x16_bf16 v[112:127], v[180:183], v[52:55], v[112:127]\n"
      "ds_read_b128 v[164:167], v224 offset:47104\n"
      "buffer_load_dwordx4 v[92:95], v224, s[36:39], s83 offen offset:3072 sc1\n"
      "s_waitcnt lgkmcnt(8)\n"
      "v_mfma_f32_32x32x16_bf16 v[128:143], a[224:227], v[56:59], v[128:143]\n"
      "ds_read_b128 v[168:171], v224 offset:19456\n"
      "s_waitcnt vmcnt(6)\n"
      "v_bitop3_b32 v217, v64, v65, s97 bitop3:0x7e\n"
      "v_bitop3_b32 v254, v66, v67, s97 bitop3:0x7e\n"
      "v_bitop3_b32 v211, v68, v69, s97 bitop3:0x7e\n"
      "v_mfma_f32_32x32x16_bf16 v[112:127], a[228:231], v[56:59], v[112:127]\n"
      "ds_read_b128 v[172:175], v224 offset:48128\n"
      "buffer_load_dwordx4 v[96:99], v224, s[36:39], s84 offen sc1\n"
      "v_or3_b32 v217, v217, v254, v211\n"
      "v_bitop3_b32 v254, v70, v71, s97 bitop3:0x7e\n"
      "v_bitop3_b32 v217, v217, v254, 1 bitop3:0xa8\n"
      "v_cmp_ne_u32 vcc, 0, v217\n"
      "s_waitcnt lgkmcnt(8)\n"
      "v_mfma_f32_32x32x16_bf16 v[128:143], a[232:235], v[60:63], v[128:143]\n"
      "ds_read_b128 v[176:179], v224 offset:20480\n"
      "s_cbranch_vccnz .Lst172\n"
      ".Lco173:\n"
      "v_mfma_f32_32x32x16_bf16 v[112:127], a[236:239], v[60:63], v[112:127]\n"
      "ds_read_b128 v[180:183], v224 offset:49152\n"
      "buffer_load_dwordx4 v[100:103], v224, s[36:39], s84 offen offset:1024 sc1\n"
      "v_bitop3_b32 v200, v184, v185, s97 bitop3:0x7e\n"
      "v_bitop3_b32 v201, v186, v187, s97 bitop3:0x7e\n"
      "v_bitop3_b32 v202, v188, v189, s97 bitop3:0x7e\n"
      "v_or3_b32 v200, v200, v201, v202\n"
      "v_bitop3_b32 v201, v190, v191, s97 bitop3:0x7e\n"
      "v_bitop3_b32 v202, v192, v193, s97 bitop3:0x7e\n"
      "v_or3_b32 v200, v200, v201, v202\n"
      "v_bitop3_b32 v201, v194, v195, s97 bitop3:0x7e\n"
      "v_bitop3_b32 v202, v196, v197, s97 bitop3:0x7e\n"
      "v_or3_b32 v200, v200, v201, v202\n"
      "v_bitop3_b32 v201, v198, v199, s97 bitop3:0x7e\n"
      "v_bitop3_b32 v200, v200, v201, 1 bitop3:0xa8\n"
      "v_cmp_ne_u32 vcc, 0, v200\n"
      "s_cbranch_vccnz .Lst131\n"
      ".Lco132:\n"
      "s_waitcnt lgkmcnt(8)\n"
      "v_mfma_f32_32x32x16_bf16 v[128:143], a[240:243], v[64:67], v[128:143]\n"
      "ds_read_b128 a[224:227], v224 offset:21504\n"
      "s_waitcnt vmcnt(6)\n"
      "v_bitop3_b32 v217, v72, v73, s97 bitop3:0x7e\n"
      "v_bitop3_b32 v254, v74, v75, s97 bitop3:0x7e\n"
      "v_bitop3_b32 v211, v76, v77, s97 bitop3:0x7e\n"
      "v_mfma_f32_32x32x16_bf16 v[112:127], a[244:247], v[64:67], v[112:127]\n"
      "ds_read_b128 a[228:231], v224 offset:50176\n"
      "buffer_load_dwordx4 v[104:107], v224, s[36:39], s84 offen offset:2048 sc1\n"
      "v_or3_b32 v217, v217, v254, v211\n"
      "v_bitop3_b32 v254, v78, v79, s97 bitop3:0x7e\n"
      "v_bitop3_b32 v217, v217, v254, 1 bitop3:0xa8\n"
      "v_cmp_ne_u32 vcc, 0, v217\n"
      "s_waitcnt lgkmcnt(8)\n"
      "v_mfma_f32_32x32x16_bf16 v[128:143], a[248:251], v[68:71], v[128:143]\n"
      "ds_read_b128 a[232:235], v224 offset:22528\n"
      "s_cbranch_vccnz .Lst176\n"
      ".Lco177:\n"
      "v_mfma_f32_32x32x16_bf16 v[112:127], a[252:255], v[68:71], v[112:127]\n"
      "ds_read_b128 a[236:239], v224 offset:51200\n"
      "buffer_load_dwordx4 v[108:111], v224, s[36:39], s84 offen offset:3072 sc1\n"
      "s_barrier\n"
      "v_pk_add_f32 v[184:185], v[184:185], v[186:187]\n"
      "v_pk_add_f32 v[188:189], v[188:189], v[190:191]\n"
      "v_pk_add_f32 v[192:193], v[192:193], v[194:195]\n"
      "v_pk_add_f32 v[196:197], v[196:197], v[198:199]\n"
      "v_pk_add_f32 v[184:185], v[184:185], v[188:189]\n"
      "v_pk_add_f32 v[192:193], v[192:193], v[196:197]\n"
      "v_pk_add_f32 v[200:201], v[184:185], v[192:193]\n"
      "v_mov_b32 v202, v200\n"
      "v_mov_b32 v203, v201\n"
      "s_waitcnt lgkmcnt(8)\n"
      "v_mfma_f32_32x32x16_bf16 v[128:143], v[160:163], v[72:75], v[128:143]\n"
      "ds_read_b128 a[240:243], v224 offset:23552\n"
      "s_waitcnt vmcnt(6)\n"
      "v_bitop3_b32 v217, v80, v81, s97 bitop3:0x7e\n"
      "v_bitop3_b32 v254, v82, v83, s97 bitop3:0x7e\n"
      "v_bitop3_b32 v211, v84, v85, s97 bitop3:0x7e\n"
      "v_mfma_f32_32x32x16_bf16 v[112:127], v[164:167], v[72:75], v[112:127]\n"
      "ds_read_b128 a[244:247], v224 offset:52224\n"
      "s_add_u32 m0, s75, 0xe000\n"
      "s_add_u32 s35, s71, 0x150000\n"
      "buffer_load_dwordx4 v224, s[36:39], s35 offen sc1 lds\n"
      "v_or3_b32 v217, v217, v254, v211\n"
      "v_bitop3_b32 v254, v86, v87, s97 bitop3:0x7e\n"
      "v_bitop3_b32 v217, v217, v254, 1 bitop3:0xa8\n"
      "v_cmp_ne_u32 vcc, 0, v217\n"
      "s_waitcnt lgkmcnt(8)\n"
      "v_mfma_f32_32x32x16_bf16 v[128:143], v[168:171], v[76:79], v[128:143]\n"
      "ds_read_b128 a[248:251], v224 offset:24576\n"
      "s_cbranch_vccnz .Lst180\n"
      ".Lco181:\n"
      "v_mfma_f32_32x32x16_bf16 v[112:127], v[172:175], v[76:79], v[112:127]\n"
      "ds_read_b128 a[252:255], v224 offset:53248\n"
      "buffer_load_dwordx4 v224, s[36:39], s35 offen offset:1024 sc1 lds\n"
      "v_permlane32_swap_b32 v200, v202\n"
      "v_permlane32_swap_b32 v201, v203\n"
      "v_pk_add_f32 v[200:201], v[200:201], v[202:203]\n"
      "v_mul_f32 v200, 0xbb124925, v200\n"
      "v_mul_f32 v201, 0x3b124925, v201\n"
      "v_fma_f32 v201, -v200, v200, v201\n"
      "v_max_f32 v201, 0, v201\n"
      "v_add_f32 v219, 0x3727c5ac, v201\n"
      "v_rsq_f32 v218, v219\n"
      "s_waitcnt lgkmcnt(8)\n"
      "v_mfma_f32_32x32x16_bf16 v[128:143], v[176:179], v[80:83], v[128:143]\n"
      "ds_read_b128 v[160:163], v224 offset:25600\n"
      "s_waitcnt vmcnt(6)\n"
      "v_bitop3_b32 v217, v88, v89, s97 bitop3:0x7e\n"
      "v_bitop3_b32 v254, v90, v91, s97 bitop3:0x7e\n"
      "v_bitop3_b32 v211, v92, v93, s97 bitop3:0x7e\n"
      "v_mfma_f32_32x32x16_bf16 v[112:127], v[180:183], v[80:83], v[112:127]\n"
      "ds_read_b128 v[164:167], v224 offset:54272\n"
      "buffer_load_dwordx4 v224, s[36:39], s35 offen offset:2048 sc1 lds\n"
      "v_or3_b32 v217, v217, v254, v211\n"
      "v_bitop3_b32 v254, v94, v95, s97 bitop3:0x7e\n"
      "v_bitop3_b32 v217, v217, v254, 1 bitop3:0xa8\n"
      "v_cmp_ne_u32 vcc, 0, v217\n"
      "s_waitcnt lgkmcnt(8)\n"
      "v_mfma_f32_32x32x16_bf16 v[128:143], a[224:227], v[84:87], v[128:143]\n"
      "ds_read_b128 v[168:171], v224 offset:26624\n"
      "s_cbranch_vccnz .Lst184\n"
      ".Lco185:\n"
      "v_mfma_f32_32x32x16_bf16 v[112:127], a[228:231], v[84:87], v[112:127]\n"
      "ds_read_b128 v[172:175], v224 offset:55296\n"
      "buffer_load_dwordx4 v224, s[36:39], s35 offen offset:3072 sc1 lds\n"
      "v_cvt_pk_bf16_f32 v220, v200, v200\n"
      "v_and_b32 v202, 0xffff0000, v220\n"
      "v_sub_f32 v202, v200, v202\n"
      "v_and_b32 v221, 0xffff, v220\n"
      "v_cvt_pk_bf16_f32 v220, v200, v202\n"
      "v_cndmask_b32 v220, v220, 0, s[56:57]\n"
      "v_cndmask_b32 v221, v221, 0, s[56:57]\n"
      "v_mul_f32 v218, 0xbfb8aa3b, v218\n"
      "ds_read_b64 v[212:213], v235\n"
      "s_waitcnt lgkmcnt(9)\n"
      "v_mfma_f32_32x32x16_bf16 v[128:143], a[232:235], v[88:91], v[128:143]\n"
      "ds_read_b128 v[176:179], v224 offset:27648\n"
      "s_waitcnt vmcnt(6)\n"
      "v_bitop3_b32 v217, v96, v97, s97 bitop3:0x7e\n"
      "v_bitop3_b32 v254, v98, v99, s97 bitop3:0x7e\n"
      "v_bitop3_b32 v211, v100, v101, s97 bitop3:0x7e\n"
      "v_mfma_f32_32x32x16_bf16 v[112:127], a[236:239], v[88:91], v[112:127]\n"
      "ds_read_b128 v[180:183], v224 offset:56320\n"
      "s_add_u32 m0, s75, 0xf000\n"
      "s_add_u32 s35, s71, 0x151000\n"
      "buffer_load_dwordx4 v224, s[36:39], s35 offen sc1 lds\n"
      "v_or3_b32 v217, v217, v254, v211\n"
      "v_bitop3_b32 v254, v102, v103, s97 bitop3:0x7e\n"
      "v_bitop3_b32 v217, v217, v254, 1 bitop3:0xa8\n"
      "v_cmp_ne_u32 vcc, 0, v217\n"
      "s_waitcnt lgkmcnt(9)\n"
      "v_mfma_f32_32x32x16_bf16 v[128:143], a[240:243], v[92:95], v[128:143]\n"
      "ds_read_b128 a[224:227], v226\n"
      "s_cbranch_vccnz .Lst188\n"
      ".Lco189:\n"
      "v_mfma_f32_32x32x16_bf16 v[112:127], a[244:247], v[92:95], v[112:127]\n"
      "ds_read_b128 a[228:231], v226 offset:1024\n"
      "buffer_load_dwordx4 v224, s[36:39], s35 offen offset:1024 sc1 lds\n"
      "s_waitcnt lgkmcnt(9)\n"
      "v_mfma_f32_32x32x16_bf16 v[128:143], a[248:251], v[96:99], v[128:143]\n"
      "ds_read_b128 a[232:235], v226 offset:2048\n"
      "s_waitcnt vmcnt(6)\n"
      "v_bitop3_b32 v217, v104, v105, s97 bitop3:0x7e\n"
      "v_bitop3_b32 v254, v106, v107, s97 bitop3:0x7e\n"
      "v_bitop3_b32 v211, v108, v109, s97 bitop3:0x7e\n"
      "v_mfma_f32_32x32x16_bf16 v[112:127], a[252:255], v[96:99], v[112:127]\n"
      "ds_read_b128 a[236:239], v226 offset:3072\n"
      "buffer_load_dwordx4 v224, s[36:39], s35 offen offset:2048 sc1 lds\n"
      "v_or3_b32 v217, v217, v254, v211\n"
      "v_bitop3_b32 v254, v110, v111, s97 bitop3:0x7e\n"
      "v_bitop3_b32 v217, v217, v254, 1 bitop3:0xa8\n"
      "v_cmp_ne_u32 vcc, 0, v217\n"
      "s_waitcnt lgkmcnt(9)\n"
      "v_mfma_f32_32x32x16_bf16 v[128:143], v[160:163], v[100:103], v[128:143]\n"
      "ds_read_b128 a[240:243], v226 offset:4096\n"
      "s_cbranch_vccnz .Lst192\n"
      ".Lco193:\n"
      "v_mfma_f32_32x32x16_bf16 v[112:127], v[164:167], v[100:103], v[112:127]\n"
      "ds_read_b128 a[244:247], v226 offset:5120\n"
      "s_waitcnt lgkmcnt(9)\n"
      "v_mfma_f32_32x32x16_bf16 v[128:143], v[168:171], v[104:107], v[128:143]\n"
      "ds_read_b128 a[248:251], v226 offset:6144\n"
      "v_mfma_f32_32x32x16_bf16 v[112:127], v[172:175], v[104:107], v[112:127]\n"
      "ds_read_b128 a[252:255], v226 offset:7168\n"
      "s_waitcnt lgkmcnt(8)\n"
      "v_mfma_f32_32x32x16_bf16 v[128:143], v[176:179], v[108:111], v[128:143]\n"
      "ds_read_b128 v[160:163], v226 offset:8192\n"
      "v_mfma_f32_32x32x16_bf16 v[112:127], v[180:183], v[108:111], v[112:127]\n"
      "ds_read_b128 v[164:167], v226 offset:9216\n"
      "v_mfma_f32_32x32x16_bf16 v[128:143], v[212:215], v[220:223], v[128:143]\n"
      "ds_read_b64 v[212:213], v235 offset:256\n"
      "s_barrier\n"
      "s_waitcnt lgkmcnt(0)\n"
      "v_mfma_f32_32x32x16_bf16 v[112:127], v[212:215], v[220:223], v[112:127]\n"
      "ds_read_b64 v[212:213], v235 offset:512\n"
      "s_cmp_lg_u32 s48, 0\n"
      "s_cbranch_scc1 .Ll1196\n"
      "v_mfma_f32_32x32x16_bf16 v[144:159], a[224:227], v[0:3], 0\n"
      "ds_read_b128 v[168:171], v226 offset:10240\n"
      "v_mov_b32 v184, 0\n"
      "v_mov_b32 v185, 0\n"
      "v_mov_b32 v186, 0\n"
      "v_mov_b32 v187, 0\n"
      "v_mul_f32 v200, v218, v128\n"
      "v_mul_f32 v201, v218, v129\n"
      "v_mul_f32 v202, v218, v130\n"
      "v_mfma_f32_32x32x16_bf16 v[144:159], a[228:231], v[4:7], v[144:159]\n"
      "ds_read_b128 v[172:175], v226 offset:11264\n"
      "s_add_u32 m0, s75, 0x15000\n"
      "s_add_u32 s35, s71, 0x1c0000\n"
      "buffer_load_dwordx4 v224, s[36:39], s35 offen sc1 lds\n"
      "v_mul_f32 v203, v218, v131\n"
      "v_mul_f32 v204, v218, v132\n"
      "v_mfma_f32_32x32x16_bf16 v[144:159], a[232:235], v[8:11], v[144:159]\n"
      "ds_read_b128 v[176:179], v226 offset:12288\n"
      "v_mul_f32 v205, v218, v133\n"
      "v_mul_f32 v206, v218, v134\n"
      "v_mul_f32 v207, v218, v135\n"
      "v_mfma_f32_32x32x16_bf16 v[144:159], a[236:239], v[12:15], v[144:159]\n"
      "ds_read_b128 v[180:183], v226 offset:13312\n"
      "buffer_load_dwordx4 v224, s[36:39], s35 offen offset:1024 sc1 lds\n"
      "v_exp_f32 v200, v200\n"
      "v_mul_f32 v136, v128, v136\n"
      "v_mfma_f32_32x32x16_bf16 v[144:159], a[240:243], v[16:19], v[144:159]\n"
      "ds_read_b128 a[224:227], v226 offset:14336\n"
      "s_mov_b32 s34, 0x1e0000\n"
      "buffer_store_dwordx4 v[184:187], v229, s[36:39], s34 offen sc1\n"
      "v_exp_f32 v201, v201\n"
      "v_mfma_f32_32x32x16_bf16 v[144:159], a[244:247], v[20:23], v[144:159]\n"
      "ds_read_b128 a[228:231], v226 offset:15360\n"
      "buffer_load_dwordx4 v224, s[36:39], s35 offen offset:2048 sc1 lds\n"
      "v_mul_f32 v137, v129, v137\n"
      "v_exp_f32 v202, v202\n"
      "v_mfma_f32_32x32x16_bf16 v[144:159], a[248:251], v[24:27], v[144:159]\n"
      "ds_read_b128 a[232:235], v226 offset:16384\n"
      "v_mul_f32 v138, v130, v138\n"
      "v_exp_f32 v203, v203\n"
      "v_mfma_f32_32x32x16_bf16 v[144:159], a[252:255], v[28:31], v[144:159]\n"
      "ds_read_b128 a[236:239], v226 offset:17408\n"
      "buffer_load_dwordx4 v224, s[36:39], s35 offen offset:3072 sc1 lds\n"
      "v_mul_f32 v139, v131, v139\n"
      "v_exp_f32 v204, v204\n"
      "s_waitcnt lgkmcnt(6)\n"
      "v_mfma_f32_32x32x16_bf16 v[144:159], v[160:163], v[32:35], v[144:159]\n"
      "ds_read_b128 a[240:243], v226 offset:18432\n"
      "v_mul_f32 v140, v132, v140\n"
      "v_mfma_f32_32x32x16_bf16 v[144:159], v[164:167], v[36:39], v[144:159]\n"
      "ds_read_b128 a[244:247], v226 offset:19456\n"
      "s_add_u32 m0, s75, 0x16000\n"
      "s_add_u32 s35, s71, 0x1c1000\n"
      "buffer_load_dwordx4 v224, s[36:39], s35 offen sc1 lds\n"
      "v_exp_f32 v205, v205\n"
      "v_mul_f32 v141, v133, v141\n"
      "v_mfma_f32_32x32x16_bf16 v[144:159], v[168:171], v[40:43], v[144:159]\n"
      "ds_read_b128 a[248:251], v226 offset:20480\n"
      "v_exp_f32 v206, v206\n"
      "v_mul_f32 v142, v134, v142\n"
      "v_mfma_f32_32x32x16_bf16 v[144:159], v[172:175], v[44:47], v[144:159]\n"
      "ds_read_b128 a[252:255], v226 offset:21504\n"
      "buffer_load_dwordx4 v224, s[36:39], s35 offen offset:1024 sc1 lds\n"
      "v_exp_f32 v207, v207\n"
      "v_mul_f32 v143, v135, v143\n"
      "s_waitcnt lgkmcnt(6)\n"
      "v_mfma_f32_32x32x16_bf16 v[144:159], v[176:179], v[48:51], v[144:159]\n"
      "ds_read_b128 v[160:163], v226 offset:22528\n"
      "v_fma_f32 v200, v200, v219, v219\n"
      "v_fma_f32 v201, v201, v219, v219\n"
      "v_fma_f32 v202, v202, v219, v219\n"
      "v_mfma_f32_32x32x16_bf16 v[144:159], v[180:183], v[52:55], v[144:159]\n"
      "ds_read_b128 v[164:167], v226 offset:23552\n"
      "buffer_load_dwordx4 v224, s[36:39], s35 offen offset:2048 sc1 lds\n"
      "v_fma_f32 v203, v203, v219, v219\n"
      "v_fma_f32 v204, v204, v219, v219\n"
      "v_mfma_f32_32x32x16_bf16 v[144:159], a[224:227], v[56:59], v[144:159]\n"
      "ds_read_b128 v[168:171], v226 offset:24576\n"
      "s_mov_b32 s34, 0x1e0000\n"
      "buffer_store_dwordx4 v[184:187], v230, s[36:39], s34 offen sc1\n"
      "v_fma_f32 v205, v205, v219, v219\n"
      "v_fma_f32 v206, v206, v219, v219\n"
      "v_fma_f32 v207, v207, v219, v219\n"
      "v_mfma_f32_32x32x16_bf16 v[144:159], a[228:231], v[60:63], v[144:159]\n"
      "ds_read_b128 v[172:175], v226 offset:25600\n"
      "s_add_u32 m0, s75, 0\n"
      "s_add_u32 s35, s71, 0x230000\n"
      "buffer_load_dwordx4 v224, s[36:39], s35 offen sc1 lds\n"
      "v_rcp_f32 v200, v200\n"
      "s_waitcnt lgkmcnt(6)\n"
      "v_mfma_f32_32x32x16_bf16 v[144:159], a[232:235], v[64:67], v[144:159]\n"
      "ds_read_b128 v[176:179], v226 offset:26624\n"
      "v_rcp_f32 v201, v201\n"
      "v_mul_f32 v136, v136, v200\n"
      "v_mfma_f32_32x32x16_bf16 v[144:159], a[236:239], v[68:71], v[144:159]\n"
      "ds_read_b128 v[180:183], v226 offset:27648\n"
      "buffer_load_dwordx4 v224, s[36:39], s35 offen offset:1024 sc1 lds\n"
      "s_waitcnt vmcnt(11)\n"
      "s_barrier\n"
      "v_rcp_f32 v202, v202\n"
      "v_mul_f32 v137, v137, v201\n"
      "v_mfma_f32_32x32x16_bf16 v[144:159], a[240:243], v[72:75], v[144:159]\n"
      "ds_read_b128 a[224:227], v225\n"
      "v_rcp_f32 v203, v203\n"
      "v_mfma_f32_32x32x16_bf16 v[144:159], a[244:247], v[76:79], v[144:159]\n"
      "ds_read_b128 a[228:231], v225 offset:1024\n"
      "buffer_load_dwordx4 v224, s[36:39], s35 offen offset:2048 sc1 lds\n"
      "v_mul_f32 v138, v138, v202\n"
      "v_rcp_f32 v204, v204\n"
      "s_waitcnt lgkmcnt(6)\n"
      "v_mfma_f32_32x32x16_bf16 v[144:159], a[248:251], v[80:83], v[144:159]\n"
      "ds_read_b128 a[232:235], v225 offset:2048\n"
      "v_mul_f32 v139, v139, v203\n"
      "v_rcp_f32 v205, v205\n"
      "v_mfma_f32_32x32x16_bf16 v[144:159], a[252:255], v[84:87], v[144:159]\n"
      "ds_read_b128 a[236:239], v225 offset:3072\n"
      "buffer_load_dwordx4 v224, s[36:39], s35 offen offset:3072 sc1 lds\n"
      "v_mul_f32 v140, v140, v204\n"
      "v_rcp_f32 v206, v206\n"
      "v_mfma_f32_32x32x16_bf16 v[144:159], v[160:163], v[88:91], v[144:159]\n"
      "ds_read_b128 a[240:243], v225 offset:4096\n"
      "v_mul_f32 v141, v141, v205\n"
      "v_rcp_f32 v207, v207\n"
      "v_mfma_f32_32x32x16_bf16 v[144:159], v[164:167], v[92:95], v[144:159]\n"
      "ds_read_b128 a[244:247], v225 offset:5120\n"
      "s_add_u32 m0, s75, 0x1000\n"
      "s_add_u32 s35, s71, 0x231000\n"
      "buffer_load_dwordx4 v224, s[36:39], s35 offen sc1 lds\n"
      "v_mul_f32 v142, v142, v206\n"
      "s_add_u32 s87, s73, 0\n"
      "s_waitcnt lgkmcnt(6)\n"
      "v_mfma_f32_32x32x16_bf16 v[144:159], v[168:171], v[96:99], v[144:159]\n"
      "ds_read_b128 a[248:251], v225 offset:6144\n"
      "s_mov_b32 s34, 0x3e0000\n"
      "buffer_store_dwordx2 v[184:185], v231, s[36:39], s34 offen sc1\n"
      "v_mul_f32 v143, v143, v207\n"
      "v_cvt_pk_bf16_f32 v208, v136, v137\n"
      "v_mfma_f32_32x32x16_bf16 v[144:159], v[172:175], v[100:103], v[144:159]\n"
      "ds_read_b128 a[252:255], v225 offset:7168\n"
      "buffer_load_dwordx4 v224, s[36:39], s35 offen offset:1024 sc1 lds\n"
      "v_cvt_pk_bf16_f32 v209, v138, v139\n"
      "v_cvt_pk_bf16_f32 v210, v140, v141\n"
      "v_cvt_pk_bf16_f32 v211, v142, v143\n"
      "v_mfma_f32_32x32x16_bf16 v[144:159], v[176:179], v[104:107], v[144:159]\n"
      "ds_read_b128 v[160:163], v225 offset:8192\n"
      "v_and_or_b32 v208, v208, -2, s97\n"
      "v_and_or_b32 v209, v209, -2, s97\n"
      "v_and_or_b32 v210, v210, -2, s97\n"
      "v_mfma_f32_32x32x16_bf16 v[144:159], v[180:183], v[108:111], v[144:159]\n"
      "ds_read_b128 v[164:167], v225 offset:9216\n"
      "buffer_load_dwordx4 v224, s[36:39], s35 offen offset:2048 sc1 lds\n"
      "v_and_or_b32 v211, v211, -2, s97\n"
      "v_mfma_f32_32x32x16_bf16 v[144:159], v[212:215], v[220:223], v[144:159]\n"
      "ds_read_b64 v[212:213], v235 offset:768\n"
      "buffer_store_dwordx4 v[208:211], v224, s[36:39], s87 offen sc1\n"
      "s_waitcnt lgkmcnt(7)\n"
      "v_mfma_f32_32x32x16_bf16 v[128:143], a[224:227], v[0:3], 0\n"
      "ds_read_b128 v[168:171], v225 offset:10240\n"
      "s_add_u32 s33, s8, 0\n"
      "buffer_load_dwordx4 a[112:115], v236, s[36:39], s33 offen sc1\n"
      "v_mul_f32 v200, v218, v112\n"
      "v_mul_f32 v201, v218, v113\n"
      "v_mul_f32 v202, v218, v114\n"
      "v_mfma_f32_32x32x16_bf16 v[128:143], a[228:231], v[4:7], v[128:143]\n"
      "ds_read_b128 v[172:175], v225 offset:11264\n"
      "s_add_u32 m0, s75, 0x7000\n"
      "s_add_u32 s35, s71, 0x2a0000\n"
      "buffer_load_dwordx4 v224, s[36:39], s35 offen sc1 lds\n"
      "v_mul_f32 v203, v218, v115\n"
      "v_mul_f32 v204, v218, v116\n"
      "v_mfma_f32_32x32x16_bf16 v[128:143], a[232:235], v[8:11], v[128:143]\n"
      "ds_read_b128 v[176:179], v225 offset:12288\n"
      "s_add_u32 s33, s8, 0\n"
      "buffer_load_dwordx4 a[116:119], v237, s[36:39], s33 offen sc1\n"
      "v_mul_f32 v205, v218, v117\n"
      "v_mul_f32 v206, v218, v118\n"
      "v_mul_f32 v207, v218, v119\n"
      "v_mfma_f32_32x32x16_bf16 v[128:143], a[236:239], v[12:15], v[128:143]\n"
      "ds_read_b128 v[180:183], v225 offset:13312\n"
      "buffer_load_dwordx4 v224, s[36:39], s35 offen offset:1024 sc1 lds\n"
      "v_exp_f32 v200, v200\n"
      "v_mul_f32 v120, v112, v120\n"
      "s_waitcnt lgkmcnt(7)\n"
      "v_mfma_f32_32x32x16_bf16 v[128:143], a[240:243], v[16:19], v[128:143]\n"
      "ds_read_b128 a[224:227], v225 offset:14336\n"
      "s_add_u32 s33, s8, 0x800\n"
      "buffer_load_dwordx4 a[120:123], v236, s[36:39], s33 offen sc1\n"
      "v_exp_f32 v201, v201\n"
      "v_mfma_f32_32x32x16_bf16 v[128:143], a[244:247], v[20:23], v[128:143]\n"
      "ds_read_b128 a[228:231], v225 offset:15360\n"
      "buffer_load_dwordx4 v224, s[36:39], s35 offen offset:2048 sc1 lds\n"
      "v_mul_f32 v121, v113, v121\n"
      "v_exp_f32 v202, v202\n"
      "v_mfma_f32_32x32x16_bf16 v[128:143], a[248:251], v[24:27], v[128:143]\n"
      "ds_read_b128 a[232:235], v225 offset:16384\n"
      "s_add_u32 s33, s8, 0x800\n"
      "buffer_load_dwordx4 a[124:127], v237, s[36:39], s33 offen sc1\n"
      "v_mul_f32 v122, v114, v122\n"
      "v_exp_f32 v203, v203\n"
      "v_mfma_f32_32x32x16_bf16 v[128:143], a[252:255], v[28:31], v[128:143]\n"
      "ds_read_b128 a[236:239], v225 offset:17408\n"
      "buffer_load_dwordx4 v224, s[36:39], s35 offen offset:3072 sc1 lds\n"
      "v_mul_f32 v123, v115, v123\n"
      "v_exp_f32 v204, v204\n"
      "s_waitcnt lgkmcnt(6)\n"
      "v_mfma_f32_32x32x16_bf16 v[128:143], v[160:163], v[32:35], v[128:143]\n"
      "ds_read_b128 a[240:243], v225 offset:18432\n"
      "s_add_u32 s33, s8, 0x1000\n"
      "buffer_load_dwordx4 a[128:131], v236, s[36:39], s33 offen sc1\n"
      "v_mul_f32 v124, v116, v124\n"
      "v_mfma_f32_32x32x16_bf16 v[128:143], v[164:167], v[36:39], v[128:143]\n"
      "ds_read_b128 a[244:247], v225 offset:19456\n"
      "s_add_u32 m0, s75, 0x8000\n"
      "s_add_u32 s35, s71, 0x2a1000\n"
      "buffer_load_dwordx4 v224, s[36:39], s35 offen sc1 lds\n"
      "v_exp_f32 v205, v205\n"
      "v_mul_f32 v125, v117, v125\n"
      "v_mfma_f32_32x32x16_bf16 v[128:143], v[168:171], v[40:43], v[128:143]\n"
      "ds_read_b128 a[248:251], v225 offset:20480\n"
      "s_add_u32 s33, s8, 0x1000\n"
      "buffer_load_dwordx4 a[132:135], v237, s[36:39], s33 offen sc1\n"
      "v_exp_f32 v206, v206\n"
      "v_mul_f32 v126, v118, v126\n"
      "v_mfma_f32_32x32x16_bf16 v[128:143], v[172:175], v[44:47], v[128:143]\n"
      "ds_read_b128 a[252:255], v225 offset:21504\n"
      "buffer_load_dwordx4 v224, s[36:39], s35 offen offset:1024 sc1 lds\n"
      "v_exp_f32 v207, v207\n"
      "v_mul_f32 v127, v119, v127\n"
      "s_waitcnt lgkmcnt(6)\n"
      "v_mfma_f32_32x32x16_bf16 v[128:143], v[176:179], v[48:51], v[128:143]\n"
      "ds_read_b128 v[160:163], v225 offset:22528\n"
      "s_add_u32 s33, s8, 0x1800\n"
      "buffer_load_dwordx4 a[136:139], v236, s[36:39], s33 offen sc1\n"
      "v_fma_f32 v200, v200, v219, v219\n"
      "v_fma_f32 v201, v201, v219, v219\n"
      "v_fma_f32 v202, v202, v219, v219\n"
      "v_mfma_f32_32x32x16_bf16 v[128:143], v[180:183], v[52:55], v[128:143]\n"
      "ds_read_b128 v[164:167], v225 offset:23552\n"
      "buffer_load_dwordx4 v224, s[36:39], s35 offen offset:2048 sc1 lds\n"
      "v_fma_f32 v203, v203, v219, v219\n"
      "v_fma_f32 v204, v204, v219, v219\n"
      "v_mfma_f32_32x32x16_bf16 v[128:143], a[224:227], v[56:59], v[128:143]\n"
      "ds_read_b128 v[168:171], v225 offset:24576\n"
      "s_add_u32 s33, s8, 0x2000\n"
      "buffer_load_dwordx4 a[144:147], v236, s[36:39], s33 offen sc1\n"
      "v_fma_f32 v205, v205, v219, v219\n"
      "v_fma_f32 v206, v206, v219, v219\n"
      "v_fma_f32 v207, v207, v219, v219\n"
      "v_mfma_f32_32x32x16_bf16 v[128:143], a[228:231], v[60:63], v[128:143]\n"
      "ds_read_b128 v[172:175], v225 offset:25600\n"
      "s_add_u32 s33, s8, 0x1800\n"
      "buffer_load_dwordx4 a[140:143], v237, s[36:39], s33 offen sc1\n"
      "v_rcp_f32 v200, v200\n"
      "s_waitcnt lgkmcnt(6)\n"
      "v_mfma_f32_32x32x16_bf16 v[128:143], a[232:235], v[64:67], v[128:143]\n"
      "ds_read_b128 v[176:179], v225 offset:26624\n"
      "s_add_u32 s33, s8, 0x2800\n"
      "buffer_load_dwordx4 a[152:155], v236, s[36:39], s33 offen sc1\n"
      "v_rcp_f32 v201, v201\n"
      "v_mul_f32 v120, v120, v200\n"
      "v_mfma_f32_32x32x16_bf16 v[128:143], a[236:239], v[68:71], v[128:143]\n"
      "ds_read_b128 v[180:183], v225 offset:27648\n"
      "s_add_u32 s33, s8, 0x2000\n"
      "buffer_load_dwordx4 a[148:151], v237, s[36:39], s33 offen sc1\n"
      "s_waitcnt vmcnt(28)\n"
      "s_barrier\n"
      "v_rcp_f32 v202, v202\n"
      "v_mul_f32 v121, v121, v201\n"
      "v_mfma_f32_32x32x16_bf16 v[128:143], a[240:243], v[72:75], v[128:143]\n"
      "ds_read_b128 a[224:227], v225 offset:28672\n"
      "s_add_u32 s33, s8, 0x2800\n"
      "buffer_load_dwordx4 a[156:159], v237, s[36:39], s33 offen sc1\n"
      "v_rcp_f32 v203, v203\n"
      "v_mfma_f32_32x32x16_bf16 v[128:143], a[244:247], v[76:79], v[128:143]\n"
      "ds_read_b128 a[228:231], v225 offset:29696\n"
      "s_add_u32 m0, s75, 0x1c000\n"
      "s_add_u32 s35, s76, 0xe000\n"
      "buffer_load_dwordx4 v224, s[40:43], s35 offen sc1 lds\n"
      "v_mul_f32 v122, v122, v202\n"
      "v_rcp_f32 v204, v204\n"
      "s_waitcnt lgkmcnt(6)\n"
      "v_mfma_f32_32x32x16_bf16 v[128:143], a[248:251], v[80:83], v[128:143]\n"
      "ds_read_b128 a[232:235], v225 offset:30720\n"
      "s_add_u32 s33, s8, 0x3000\n"
      "buffer_load_dwordx4 a[160:163], v236, s[36:39], s33 offen sc1\n"
      "s_xor_b32 s80, s5, 0\n"
      "s_mul_i32 s80, s80, 0x1c00\n"
      "v_add_u32 v192, s80, v224\n"
      "s_xor_b32 s80, s5, 1\n"
      "s_mul_i32 s80, s80, 0x1c00\n"
      "v_add_u32 v193, s80, v224\n"
      "s_xor_b32 s80, s5, 2\n"
      "s_mul_i32 s80, s80, 0x1c00\n"
      "v_add_u32 v194, s80, v224\n"
      "s_xor_b32 s80, s5, 3\n"
      "s_mul_i32 s80, s80, 0x1c00\n"
      "v_add_u32 v195, s80, v224\n"
      "s_add_u32 s78, s74, 0x1000\n"
      "s_add_u32 s79, s89, 0x1000\n"
      "v_mul_f32 v123, v123, v203\n"
      "v_rcp_f32 v205, v205\n"
      "v_mfma_f32_32x32x16_bf16 v[128:143], a[252:255], v[84:87], v[128:143]\n"
      "ds_read_b128 a[236:239], v225 offset:31744\n"
      "buffer_load_dwordx4 v224, s[40:43], s35 offen offset:1024 sc1 lds\n"
      "v_mul_f32 v124, v124, v204\n"
      "v_rcp_f32 v206, v206\n"
      "v_mfma_f32_32x32x16_bf16 v[128:143], v[160:163], v[88:91], v[128:143]\n"
      "ds_read_b128 a[240:243], v225 offset:32768\n"
      "s_add_u32 s33, s8, 0x3000\n"
      "buffer_load_dwordx4 a[164:167], v237, s[36:39], s33 offen sc1\n"
      "v_mul_f32 v125, v125, v205\n"
      "v_rcp_f32 v207, v207\n"
      "v_mfma_f32_32x32x16_bf16 v[128:143], v[164:167], v[92:95], v[128:143]\n"
      "ds_read_b128 a[244:247], v225 offset:33792\n"
      "buffer_load_dwordx4 v224, s[40:43], s35 offen offset:2048 sc1 lds\n"
      "v_mul_f32 v126, v126, v206\n"
      "s_add_u32 s87, s73, 0x400\n"
      "s_waitcnt lgkmcnt(6)\n"
      "v_mfma_f32_32x32x16_bf16 v[128:143], v[168:171], v[96:99], v[128:143]\n"
      "ds_read_b128 a[248:251], v225 offset:34816\n"
      "s_add_u32 s33, s9, 0\n"
      "buffer_load_dwordx4 a[168:171], v236, s[36:39], s33 offen sc1\n"
      "v_mul_f32 v127, v127, v207\n"
      "v_cvt_pk_bf16_f32 v208, v120, v121\n"
      "v_mfma_f32_32x32x16_bf16 v[128:143], v[172:175], v[100:103], v[128:143]\n"
      "ds_read_b128 a[252:255], v225 offset:35840\n"
      "buffer_load_dwordx4 v224, s[40:43], s35 offen offset:3072 sc1 lds\n"
      "v_cvt_pk_bf16_f32 v209, v122, v123\n"
      "v_cvt_pk_bf16_f32 v210, v124, v125\n"
      "v_cvt_pk_bf16_f32 v211, v126, v127\n"
      "v_mfma_f32_32x32x16_bf16 v[128:143], v[176:179], v[104:107], v[128:143]\n"
      "ds_read_b128 v[160:163], v225 offset:36864\n"
      "s_add_u32 s33, s9, 0\n"
      "buffer_load_dwordx4 a[172:175], v237, s[36:39], s33 offen sc1\n"
      "v_and_or_b32 v208, v208, -2, s97\n"
      "v_and_or_b32 v209, v209, -2, s97\n"
      "v_and_or_b32 v210, v210, -2, s97\n"
      "v_mfma_f32_32x32x16_bf16 v[128:143], v[180:183], v[108:111], v[128:143]\n"
      "ds_read_b128 v[164:167], v225 offset:37888\n"
      "s_add_u32 m0, s75, 0x1d000\n"
      "s_add_u32 s35, s76, 0xf000\n"
      "buffer_load_dwordx4 v224, s[40:43], s35 offen sc1 lds\n"
      "v_and_or_b32 v211, v211, -2, s97\n"
      "v_mfma_f32_32x32x16_bf16 v[128:143], v[212:215], v[220:223], v[128:143]\n"
      "ds_read_b64 v[212:213], v235 offset:1024\n"
      "buffer_store_dwordx4 v[208:211], v224, s[36:39], s87 offen sc1\n"
      "s_waitcnt lgkmcnt(7)\n"
      "v_mfma_f32_32x32x16_bf16 v[112:127], a[224:227], v[0:3], 0\n"
      "ds_read_b128 v[168:171], v225 offset:38912\n"
      "s_add_u32 s33, s9, 0x800\n"
      "buffer_load_dwordx4 a[176:179], v236, s[36:39], s33 offen sc1\n"
      "v_mul_f32 v200, v218, v144\n"
      "v_mul_f32 v201, v218, v145\n"
      "v_mul_f32 v202, v218, v146\n"
      "v_mfma_f32_32x32x16_bf16 v[112:127], a[228:231], v[4:7], v[112:127]\n"
      "ds_read_b128 v[172:175], v225 offset:39936\n"
      "buffer_load_dwordx4 v224, s[40:43], s35 offen offset:1024 sc1 lds\n"
      "v_mul_f32 v203, v218, v147\n"
      "v_mul_f32 v204, v218, v148\n"
      "v_mfma_f32_32x32x16_bf16 v[112:127], a[232:235], v[8:11], v[112:127]\n"
      "ds_read_b128 v[176:179], v225 offset:40960\n"
      "s_add_u32 s33, s9, 0x800\n"
      "buffer_load_dwordx4 a[180:183], v237, s[36:39], s33 offen sc1\n"
      "v_mul_f32 v205, v218, v149\n"
      "v_mul_f32 v206, v218, v150\n"
      "v_mul_f32 v207, v218, v151\n"
      "v_mfma_f32_32x32x16_bf16 v[112:127], a[236:239], v[12:15], v[112:127]\n"
      "ds_read_b128 v[180:183], v225 offset:41984\n"
      "buffer_load_dwordx4 v224, s[40:43], s35 offen offset:2048 sc1 lds\n"
      "v_exp_f32 v200, v200\n"
      "v_mul_f32 v152, v144, v152\n"
      "s_waitcnt lgkmcnt(7)\n"
      "v_mfma_f32_32x32x16_bf16 v[112:127], a[240:243], v[16:19], v[112:127]\n"
      "ds_read_b128 a[224:227], v225 offset:43008\n"
      "s_add_u32 s33, s9, 0x1000\n"
      "buffer_load_dwordx4 a[188:191], v237, s[36:39], s33 offen sc1\n"
      "v_exp_f32 v201, v201\n"
      "v_mfma_f32_32x32x16_bf16 v[112:127], a[244:247], v[20:23], v[112:127]\n"
      "ds_read_b128 a[228:231], v225 offset:44032\n"
      "s_add_u32 s33, s9, 0x1000\n"
      "buffer_load_dwordx4 a[184:187], v236, s[36:39], s33 offen sc1\n"
      "v_mul_f32 v153, v145, v153\n"
      "v_exp_f32 v202, v202\n"
      "v_mfma_f32_32x32x16_bf16 v[112:127], a[248:251], v[24:27], v[112:127]\n"
      "ds_read_b128 a[232:235], v225 offset:45056\n"
      "s_add_u32 s33, s9, 0x1800\n"
      "buffer_load_dwordx4 a[196:199], v237, s[36:39], s33 offen sc1\n"
      "v_mul_f32 v154, v146, v154\n"
      "v_exp_f32 v203, v203\n"
      "v_mfma_f32_32x32x16_bf16 v[112:127], a[252:255], v[28:31], v[112:127]\n"
      "ds_read_b128 a[236:239], v225 offset:46080\n"
      "s_add_u32 s33, s9, 0x1800\n"
      "buffer_load_dwordx4 a[192:195], v236, s[36:39], s33 offen sc1\n"
      "v_mul_f32 v155, v147, v155\n"
      "v_exp_f32 v204, v204\n"
      "s_waitcnt lgkmcnt(6)\n"
      "v_mfma_f32_32x32x16_bf16 v[112:127], v[160:163], v[32:35], v[112:127]\n"
      "ds_read_b128 a[240:243], v225 offset:47104\n"
      "s_add_u32 s33, s9, 0x2000\n"
      "buffer_load_dwordx4 a[204:207], v237, s[36:39], s33 offen sc1\n"
      "v_mul_f32 v156, v148, v156\n"
      "v_mfma_f32_32x32x16_bf16 v[112:127], v[164:167], v[36:39], v[112:127]\n"
      "ds_read_b128 a[244:247], v225 offset:48128\n"
      "s_add_u32 s33, s9, 0x2000\n"
      "buffer_load_dwordx4 a[200:203], v236, s[36:39], s33 offen sc1\n"
      "v_exp_f32 v205, v205\n"
      "v_mul_f32 v157, v149, v157\n"
      "v_mfma_f32_32x32x16_bf16 v[112:127], v[168:171], v[40:43], v[112:127]\n"
      "ds_read_b128 a[248:251], v225 offset:49152\n"
      "s_add_u32 s33, s9, 0x2800\n"
      "buffer_load_dwordx4 a[212:215], v237, s[36:39], s33 offen sc1\n"
      "v_exp_f32 v206, v206\n"
      "v_mul_f32 v158, v150, v158\n"
      "v_mfma_f32_32x32x16_bf16 v[112:127], v[172:175], v[44:47], v[112:127]\n"
      "ds_read_b128 a[252:255], v225 offset:50176\n"
      "s_add_u32 s33, s9, 0x2800\n"
      "buffer_load_dwordx4 a[208:211], v236, s[36:39], s33 offen sc1\n"
      "v_exp_f32 v207, v207\n"
      "v_mul_f32 v159, v151, v159\n"
      "s_waitcnt lgkmcnt(6)\n"
      "v_mfma_f32_32x32x16_bf16 v[112:127], v[176:179], v[48:51], v[112:127]\n"
      "ds_read_b128 v[160:163], v225 offset:51200\n"
      "s_add_u32 s33, s9, 0x3000\n"
      "buffer_load_dwordx4 a[220:223], v237, s[36:39], s33 offen sc1\n"
      "v_fma_f32 v200, v200, v219, v219\n"
      "v_fma_f32 v201, v201, v219, v219\n"
      "v_fma_f32 v202, v202, v219, v219\n"
      "v_mfma_f32_32x32x16_bf16 v[112:127], v[180:183], v[52:55], v[112:127]\n"
      "ds_read_b128 v[164:167], v225 offset:52224\n"
      "s_add_u32 s33, s9, 0x3000\n"
      "buffer_load_dwordx4 a[216:219], v236, s[36:39], s33 offen sc1\n"
      "v_fma_f32 v203, v203, v219, v219\n"
      "v_fma_f32 v204, v204, v219, v219\n"
      "v_mfma_f32_32x32x16_bf16 v[112:127], a[224:227], v[56:59], v[112:127]\n"
      "ds_read_b128 v[168:171], v225 offset:53248\n"
      "s_add_u32 s33, s7, 0x800\n"
      "buffer_load_dwordx4 a[68:71], v237, s[36:39], s33 offen sc1\n"
      "v_fma_f32 v205, v205, v219, v219\n"
      "v_fma_f32 v206, v206, v219, v219\n"
      "v_fma_f32 v207, v207, v219, v219\n"
      "v_mfma_f32_32x32x16_bf16 v[112:127], a[228:231], v[60:63], v[112:127]\n"
      "ds_read_b128 v[172:175], v225 offset:54272\n"
      "s_add_u32 s33, s7, 0x800\n"
      "buffer_load_dwordx4 a[64:67], v236, s[36:39], s33 offen sc1\n"
      "v_rcp_f32 v200, v200\n"
      "s_waitcnt lgkmcnt(6)\n"
      "v_mfma_f32_32x32x16_bf16 v[112:127], a[232:235], v[64:67], v[112:127]\n"
      "ds_read_b128 v[176:179], v225 offset:55296\n"
      "s_add_u32 s33, s7, 0x1000\n"
      "buffer_load_dwordx4 a[76:79], v237, s[36:39], s33 offen sc1\n"
      "v_rcp_f32 v201, v201\n"
      "v_mul_f32 v152, v152, v200\n"
      "v_mfma_f32_32x32x16_bf16 v[112:127], a[236:239], v[68:71], v[112:127]\n"
      "ds_read_b128 v[180:183], v225 offset:56320\n"
      "s_add_u32 s33, s7, 0x1000\n"
      "buffer_load_dwordx4 a[72:75], v236, s[36:39], s33 offen sc1\n"
      "s_waitcnt vmcnt(48)\n"
      "s_barrier\n"
      "v_rcp_f32 v202, v202\n"
      "v_mul_f32 v153, v153, v201\n"
      "v_mfma_f32_32x32x16_bf16 v[112:127], a[240:243], v[72:75], v[112:127]\n"
      "ds_read_b128 a[224:227], v224\n"
      "v_rcp_f32 v203, v203\n"
      "v_mfma_f32_32x32x16_bf16 v[112:127], a[244:247], v[76:79], v[112:127]\n"
      "ds_read_b128 a[228:231], v224 offset:1024\n"
      "s_add_u32 m0, s75, 0xe000\n"
      "s_add_u32 s35, s76, 0\n"
      "buffer_load_dwordx4 v224, s[40:43], s35 offen sc1 lds\n"
      "v_mul_f32 v154, v154, v202\n"
      "v_rcp_f32 v204, v204\n"
      "s_waitcnt lgkmcnt(6)\n"
      "v_mfma_f32_32x32x16_bf16 v[112:127], a[248:251], v[80:83], v[112:127]\n"
      "ds_read_b128 a[232:235], v224 offset:2048\n"
      "v_mul_f32 v155, v155, v203\n"
      "v_rcp_f32 v205, v205\n"
      "v_mfma_f32_32x32x16_bf16 v[112:127], a[252:255], v[84:87], v[112:127]\n"
      "ds_read_b128 a[236:239], v224 offset:3072\n"
      "buffer_load_dwordx4 v224, s[40:43], s35 offen offset:1024 sc1 lds\n"
      "v_mul_f32 v156, v156, v204\n"
      "v_rcp_f32 v206, v206\n"
      "v_mfma_f32_32x32x16_bf16 v[112:127], v[160:163], v[88:91], v[112:127]\n"
      "ds_read_b128 a[240:243], v224 offset:4096\n"
      "v_mul_f32 v157, v157, v205\n"
      "v_rcp_f32 v207, v207\n"
      "v_mfma_f32_32x32x16_bf16 v[112:127], v[164:167], v[92:95], v[112:127]\n"
      "ds_read_b128 a[244:247], v224 offset:5120\n"
      "buffer_load_dwordx4 v224, s[40:43], s35 offen offset:2048 sc1 lds\n"
      "v_mul_f32 v158, v158, v206\n"
      "s_add_u32 s87, s73, 0x800\n"
      "s_waitcnt lgkmcnt(6)\n"
      "v_mfma_f32_32x32x16_bf16 v[112:127], v[168:171], v[96:99], v[112:127]\n"
      "ds_read_b128 a[248:251], v224 offset:6144\n"
      "v_mul_f32 v159, v159, v207\n"
      "v_cvt_pk_bf16_f32 v208, v152, v153\n"
      "v_mfma_f32_32x32x16_bf16 v[112:127], v[172:175], v[100:103], v[112:127]\n"
      "ds_read_b128 a[252:255], v224 offset:7168\n"
      "buffer_load_dwordx4 v224, s[40:43], s35 offen offset:3072 sc1 lds\n"
      "v_cvt_pk_bf16_f32 v209, v154, v155\n"
      "v_cvt_pk_bf16_f32 v210, v156, v157\n"
      "v_cvt_pk_bf16_f32 v211, v158, v159\n"
      "v_mfma_f32_32x32x16_bf16 v[112:127], v[176:179], v[104:107], v[112:127]\n"
      "ds_read_b128 v[160:163], v224 offset:8192\n"
      "v_and_or_b32 v208, v208, -2, s97\n"
      "v_and_or_b32 v209, v209, -2, s97\n"
      "v_and_or_b32 v210, v210, -2, s97\n"
      "v_mfma_f32_32x32x16_bf16 v[112:127], v[180:183], v[108:111], v[112:127]\n"
      "ds_read_b128 v[164:167], v224 offset:9216\n"
      "s_add_u32 m0, s75, 0xf000\n"
      "s_add_u32 s35, s76, 0x1000\n"
      "buffer_load_dwordx4 v224, s[40:43], s35 offen sc1 lds\n"
      "v_and_or_b32 v211, v211, -2, s97\n"
      "v_mfma_f32_32x32x16_bf16 v[112:127], v[212:215], v[220:223], v[112:127]\n"
      "ds_read_b64 v[212:213], v235 offset:1280\n"
      "buffer_store_dwordx4 v[208:211], v224, s[36:39], s87 offen sc1\n"
      "s_waitcnt lgkmcnt(7)\n"
      "v_mfma_f32_32x32x16_bf16 v[144:159], a[224:227], v[0:3], 0\n"
      "ds_read_b128 v[168:171], v224 offset:10240\n"
      "v_mul_f32 v200, v218, v128\n"
      "v_mul_f32 v201, v218, v129\n"
      "v_mul_f32 v202, v218, v130\n"
      "v_mfma_f32_32x32x16_bf16 v[144:159], a[228:231], v[4:7], v[144:159]\n"
      "ds_read_b128 v[172:175], v224 offset:11264\n"
      "buffer_load_dwordx4 v224, s[40:43], s35 offen offset:1024 sc1 lds\n"
      "v_mul_f32 v203, v218, v131\n"
      "v_mul_f32 v204, v218, v132\n"
      "v_mfma_f32_32x32x16_bf16 v[144:159], a[232:235], v[8:11], v[144:159]\n"
      "ds_read_b128 v[176:179], v224 offset:12288\n"
      "v_mul_f32 v205, v218, v133\n"
      "v_mul_f32 v206, v218, v134\n"
      "v_mul_f32 v207, v218, v135\n"
      "v_mfma_f32_32x32x16_bf16 v[144:159], a[236:239], v[12:15], v[144:159]\n"
      "ds_read_b128 v[180:183], v224 offset:13312\n"
      "buffer_load_dwordx4 v224, s[40:43], s35 offen offset:2048 sc1 lds\n"
      "v_exp_f32 v200, v200\n"
      "v_mul_f32 v136, v128, v136\n"
      "s_waitcnt lgkmcnt(7)\n"
      "v_mfma_f32_32x32x16_bf16 v[144:159], a[240:243], v[16:19], v[144:159]\n"
      "ds_read_b128 a[224:227], v224 offset:14336\n"
      "v_exp_f32 v201, v201\n"
      "v_mfma_f32_32x32x16_bf16 v[144:159], a[244:247], v[20:23], v[144:159]\n"
      "ds_read_b128 a[228:231], v224 offset:15360\n"
      "s_add_u32 s33, s7, 0x1800\n"
      "buffer_load_dwordx4 a[80:83], v236, s[36:39], s33 offen sc1\n"
      "v_mul_f32 v137, v129, v137\n"
      "v_exp_f32 v202, v202\n"
      "v_mfma_f32_32x32x16_bf16 v[144:159], a[248:251], v[24:27], v[144:159]\n"
      "ds_read_b128 a[232:235], v224 offset:16384\n"
      "v_mul_f32 v138, v130, v138\n"
      "v_exp_f32 v203, v203\n"
      "v_mfma_f32_32x32x16_bf16 v[144:159], a[252:255], v[28:31], v[144:159]\n"
      "ds_read_b128 a[236:239], v224 offset:17408\n"
      "s_add_u32 s33, s7, 0x1800\n"
      "buffer_load_dwordx4 a[84:87], v237, s[36:39], s33 offen sc1\n"
      "v_mul_f32 v139, v131, v139\n"
      "v_exp_f32 v204, v204\n"
      "s_waitcnt lgkmcnt(6)\n"
      "v_mfma_f32_32x32x16_bf16 v[144:159], v[160:163], v[32:35], v[144:159]\n"
      "ds_read_b128 a[240:243], v224 offset:18432\n"
      "v_mul_f32 v140, v132, v140\n"
      "v_mfma_f32_32x32x16_bf16 v[144:159], v[164:167], v[36:39], v[144:159]\n"
      "ds_read_b128 a[244:247], v224 offset:19456\n"
      "s_add_u32 s33, s7, 0x2000\n"
      "buffer_load_dwordx4 a[88:91], v236, s[36:39], s33 offen sc1\n"
      "v_exp_f32 v205, v205\n"
      "v_mul_f32 v141, v133, v141\n"
      "v_mfma_f32_32x32x16_bf16 v[144:159], v[168:171], v[40:43], v[144:159]\n"
      "ds_read_b128 a[248:251], v224 offset:20480\n"
      "v_exp_f32 v206, v206\n"
      "v_mul_f32 v142, v134, v142\n"
      "v_mfma_f32_32x32x16_bf16 v[144:159], v[172:175], v[44:47], v[144:159]\n"
      "ds_read_b128 a[252:255], v224 offset:21504\n"
      "s_add_u32 s33, s7, 0x2000\n"
      "buffer_load_dwordx4 a[92:95], v237, s[36:39], s33 offen sc1\n"
      "v_exp_f32 v207, v207\n"
      "v_mul_f32 v143, v135, v143\n"
      "s_waitcnt lgkmcnt(6)\n"
      "v_mfma_f32_32x32x16_bf16 v[144:159], v[176:179], v[48:51], v[144:159]\n"
      "ds_read_b128 v[160:163], v224 offset:22528\n"
      "v_fma_f32 v200, v200, v219, v219\n"
      "v_fma_f32 v201, v201, v219, v219\n"
      "v_fma_f32 v202, v202, v219, v219\n"
      "v_mfma_f32_32x32x16_bf16 v[144:159], v[180:183], v[52:55], v[144:159]\n"
      "ds_read_b128 v[164:167], v224 offset:23552\n"
      "s_add_u32 s33, s7, 0x2800\n"
      "buffer_load_dwordx4 a[96:99], v236, s[36:39], s33 offen sc1\n"
      "v_fma_f32 v203, v203, v219, v219\n"
      "v_fma_f32 v204, v204, v219, v219\n"
      "v_mfma_f32_32x32x16_bf16 v[144:159], a[224:227], v[56:59], v[144:159]\n"
      "ds_read_b128 v[168:171], v224 offset:24576\n"
      "v_fma_f32 v205, v205, v219, v219\n"
      "v_fma_f32 v206, v206, v219, v219\n"
      "v_fma_f32 v207, v207, v219, v219\n"
      "v_mfma_f32_32x32x16_bf16 v[144:159], a[228:231], v[60:63], v[144:159]\n"
      "ds_read_b128 v[172:175], v224 offset:25600\n"
      "s_add_u32 s33, s7, 0x2800\n"
      "buffer_load_dwordx4 a[100:103], v237, s[36:39], s33 offen sc1\n"
      "v_rcp_f32 v200, v200\n"
      "s_waitcnt lgkmcnt(6)\n"
      "v_mfma_f32_32x32x16_bf16 v[144:159], a[232:235], v[64:67], v[144:159]\n"
      "ds_read_b128 v[176:179], v224 offset:26624\n"
      "v_rcp_f32 v201, v201\n"
      "v_mul_f32 v136, v136, v200\n"
      "v_mfma_f32_32x32x16_bf16 v[144:159], a[236:239], v[68:71], v[144:159]\n"
      "ds_read_b128 v[180:183], v224 offset:27648\n"
      "s_waitcnt vmcnt(47)\n"
      "s_barrier\n"
      "v_rcp_f32 v202, v202\n"
      "v_mul_f32 v137, v137, v201\n"
      "v_mfma_f32_32x32x16_bf16 v[144:159], a[240:243], v[72:75], v[144:159]\n"
      "ds_read_b128 a[224:227], v224 offset:28672\n"
      "v_rcp_f32 v203, v203\n"
      "v_mfma_f32_32x32x16_bf16 v[144:159], a[244:247], v[76:79], v[144:159]\n"
      "ds_read_b128 a[228:231], v224 offset:29696\n"
      "s_add_u32 m0, s75, 0x15000\n"
      "s_add_u32 s35, s76, 0x7000\n"
      "buffer_load_dwordx4 v224, s[40:43], s35 offen sc1 lds\n"
      "v_mul_f32 v138, v138, v202\n"
      "v_rcp_f32 v204, v204\n"
      "s_waitcnt lgkmcnt(6)\n"
      "v_mfma_f32_32x32x16_bf16 v[144:159], a[248:251], v[80:83], v[144:159]\n"
      "ds_read_b128 a[232:235], v224 offset:30720\n"
      "v_mul_f32 v139, v139, v203\n"
      "v_rcp_f32 v205, v205\n"
      "v_mfma_f32_32x32x16_bf16 v[144:159], a[252:255], v[84:87], v[144:159]\n"
      "ds_read_b128 a[236:239], v224 offset:31744\n"
      "buffer_load_dwordx4 v224, s[40:43], s35 offen offset:1024 sc1 lds\n"
      "v_mul_f32 v140, v140, v204\n"
      "v_rcp_f32 v206, v206\n"
      "v_mfma_f32_32x32x16_bf16 v[144:159], v[160:163], v[88:91], v[144:159]\n"
      "ds_read_b128 a[240:243], v224 offset:32768\n"
      "v_mul_f32 v141, v141, v205\n"
      "v_rcp_f32 v207, v207\n"
      "v_mfma_f32_32x32x16_bf16 v[144:159], v[164:167], v[92:95], v[144:159]\n"
      "ds_read_b128 a[244:247], v224 offset:33792\n"
      "buffer_load_dwordx4 v224, s[40:43], s35 offen offset:2048 sc1 lds\n"
      "v_mul_f32 v142, v142, v206\n"
      "s_add_u32 s87, s73, 0xc00\n"
      "s_waitcnt lgkmcnt(6)\n"
      "v_mfma_f32_32x32x16_bf16 v[144:159], v[168:171], v[96:99], v[144:159]\n"
      "ds_read_b128 a[248:251], v224 offset:34816\n"
      "v_mul_f32 v143, v143, v207\n"
      "v_cvt_pk_bf16_f32 v208, v136, v137\n"
      "v_mfma_f32_32x32x16_bf16 v[144:159], v[172:175], v[100:103], v[144:159]\n"
      "ds_read_b128 a[252:255], v224 offset:35840\n"
      "buffer_load_dwordx4 v224, s[40:43], s35 offen offset:3072 sc1 lds\n"
      "v_cvt_pk_bf16_f32 v209, v138, v139\n"
      "v_cvt_pk_bf16_f32 v210, v140, v141\n"
      "v_cvt_pk_bf16_f32 v211, v142, v143\n"
      "v_mfma_f32_32x32x16_bf16 v[144:159], v[176:179], v[104:107], v[144:159]\n"
      "ds_read_b128 v[160:163], v224 offset:36864\n"
      "v_and_or_b32 v208, v208, -2, s97\n"
      "v_and_or_b32 v209, v209, -2, s97\n"
      "v_and_or_b32 v210, v210, -2, s97\n"
      "v_mfma_f32_32x32x16_bf16 v[144:159], v[180:183], v[108:111], v[144:159]\n"
      "ds_read_b128 v[164:167], v224 offset:37888\n"
      "s_add_u32 m0, s75, 0x16000\n"
      "s_add_u32 s35, s76, 0x8000\n"
      "buffer_load_dwordx4 v224, s[40:43], s35 offen sc1 lds\n"
      "v_and_or_b32 v211, v211, -2, s97\n"
      "v_mfma_f32_32x32x16_bf16 v[144:159], v[212:215], v[220:223], v[144:159]\n"
      "ds_read_b64 v[212:213], v235 offset:1536\n"
      "buffer_store_dwordx4 v[208:211], v224, s[36:39], s87 offen sc1\n"
      "s_waitcnt lgkmcnt(7)\n"
      "v_mfma_f32_32x32x16_bf16 v[128:143], a[224:227], v[0:3], 0\n"
      "ds_read_b128 v[168:171], v224 offset:38912\n"
      "v_mul_f32 v200, v218, v112\n"
      "v_mul_f32 v201, v218, v113\n"
      "v_mul_f32 v202, v218, v114\n"
      "v_mfma_f32_32x32x16_bf16 v[128:143], a[228:231], v[4:7], v[128:143]\n"
      "ds_read_b128 v[172:175], v224 offset:39936\n"
      "buffer_load_dwordx4 v224, s[40:43], s35 offen offset:1024 sc1 lds\n"
      "v_mul_f32 v203, v218, v115\n"
      "v_mul_f32 v204, v218, v116\n"
      "v_mfma_f32_32x32x16_bf16 v[128:143], a[232:235], v[8:11], v[128:143]\n"
      "ds_read_b128 v[176:179], v224 offset:40960\n"
      "v_mul_f32 v205, v218, v117\n"
      "v_mul_f32 v206, v218, v118\n"
      "v_mul_f32 v207, v218, v119\n"
      "v_mfma_f32_32x32x16_bf16 v[128:143], a[236:239], v[12:15], v[128:143]\n"
      "ds_read_b128 v[180:183], v224 offset:41984\n"
      "buffer_load_dwordx4 v224, s[40:43], s35 offen offset:2048 sc1 lds\n"
      "v_exp_f32 v200, v200\n"
      "v_mul_f32 v120, v112, v120\n"
      "s_waitcnt lgkmcnt(7)\n"
      "v_mfma_f32_32x32x16_bf16 v[128:143], a[240:243], v[16:19], v[128:143]\n"
      "ds_read_b128 a[224:227], v224 offset:43008\n"
      "v_exp_f32 v201, v201\n"
      "v_mfma_f32_32x32x16_bf16 v[128:143], a[244:247], v[20:23], v[128:143]\n"
      "ds_read_b128 a[228:231], v224 offset:44032\n"
      "v_mul_f32 v121, v113, v121\n"
      "v_exp_f32 v202, v202\n"
      "v_mfma_f32_32x32x16_bf16 v[128:143], a[248:251], v[24:27], v[128:143]\n"
      "ds_read_b128 a[232:235], v224 offset:45056\n"
      "v_mul_f32 v122, v114, v122\n"
      "v_exp_f32 v203, v203\n"
      "v_mfma_f32_32x32x16_bf16 v[128:143], a[252:255], v[28:31], v[128:143]\n"
      "ds_read_b128 a[236:239], v224 offset:46080\n"
      "buffer_load_dwordx4 v[0:3], v192, s[36:39], s74 offen sc1\n"
      "v_mul_f32 v123, v115, v123\n"
      "v_exp_f32 v204, v204\n"
      "s_waitcnt lgkmcnt(6)\n"
      "v_mfma_f32_32x32x16_bf16 v[128:143], v[160:163], v[32:35], v[128:143]\n"
      "ds_read_b128 a[240:243], v224 offset:47104\n"
      "v_mul_f32 v124, v116, v124\n"
      "v_mfma_f32_32x32x16_bf16 v[128:143], v[164:167], v[36:39], v[128:143]\n"
      "ds_read_b128 a[244:247], v224 offset:48128\n"
      "buffer_load_dwordx4 v[4:7], v193, s[36:39], s74 offen sc1\n"
      "v_exp_f32 v205, v205\n"
      "v_mul_f32 v125, v117, v125\n"
      "v_mfma_f32_32x32x16_bf16 v[128:143], v[168:171], v[40:43], v[128:143]\n"
      "ds_read_b128 a[248:251], v224 offset:49152\n"
      "v_exp_f32 v206, v206\n"
      "v_mul_f32 v126, v118, v126\n"
      "v_mfma_f32_32x32x16_bf16 v[128:143], v[172:175], v[44:47], v[128:143]\n"
      "ds_read_b128 a[252:255], v224 offset:50176\n"
      "buffer_load_dwordx4 v[8:11], v194, s[36:39], s74 offen sc1\n"
      "v_exp_f32 v207, v207\n"
      "v_mul_f32 v127, v119, v127\n"
      "s_waitcnt lgkmcnt(6)\n"
      "v_mfma_f32_32x32x16_bf16 v[128:143], v[176:179], v[48:51], v[128:143]\n"
      "ds_read_b128 v[160:163], v224 offset:51200\n"
      "v_fma_f32 v200, v200, v219, v219\n"
      "v_fma_f32 v201, v201, v219, v219\n"
      "v_fma_f32 v202, v202, v219, v219\n"
      "v_mfma_f32_32x32x16_bf16 v[128:143], v[180:183], v[52:55], v[128:143]\n"
      "ds_read_b128 v[164:167], v224 offset:52224\n"
      "buffer_load_dwordx4 v[12:15], v195, s[36:39], s74 offen sc1\n"
      "v_fma_f32 v203, v203, v219, v219\n"
      "v_fma_f32 v204, v204, v219, v219\n"
      "v_mfma_f32_32x32x16_bf16 v[128:143], a[224:227], v[56:59], v[128:143]\n"
      "ds_read_b128 v[168:171], v224 offset:53248\n"
      "v_fma_f32 v205, v205, v219, v219\n"
      "v_fma_f32 v206, v206, v219, v219\n"
      "v_fma_f32 v207, v207, v219, v219\n"
      "v_mfma_f32_32x32x16_bf16 v[128:143], a[228:231], v[60:63], v[128:143]\n"
      "ds_read_b128 v[172:175], v224 offset:54272\n"
      "buffer_load_dwordx4 v[16:19], v192, s[36:39], s74 offen offset:1024 sc1\n"
      "v_rcp_f32 v200, v200\n"
      "s_waitcnt lgkmcnt(6)\n"
      "v_mfma_f32_32x32x16_bf16 v[128:143], a[232:235], v[64:67], v[128:143]\n"
      "ds_read_b128 v[176:179], v224 offset:55296\n"
      "v_rcp_f32 v201, v201\n"
      "v_mul_f32 v120, v120, v200\n"
      "v_mfma_f32_32x32x16_bf16 v[128:143], a[236:239], v[68:71], v[128:143]\n"
      "ds_read_b128 v[180:183], v224 offset:56320\n"
      "buffer_load_dwordx4 v[20:23], v193, s[36:39], s74 offen offset:1024 sc1\n"
      "v_rcp_f32 v202, v202\n"
      "v_mul_f32 v121, v121, v201\n"
      "v_mfma_f32_32x32x16_bf16 v[128:143], a[240:243], v[72:75], v[128:143]\n"
      "v_rcp_f32 v203, v203\n"
      "v_mfma_f32_32x32x16_bf16 v[128:143], a[244:247], v[76:79], v[128:143]\n"
      "buffer_load_dwordx4 v[24:27], v194, s[36:39], s74 offen offset:1024 sc1\n"
      "v_mul_f32 v122, v122, v202\n"
      "v_rcp_f32 v204, v204\n"
      "s_waitcnt lgkmcnt(4)\n"
      "v_mfma_f32_32x32x16_bf16 v[128:143], a[248:251], v[80:83], v[128:143]\n"
      "v_mul_f32 v123, v123, v203\n"
      "v_rcp_f32 v205, v205\n"
      "v_mfma_f32_32x32x16_bf16 v[128:143], a[252:255], v[84:87], v[128:143]\n"
      "buffer_load_dwordx4 v[28:31], v195, s[36:39], s74 offen offset:1024 sc1\n"
      "v_mul_f32 v124, v124, v204\n"
      "v_rcp_f32 v206, v206\n"
      "v_mfma_f32_32x32x16_bf16 v[128:143], v[160:163], v[88:91], v[128:143]\n"
      "v_mul_f32 v125, v125, v205\n"
      "v_rcp_f32 v207, v207\n"
      "v_mfma_f32_32x32x16_bf16 v[128:143], v[164:167], v[92:95], v[128:143]\n"
      "buffer_load_dwordx4 v[32:35], v192, s[36:39], s74 offen offset:2048 sc1\n"
      "v_mul_f32 v126, v126, v206\n"
      "s_add_u32 s87, s73, 0x1000\n"
      "s_waitcnt lgkmcnt(0)\n"
      "v_mfma_f32_32x32x16_bf16 v[128:143], v[168:171], v[96:99], v[128:143]\n"
      "v_mul_f32 v127, v127, v207\n"
      "v_cvt_pk_bf16_f32 v208, v120, v121\n"
      "v_mfma_f32_32x32x16_bf16 v[128:143], v[172:175], v[100:103], v[128:143]\n"
      "buffer_load_dwordx4 v[36:39], v193, s[36:39], s74 offen offset:2048 sc1\n"
      "v_cvt_pk_bf16_f32 v209, v122, v123\n"
      "v_cvt_pk_bf16_f32 v210, v124, v125\n"
      "v_cvt_pk_bf16_f32 v211, v126, v127\n"
      "v_mfma_f32_32x32x16_bf16 v[128:143], v[176:179], v[104:107], v[128:143]\n"
      "v_and_or_b32 v208, v208, -2, s97\n"
      "v_and_or_b32 v209, v209, -2, s97\n"
      "v_and_or_b32 v210, v210, -2, s97\n"
      "v_mfma_f32_32x32x16_bf16 v[128:143], v[180:183], v[108:111], v[128:143]\n"
      "buffer_load_dwordx4 v[40:43], v194, s[36:39], s74 offen offset:2048 sc1\n"
      "v_and_or_b32 v211, v211, -2, s97\n"
      "v_mfma_f32_32x32x16_bf16 v[128:143], v[212:215], v[220:223], v[128:143]\n"
      "buffer_store_dwordx4 v[208:211], v224, s[36:39], s87 offen sc1\n"
      "s_branch .Ljn197\n"
      ".Ll1196:\n"
      "v_mfma_f32_32x32x16_bf16 v[144:159], a[224:227], v[0:3], 0\n"
      "ds_read_b128 v[168:171], v226 offset:10240\n"
      "s_nop 2\n"
      "v_mul_f32 v200, v218, v128\n"
      "v_mul_f32 v201, v218, v129\n"
      "v_mul_f32 v202, v218, v130\n"
      "v_mfma_f32_32x32x16_bf16 v[144:159], a[228:231], v[4:7], v[144:159]\n"
      "ds_read_b128 v[172:175], v226 offset:11264\n"
      "s_add_u32 m0, s75, 0x15000\n"
      "s_add_u32 s35, s71, 0x1c0000\n"
      "buffer_load_dwordx4 v224, s[36:39], s35 offen sc1 lds\n"
      "v_mul_f32 v203, v218, v131\n"
      "v_mul_f32 v204, v218, v132\n"
      "v_mfma_f32_32x32x16_bf16 v[144:159], a[232:235], v[8:11], v[144:159]\n"
      "ds_read_b128 v[176:179], v226 offset:12288\n"
      "v_mul_f32 v205, v218, v133\n"
      "v_mul_f32 v206, v218, v134\n"
      "v_mul_f32 v207, v218, v135\n"
      "v_mfma_f32_32x32x16_bf16 v[144:159], a[236:239], v[12:15], v[144:159]\n"
      "ds_read_b128 v[180:183], v226 offset:13312\n"
      "buffer_load_dwordx4 v224, s[36:39], s35 offen offset:1024 sc1 lds\n"
      "v_exp_f32 v200, v200\n"
      "v_mul_f32 v136, v128, v136\n"
      "v_mfma_f32_32x32x16_bf16 v[144:159], a[240:243], v[16:19], v[144:159]\n"
      "ds_read_b128 a[224:227], v226 offset:14336\n"
      "v_exp_f32 v201, v201\n"
      "v_mfma_f32_32x32x16_bf16 v[144:159], a[244:247], v[20:23], v[144:159]\n"
      "ds_read_b128 a[228:231], v226 offset:15360\n"
      "buffer_load_dwordx4 v224, s[36:39], s35 offen offset:2048 sc1 lds\n"
      "v_mul_f32 v137, v129, v137\n"
      "v_exp_f32 v202, v202\n"
      "v_mfma_f32_32x32x16_bf16 v[144:159], a[248:251], v[24:27], v[144:159]\n"
      "ds_read_b128 a[232:235], v226 offset:16384\n"
      "v_mul_f32 v138, v130, v138\n"
      "v_exp_f32 v203, v203\n"
      "v_mfma_f32_32x32x16_bf16 v[144:159], a[252:255], v[28:31], v[144:159]\n"
      "ds_read_b128 a[236:239], v226 offset:17408\n"
      "buffer_load_dwordx4 v224, s[36:39], s35 offen offset:3072 sc1 lds\n"
      "v_mul_f32 v139, v131, v139\n"
      "v_exp_f32 v204, v204\n"
      "s_waitcnt lgkmcnt(6)\n"
      "v_mfma_f32_32x32x16_bf16 v[144:159], v[160:163], v[32:35], v[144:159]\n"
      "ds_read_b128 a[240:243], v226 offset:18432\n"
      "v_mul_f32 v140, v132, v140\n"
      "v_mfma_f32_32x32x16_bf16 v[144:159], v[164:167], v[36:39], v[144:159]\n"
      "ds_read_b128 a[244:247], v226 offset:19456\n"
      "s_add_u32 m0, s75, 0x16000\n"
      "s_add_u32 s35, s71, 0x1c1000\n"
      "buffer_load_dwordx4 v224, s[36:39], s35 offen sc1 lds\n"
      "v_exp_f32 v205, v205\n"
      "v_mul_f32 v141, v133, v141\n"
      "v_mfma_f32_32x32x16_bf16 v[144:159], v[168:171], v[40:43], v[144:159]\n"
      "ds_read_b128 a[248:251], v226 offset:20480\n"
      "v_exp_f32 v206, v206\n"
      "v_mul_f32 v142, v134, v142\n"
      "v_mfma_f32_32x32x16_bf16 v[144:159], v[172:175], v[44:47], v[144:159]\n"
      "ds_read_b128 a[252:255], v226 offset:21504\n"
      "buffer_load_dwordx4 v224, s[36:39], s35 offen offset:1024 sc1 lds\n"
      "v_exp_f32 v207, v207\n"
      "v_mul_f32 v143, v135, v143\n"
      "s_waitcnt lgkmcnt(6)\n"
      "v_mfma_f32_32x32x16_bf16 v[144:159], v[176:179], v[48:51], v[144:159]\n"
      "ds_read_b128 v[160:163], v226 offset:22528\n"
      "v_fma_f32 v200, v200, v219, v219\n"
      "v_fma_f32 v201, v201, v219, v219\n"
      "v_fma_f32 v202, v202, v219, v219\n"
      "v_mfma_f32_32x32x16_bf16 v[144:159], v[180:183], v[52:55], v[144:159]\n"
      "ds_read_b128 v[164:167], v226 offset:23552\n"
      "buffer_load_dwordx4 v224, s[36:39], s35 offen offset:2048 sc1 lds\n"
      "v_fma_f32 v203, v203, v219, v219\n"
      "v_fma_f32 v204, v204, v219, v219\n"
      "v_mfma_f32_32x32x16_bf16 v[144:159], a[224:227], v[56:59], v[144:159]\n"
      "ds_read_b128 v[168:171], v226 offset:24576\n"
      "v_fma_f32 v205, v205, v219, v219\n"
      "v_fma_f32 v206, v206, v219, v219\n"
      "v_fma_f32 v207, v207, v219, v219\n"
      "v_mfma_f32_32x32x16_bf16 v[144:159], a[228:231], v[60:63], v[144:159]\n"
      "ds_read_b128 v[172:175], v226 offset:25600\n"
      "v_rcp_f32 v200, v200\n"
      "s_waitcnt lgkmcnt(6)\n"
      "v_mfma_f32_32x32x16_bf16 v[144:159], a[232:235], v[64:67], v[144:159]\n"
      "ds_read_b128 v[176:179], v226 offset:26624\n"
      "v_rcp_f32 v201, v201\n"
      "v_mul_f32 v136, v136, v200\n"
      "v_mfma_f32_32x32x16_bf16 v[144:159], a[236:239], v[68:71], v[144:159]\n"
      "ds_read_b128 v[180:183], v226 offset:27648\n"
      "s_waitcnt vmcnt(7)\n"
      "s_barrier\n"
      "v_rcp_f32 v202, v202\n"
      "v_mul_f32 v137, v137, v201\n"
      "v_mfma_f32_32x32x16_bf16 v[144:159], a[240:243], v[72:75], v[144:159]\n"
      "ds_read_b128 a[224:227], v225\n"
      "v_rcp_f32 v203, v203\n"
      "v_mfma_f32_32x32x16_bf16 v[144:159], a[244:247], v[76:79], v[144:159]\n"
      "ds_read_b128 a[228:231], v225 offset:1024\n"
      "v_mul_f32 v138, v138, v202\n"
      "v_rcp_f32 v204, v204\n"
      "s_waitcnt lgkmcnt(6)\n"
      "v_mfma_f32_32x32x16_bf16 v[144:159], a[248:251], v[80:83], v[144:159]\n"
      "ds_read_b128 a[232:235], v225 offset:2048\n"
      "v_mul_f32 v139, v139, v203\n"
      "v_rcp_f32 v205, v205\n"
      "v_mfma_f32_32x32x16_bf16 v[144:159], a[252:255], v[84:87], v[144:159]\n"
      "ds_read_b128 a[236:239], v225 offset:3072\n"
      "v_mul_f32 v140, v140, v204\n"
      "v_rcp_f32 v206, v206\n"
      "v_mfma_f32_32x32x16_bf16 v[144:159], v[160:163], v[88:91], v[144:159]\n"
      "ds_read_b128 a[240:243], v225 offset:4096\n"
      "v_mul_f32 v141, v141, v205\n"
      "v_rcp_f32 v207, v207\n"
      "v_mfma_f32_32x32x16_bf16 v[144:159], v[164:167], v[92:95], v[144:159]\n"
      "ds_read_b128 a[244:247], v225 offset:5120\n"
      "v_mul_f32 v142, v142, v206\n"
      "s_add_u32 s87, s73, 0\n"
      "s_waitcnt lgkmcnt(6)\n"
      "v_mfma_f32_32x32x16_bf16 v[144:159], v[168:171], v[96:99], v[144:159]\n"
      "ds_read_b128 a[248:251], v225 offset:6144\n"
      "v_mul_f32 v143, v143, v207\n"
      "v_cvt_pk_bf16_f32 v208, v136, v137\n"
      "v_mfma_f32_32x32x16_bf16 v[144:159], v[172:175], v[100:103], v[144:159]\n"
      "ds_read_b128 a[252:255], v225 offset:7168\n"
      "v_cvt_pk_bf16_f32 v209, v138, v139\n"
      "v_cvt_pk_bf16_f32 v210, v140, v141\n"
      "v_cvt_pk_bf16_f32 v211, v142, v143\n"
      "v_mfma_f32_32x32x16_bf16 v[144:159], v[176:179], v[104:107], v[144:159]\n"
      "ds_read_b128 v[160:163], v225 offset:8192\n"
      "v_and_or_b32 v208, v208, -2, s97\n"
      "v_and_or_b32 v209, v209, -2, s97\n"
      "v_and_or_b32 v210, v210, -2, s97\n"
      "v_mfma_f32_32x32x16_bf16 v[144:159], v[180:183], v[108:111], v[144:159]\n"
      "ds_read_b128 v[164:167], v225 offset:9216\n"
      "v_and_or_b32 v211, v211, -2, s97\n"
      "v_mfma_f32_32x32x16_bf16 v[144:159], v[212:215], v[220:223], v[144:159]\n"
      "ds_read_b64 v[212:213], v235 offset:768\n"
      "buffer_store_dwordx4 v[208:211], v224, s[36:39], s87 offen sc1\n"
      "s_waitcnt lgkmcnt(7)\n"
      "v_mfma_f32_32x32x16_bf16 v[128:143], a[224:227], v[0:3], 0\n"
      "ds_read_b128 v[168:171], v225 offset:10240\n"
      "v_mul_f32 v200, v218, v112\n"
      "v_mul_f32 v201, v218, v113\n"
      "v_mul_f32 v202, v218, v114\n"
      "v_mfma_f32_32x32x16_bf16 v[128:143], a[228:231], v[4:7], v[128:143]\n"
      "ds_read_b128 v[172:175], v225 offset:11264\n"
      "s_add_u32 m0, s75, 0\n"
      "s_add_u32 s35, s71, 0x230000\n"
      "buffer_load_dwordx4 v224, s[36:39], s35 offen sc1 lds\n"
      "v_mul_f32 v203, v218, v115\n"
      "v_mul_f32 v204, v218, v116\n"
      "v_mfma_f32_32x32x16_bf16 v[128:143], a[232:235], v[8:11], v[128:143]\n"
      "ds_read_b128 v[176:179], v225 offset:12288\n"
      "v_mul_f32 v205, v218, v117\n"
      "v_mul_f32 v206, v218, v118\n"
      "v_mul_f32 v207, v218, v119\n"
      "v_mfma_f32_32x32x16_bf16 v[128:143], a[236:239], v[12:15], v[128:143]\n"
      "ds_read_b128 v[180:183], v225 offset:13312\n"
      "buffer_load_dwordx4 v224, s[36:39], s35 offen offset:1024 sc1 lds\n"
      "v_exp_f32 v200, v200\n"
      "v_mul_f32 v120, v112, v120\n"
      "s_waitcnt lgkmcnt(7)\n"
      "v_mfma_f32_32x32x16_bf16 v[128:143], a[240:243], v[16:19], v[128:143]\n"
      "ds_read_b128 a[224:227], v225 offset:14336\n"
      "v_exp_f32 v201, v201\n"
      "v_mfma_f32_32x32x16_bf16 v[128:143], a[244:247], v[20:23], v[128:143]\n"
      "ds_read_b128 a[228:231], v225 offset:15360\n"
      "buffer_load_dwordx4 v224, s[36:39], s35 offen offset:2048 sc1 lds\n"
      "v_mul_f32 v121, v113, v121\n"
      "v_exp_f32 v202, v202\n"
      "v_mfma_f32_32x32x16_bf16 v[128:143], a[248:251], v[24:27], v[128:143]\n"
      "ds_read_b128 a[232:235], v225 offset:16384\n"
      "v_mul_f32 v122, v114, v122\n"
      "v_exp_f32 v203, v203\n"
      "v_mfma_f32_32x32x16_bf16 v[128:143], a[252:255], v[28:31], v[128:143]\n"
      "ds_read_b128 a[236:239], v225 offset:17408\n"
      "buffer_load_dwordx4 v224, s[36:39], s35 offen offset:3072 sc1 lds\n"
      "v_mul_f32 v123, v115, v123\n"
      "v_exp_f32 v204, v204\n"
      "s_waitcnt lgkmcnt(6)\n"
      "v_mfma_f32_32x32x16_bf16 v[128:143], v[160:163], v[32:35], v[128:143]\n"
      "ds_read_b128 a[240:243], v225 offset:18432\n"
      "v_mul_f32 v124, v116, v124\n"
      "v_mfma_f32_32x32x16_bf16 v[128:143], v[164:167], v[36:39], v[128:143]\n"
      "ds_read_b128 a[244:247], v225 offset:19456\n"
      "s_add_u32 m0, s75, 0x1000\n"
      "s_add_u32 s35, s71, 0x231000\n"
      "buffer_load_dwordx4 v224, s[36:39], s35 offen sc1 lds\n"
      "v_exp_f32 v205, v205\n"
      "v_mul_f32 v125, v117, v125\n"
      "v_mfma_f32_32x32x16_bf16 v[128:143], v[168:171], v[40:43], v[128:143]\n"
      "ds_read_b128 a[248:251], v225 offset:20480\n"
      "v_exp_f32 v206, v206\n"
      "v_mul_f32 v126, v118, v126\n"
      "v_mfma_f32_32x32x16_bf16 v[128:143], v[172:175], v[44:47], v[128:143]\n"
      "ds_read_b128 a[252:255], v225 offset:21504\n"
      "buffer_load_dwordx4 v224, s[36:39], s35 offen offset:1024 sc1 lds\n"
      "v_exp_f32 v207, v207\n"
      "v_mul_f32 v127, v119, v127\n"
      "s_waitcnt lgkmcnt(6)\n"
      "v_mfma_f32_32x32x16_bf16 v[128:143], v[176:179], v[48:51], v[128:143]\n"
      "ds_read_b128 v[160:163], v225 offset:22528\n"
      "v_fma_f32 v200, v200, v219, v219\n"
      "v_fma_f32 v201, v201, v219, v219\n"
      "v_fma_f32 v202, v202, v219, v219\n"
      "v_mfma_f32_32x32x16_bf16 v[128:143], v[180:183], v[52:55], v[128:143]\n"
      "ds_read_b128 v[164:167], v225 offset:23552\n"
      "buffer_load_dwordx4 v224, s[36:39], s35 offen offset:2048 sc1 lds\n"
      "v_fma_f32 v203, v203, v219, v219\n"
      "v_fma_f32 v204, v204, v219, v219\n"
      "v_mfma_f32_32x32x16_bf16 v[128:143], a[224:227], v[56:59], v[128:143]\n"
      "ds_read_b128 v[168:171], v225 offset:24576\n"
      "v_fma_f32 v205, v205, v219, v219\n"
      "v_fma_f32 v206, v206, v219, v219\n"
      "v_fma_f32 v207, v207, v219, v219\n"
      "v_mfma_f32_32x32x16_bf16 v[128:143], a[228:231], v[60:63], v[128:143]\n"
      "ds_read_b128 v[172:175], v225 offset:25600\n"
      "v_rcp_f32 v200, v200\n"
      "s_waitcnt lgkmcnt(6)\n"
      "v_mfma_f32_32x32x16_bf16 v[128:143], a[232:235], v[64:67], v[128:143]\n"
      "ds_read_b128 v[176:179], v225 offset:26624\n"
      "v_rcp_f32 v201, v201\n"
      "v_mul_f32 v120, v120, v200\n"
      "v_mfma_f32_32x32x16_bf16 v[128:143], a[236:239], v[68:71], v[128:143]\n"
      "ds_read_b128 v[180:183], v225 offset:27648\n"
      "s_waitcnt vmcnt(8)\n"
      "s_barrier\n"
      "v_rcp_f32 v202, v202\n"
      "v_mul_f32 v121, v121, v201\n"
      "v_mfma_f32_32x32x16_bf16 v[128:143], a[240:243], v[72:75], v[128:143]\n"
      "ds_read_b128 a[224:227], v225 offset:28672\n"
      "v_rcp_f32 v203, v203\n"
      "v_mfma_f32_32x32x16_bf16 v[128:143], a[244:247], v[76:79], v[128:143]\n"
      "ds_read_b128 a[228:231], v225 offset:29696\n"
      "v_mul_f32 v122, v122, v202\n"
      "v_rcp_f32 v204, v204\n"
      "s_waitcnt lgkmcnt(6)\n"
      "v_mfma_f32_32x32x16_bf16 v[128:143], a[248:251], v[80:83], v[128:143]\n"
      "ds_read_b128 a[232:235], v225 offset:30720\n"
      "s_xor_b32 s80, s5, 0\n"
      "s_mul_i32 s80, s80, 0x1c00\n"
      "v_add_u32 v192, s80, v224\n"
      "s_xor_b32 s80, s5, 1\n"
      "s_mul_i32 s80, s80, 0x1c00\n"
      "v_add_u32 v193, s80, v224\n"
      "s_xor_b32 s80, s5, 2\n"
      "s_mul_i32 s80, s80, 0x1c00\n"
      "v_add_u32 v194, s80, v224\n"
      "s_xor_b32 s80, s5, 3\n"
      "s_mul_i32 s80, s80, 0x1c00\n"
      "v_add_u32 v195, s80, v224\n"
      "s_add_u32 s78, s74, 0x1000\n"
      "s_add_u32 s79, s89, 0x1000\n"
      "v_mul_f32 v123, v123, v203\n"
      "v_rcp_f32 v205, v205\n"
      "v_mfma_f32_32x32x16_bf16 v[128:143], a[252:255], v[84:87], v[128:143]\n"
      "ds_read_b128 a[236:239], v225 offset:31744\n"
      "v_mul_f32 v124, v124, v204\n"
      "v_rcp_f32 v206, v206\n"
      "v_mfma_f32_32x32x16_bf16 v[128:143], v[160:163], v[88:91], v[128:143]\n"
      "ds_read_b128 a[240:243], v225 offset:32768\n"
      "v_mul_f32 v125, v125, v205\n"
      "v_rcp_f32 v207, v207\n"
      "v_mfma_f32_32x32x16_bf16 v[128:143], v[164:167], v[92:95], v[128:143]\n"
      "ds_read_b128 a[244:247], v225 offset:33792\n"
      "v_mul_f32 v126, v126, v206\n"
      "s_add_u32 s87, s73, 0x400\n"
      "s_waitcnt lgkmcnt(6)\n"
      "v_mfma_f32_32x32x16_bf16 v[128:143], v[168:171], v[96:99], v[128:143]\n"
      "ds_read_b128 a[248:251], v225 offset:34816\n"
      "v_mul_f32 v127, v127, v207\n"
      "v_cvt_pk_bf16_f32 v208, v120, v121\n"
      "v_mfma_f32_32x32x16_bf16 v[128:143], v[172:175], v[100:103], v[128:143]\n"
      "ds_read_b128 a[252:255], v225 offset:35840\n"
      "v_cvt_pk_bf16_f32 v209, v122, v123\n"
      "v_cvt_pk_bf16_f32 v210, v124, v125\n"
      "v_cvt_pk_bf16_f32 v211, v126, v127\n"
      "v_mfma_f32_32x32x16_bf16 v[128:143], v[176:179], v[104:107], v[128:143]\n"
      "ds_read_b128 v[160:163], v225 offset:36864\n"
      "v_and_or_b32 v208, v208, -2, s97\n"
      "v_and_or_b32 v209, v209, -2, s97\n"
      "v_and_or_b32 v210, v210, -2, s97\n"
      "v_mfma_f32_32x32x16_bf16 v[128:143], v[180:183], v[108:111], v[128:143]\n"
      "ds_read_b128 v[164:167], v225 offset:37888\n"
      "v_and_or_b32 v211, v211, -2, s97\n"
      "v_mfma_f32_32x32x16_bf16 v[128:143], v[212:215], v[220:223], v[128:143]\n"
      "ds_read_b64 v[212:213], v235 offset:1024\n"
      "buffer_store_dwordx4 v[208:211], v224, s[36:39], s87 offen sc1\n"
      "s_waitcnt lgkmcnt(7)\n"
      "v_mfma_f32_32x32x16_bf16 v[112:127], a[224:227], v[0:3], 0\n"
      "ds_read_b128 v[168:171], v225 offset:38912\n"
      "v_mul_f32 v200, v218, v144\n"
      "v_mul_f32 v201, v218, v145\n"
      "v_mul_f32 v202, v218, v146\n"
      "v_mfma_f32_32x32x16_bf16 v[112:127], a[228:231], v[4:7], v[112:127]\n"
      "ds_read_b128 v[172:175], v225 offset:39936\n"
      "s_add_u32 m0, s75, 0x7000\n"
      "s_add_u32 s35, s71, 0x2a0000\n"
      "buffer_load_dwordx4 v224, s[36:39], s35 offen sc1 lds\n"
      "v_mul_f32 v203, v218, v147\n"
      "v_mul_f32 v204, v218, v148\n"
      "v_mfma_f32_32x32x16_bf16 v[112:127], a[232:235], v[8:11], v[112:127]\n"
      "ds_read_b128 v[176:179], v225 offset:40960\n"
      "v_mul_f32 v205, v218, v149\n"
      "v_mul_f32 v206, v218, v150\n"
      "v_mul_f32 v207, v218, v151\n"
      "v_mfma_f32_32x32x16_bf16 v[112:127], a[236:239], v[12:15], v[112:127]\n"
      "ds_read_b128 v[180:183], v225 offset:41984\n"
      "buffer_load_dwordx4 v224, s[36:39], s35 offen offset:1024 sc1 lds\n"
      "v_exp_f32 v200, v200\n"
      "v_mul_f32 v152, v144, v152\n"
      "s_waitcnt lgkmcnt(7)\n"
      "v_mfma_f32_32x32x16_bf16 v[112:127], a[240:243], v[16:19], v[112:127]\n"
      "ds_read_b128 a[224:227], v225 offset:43008\n"
      "v_exp_f32 v201, v201\n"
      "v_mfma_f32_32x32x16_bf16 v[112:127], a[244:247], v[20:23], v[112:127]\n"
      "ds_read_b128 a[228:231], v225 offset:44032\n"
      "buffer_load_dwordx4 v224, s[36:39], s35 offen offset:2048 sc1 lds\n"
      "v_mul_f32 v153, v145, v153\n"
      "v_exp_f32 v202, v202\n"
      "v_mfma_f32_32x32x16_bf16 v[112:127], a[248:251], v[24:27], v[112:127]\n"
      "ds_read_b128 a[232:235], v225 offset:45056\n"
      "v_mul_f32 v154, v146, v154\n"
      "v_exp_f32 v203, v203\n"
      "v_mfma_f32_32x32x16_bf16 v[112:127], a[252:255], v[28:31], v[112:127]\n"
      "ds_read_b128 a[236:239], v225 offset:46080\n"
      "buffer_load_dwordx4 v224, s[36:39], s35 offen offset:3072 sc1 lds\n"
      "v_mul_f32 v155, v147, v155\n"
      "v_exp_f32 v204, v204\n"
      "s_waitcnt lgkmcnt(6)\n"
      "v_mfma_f32_32x32x16_bf16 v[112:127], v[160:163], v[32:35], v[112:127]\n"
      "ds_read_b128 a[240:243], v225 offset:47104\n"
      "v_mul_f32 v156, v148, v156\n"
      "v_mfma_f32_32x32x16_bf16 v[112:127], v[164:167], v[36:39], v[112:127]\n"
      "ds_read_b128 a[244:247], v225 offset:48128\n"
      "s_add_u32 m0, s75, 0x8000\n"
      "s_add_u32 s35, s71, 0x2a1000\n"
      "buffer_load_dwordx4 v224, s[36:39], s35 offen sc1 lds\n"
      "v_exp_f32 v205, v205\n"
      "v_mul_f32 v157, v149, v157\n"
      "v_mfma_f32_32x32x16_bf16 v[112:127], v[168:171], v[40:43], v[112:127]\n"
      "ds_read_b128 a[248:251], v225 offset:49152\n"
      "v_exp_f32 v206, v206\n"
      "v_mul_f32 v158, v150, v158\n"
      "v_mfma_f32_32x32x16_bf16 v[112:127], v[172:175], v[44:47], v[112:127]\n"
      "ds_read_b128 a[252:255], v225 offset:50176\n"
      "buffer_load_dwordx4 v224, s[36:39], s35 offen offset:1024 sc1 lds\n"
      "v_exp_f32 v207, v207\n"
      "v_mul_f32 v159, v151, v159\n"
      "s_waitcnt lgkmcnt(6)\n"
      "v_mfma_f32_32x32x16_bf16 v[112:127], v[176:179], v[48:51], v[112:127]\n"
      "ds_read_b128 v[160:163], v225 offset:51200\n"
      "v_fma_f32 v200, v200, v219, v219\n"
      "v_fma_f32 v201, v201, v219, v219\n"
      "v_fma_f32 v202, v202, v219, v219\n"
      "v_mfma_f32_32x32x16_bf16 v[112:127], v[180:183], v[52:55], v[112:127]\n"
      "ds_read_b128 v[164:167], v225 offset:52224\n"
      "buffer_load_dwordx4 v224, s[36:39], s35 offen offset:2048 sc1 lds\n"
      "v_fma_f32 v203, v203, v219, v219\n"
      "v_fma_f32 v204, v204, v219, v219\n"
      "v_mfma_f32_32x32x16_bf16 v[112:127], a[224:227], v[56:59], v[112:127]\n"
      "ds_read_b128 v[168:171], v225 offset:53248\n"
      "v_fma_f32 v205, v205, v219, v219\n"
      "v_fma_f32 v206, v206, v219, v219\n"
      "v_fma_f32 v207, v207, v219, v219\n"
      "v_mfma_f32_32x32x16_bf16 v[112:127], a[228:231], v[60:63], v[112:127]\n"
      "ds_read_b128 v[172:175], v225 offset:54272\n"
      "v_rcp_f32 v200, v200\n"
      "s_waitcnt lgkmcnt(6)\n"
      "v_mfma_f32_32x32x16_bf16 v[112:127], a[232:235], v[64:67], v[112:127]\n"
      "ds_read_b128 v[176:179], v225 offset:55296\n"
      "v_rcp_f32 v201, v201\n"
      "v_mul_f32 v152, v152, v200\n"
      "v_mfma_f32_32x32x16_bf16 v[112:127], a[236:239], v[68:71], v[112:127]\n"
      "ds_read_b128 v[180:183], v225 offset:56320\n"
      "s_waitcnt vmcnt(8)\n"
      "s_barrier\n"
      "v_rcp_f32 v202, v202\n"
      "v_mul_f32 v153, v153, v201\n"
      "v_mfma_f32_32x32x16_bf16 v[112:127], a[240:243], v[72:75], v[112:127]\n"
      "ds_read_b128 a[224:227], v224\n"
      "v_rcp_f32 v203, v203\n"
      "v_mfma_f32_32x32x16_bf16 v[112:127], a[244:247], v[76:79], v[112:127]\n"
      "ds_read_b128 a[228:231], v224 offset:1024\n"
      "v_mul_f32 v154, v154, v202\n"
      "v_rcp_f32 v204, v204\n"
      "s_waitcnt lgkmcnt(6)\n"
      "v_mfma_f32_32x32x16_bf16 v[112:127], a[248:251], v[80:83], v[112:127]\n"
      "ds_read_b128 a[232:235], v224 offset:2048\n"
      "v_mul_f32 v155, v155, v203\n"
      "v_rcp_f32 v205, v205\n"
      "v_mfma_f32_32x32x16_bf16 v[112:127], a[252:255], v[84:87], v[112:127]\n"
      "ds_read_b128 a[236:239], v224 offset:3072\n"
      "v_mul_f32 v156, v156, v204\n"
      "v_rcp_f32 v206, v206\n"
      "v_mfma_f32_32x32x16_bf16 v[112:127], v[160:163], v[88:91], v[112:127]\n"
      "ds_read_b128 a[240:243], v224 offset:4096\n"
      "v_mul_f32 v157, v157, v205\n"
      "v_rcp_f32 v207, v207\n"
      "v_mfma_f32_32x32x16_bf16 v[112:127], v[164:167], v[92:95], v[112:127]\n"
      "ds_read_b128 a[244:247], v224 offset:5120\n"
      "v_mul_f32 v158, v158, v206\n"
      "s_add_u32 s87, s73, 0x800\n"
      "s_waitcnt lgkmcnt(6)\n"
      "v_mfma_f32_32x32x16_bf16 v[112:127], v[168:171], v[96:99], v[112:127]\n"
      "ds_read_b128 a[248:251], v224 offset:6144\n"
      "v_mul_f32 v159, v159, v207\n"
      "v_cvt_pk_bf16_f32 v208, v152, v153\n"
      "v_mfma_f32_32x32x16_bf16 v[112:127], v[172:175], v[100:103], v[112:127]\n"
      "ds_read_b128 a[252:255], v224 offset:7168\n"
      "v_cvt_pk_bf16_f32 v209, v154, v155\n"
      "v_cvt_pk_bf16_f32 v210, v156, v157\n"
      "v_cvt_pk_bf16_f32 v211, v158, v159\n"
      "v_mfma_f32_32x32x16_bf16 v[112:127], v[176:179], v[104:107], v[112:127]\n"
      "ds_read_b128 v[160:163], v224 offset:8192\n"
      "v_and_or_b32 v208, v208, -2, s97\n"
      "v_and_or_b32 v209, v209, -2, s97\n"
      "v_and_or_b32 v210, v210, -2, s97\n"
      "v_mfma_f32_32x32x16_bf16 v[112:127], v[180:183], v[108:111], v[112:127]\n"
      "ds_read_b128 v[164:167], v224 offset:9216\n"
      "v_and_or_b32 v211, v211, -2, s97\n"
      "v_mfma_f32_32x32x16_bf16 v[112:127], v[212:215], v[220:223], v[112:127]\n"
      "ds_read_b64 v[212:213], v235 offset:1280\n"
      "buffer_store_dwordx4 v[208:211], v224, s[36:39], s87 offen sc1\n"
      "s_waitcnt lgkmcnt(7)\n"
      "v_mfma_f32_32x32x16_bf16 v[144:159], a[224:227], v[0:3], 0\n"
      "ds_read_b128 v[168:171], v224 offset:10240\n"
      "v_mul_f32 v200, v218, v128\n"
      "v_mul_f32 v201, v218, v129\n"
      "v_mul_f32 v202, v218, v130\n"
      "v_mfma_f32_32x32x16_bf16 v[144:159], a[228:231], v[4:7], v[144:159]\n"
      "ds_read_b128 v[172:175], v224 offset:11264\n"
      "v_mul_f32 v203, v218, v131\n"
      "v_mul_f32 v204, v218, v132\n"
      "v_mfma_f32_32x32x16_bf16 v[144:159], a[232:235], v[8:11], v[144:159]\n"
      "ds_read_b128 v[176:179], v224 offset:12288\n"
      "v_mul_f32 v205, v218, v133\n"
      "v_mul_f32 v206, v218, v134\n"
      "v_mul_f32 v207, v218, v135\n"
      "v_mfma_f32_32x32x16_bf16 v[144:159], a[236:239], v[12:15], v[144:159]\n"
      "ds_read_b128 v[180:183], v224 offset:13312\n"
      "v_exp_f32 v200, v200\n"
      "v_mul_f32 v136, v128, v136\n"
      "s_waitcnt lgkmcnt(7)\n"
      "v_mfma_f32_32x32x16_bf16 v[144:159], a[240:243], v[16:19], v[144:159]\n"
      "ds_read_b128 a[224:227], v224 offset:14336\n"
      "v_exp_f32 v201, v201\n"
      "v_mfma_f32_32x32x16_bf16 v[144:159], a[244:247], v[20:23], v[144:159]\n"
      "ds_read_b128 a[228:231], v224 offset:15360\n"
      "v_mul_f32 v137, v129, v137\n"
      "v_exp_f32 v202, v202\n"
      "v_mfma_f32_32x32x16_bf16 v[144:159], a[248:251], v[24:27], v[144:159]\n"
      "ds_read_b128 a[232:235], v224 offset:16384\n"
      "v_mul_f32 v138, v130, v138\n"
      "v_exp_f32 v203, v203\n"
      "v_mfma_f32_32x32x16_bf16 v[144:159], a[252:255], v[28:31], v[144:159]\n"
      "ds_read_b128 a[236:239], v224 offset:17408\n"
      "v_mul_f32 v139, v131, v139\n"
      "v_exp_f32 v204, v204\n"
      "s_waitcnt lgkmcnt(6)\n"
      "v_mfma_f32_32x32x16_bf16 v[144:159], v[160:163], v[32:35], v[144:159]\n"
      "ds_read_b128 a[240:243], v224 offset:18432\n"
      "v_mul_f32 v140, v132, v140\n"
      "v_mfma_f32_32x32x16_bf16 v[144:159], v[164:167], v[36:39], v[144:159]\n"
      "ds_read_b128 a[244:247], v224 offset:19456\n"
      "v_exp_f32 v205, v205\n"
      "v_mul_f32 v141, v133, v141\n"
      "v_mfma_f32_32x32x16_bf16 v[144:159], v[168:171], v[40:43], v[144:159]\n"
      "ds_read_b128 a[248:251], v224 offset:20480\n"
      "v_exp_f32 v206, v206\n"
      "v_mul_f32 v142, v134, v142\n"
      "v_mfma_f32_32x32x16_bf16 v[144:159], v[172:175], v[44:47], v[144:159]\n"
      "ds_read_b128 a[252:255], v224 offset:21504\n"
      "v_exp_f32 v207, v207\n"
      "v_mul_f32 v143, v135, v143\n"
      "s_waitcnt lgkmcnt(6)\n"
      "v_mfma_f32_32x32x16_bf16 v[144:159], v[176:179], v[48:51], v[144:159]\n"
      "ds_read_b128 v[160:163], v224 offset:22528\n"
      "v_fma_f32 v200, v200, v219, v219\n"
      "v_fma_f32 v201, v201, v219, v219\n"
      "v_fma_f32 v202, v202, v219, v219\n"
      "v_mfma_f32_32x32x16_bf16 v[144:159], v[180:183], v[52:55], v[144:159]\n"
      "ds_read_b128 v[164:167], v224 offset:23552\n"
      "v_fma_f32 v203, v203, v219, v219\n"
      "v_fma_f32 v204, v204, v219, v219\n"
      "v_mfma_f32_32x32x16_bf16 v[144:159], a[224:227], v[56:59], v[144:159]\n"
      "ds_read_b128 v[168:171], v224 offset:24576\n"
      "v_fma_f32 v205, v205, v219, v219\n"
      "v_fma_f32 v206, v206, v219, v219\n"
      "v_fma_f32 v207, v207, v219, v219\n"
      "v_mfma_f32_32x32x16_bf16 v[144:159], a[228:231], v[60:63], v[144:159]\n"
      "ds_read_b128 v[172:175], v224 offset:25600\n"
      "v_rcp_f32 v200, v200\n"
      "s_waitcnt lgkmcnt(6)\n"
      "v_mfma_f32_32x32x16_bf16 v[144:159], a[232:235], v[64:67], v[144:159]\n"
      "ds_read_b128 v[176:179], v224 offset:26624\n"
      "v_rcp_f32 v201, v201\n"
      "v_mul_f32 v136, v136, v200\n"
      "v_mfma_f32_32x32x16_bf16 v[144:159], a[236:239], v[68:71], v[144:159]\n"
      "ds_read_b128 v[180:183], v224 offset:27648\n"
      "s_waitcnt vmcnt(1)\n"
      "s_barrier\n"
      "v_rcp_f32 v202, v202\n"
      "v_mul_f32 v137, v137, v201\n"
      "v_mfma_f32_32x32x16_bf16 v[144:159], a[240:243], v[72:75], v[144:159]\n"
      "ds_read_b128 a[224:227], v224 offset:28672\n"
      "v_rcp_f32 v203, v203\n"
      "v_mfma_f32_32x32x16_bf16 v[144:159], a[244:247], v[76:79], v[144:159]\n"
      "ds_read_b128 a[228:231], v224 offset:29696\n"
      "v_mul_f32 v138, v138, v202\n"
      "v_rcp_f32 v204, v204\n"
      "s_waitcnt lgkmcnt(6)\n"
      "v_mfma_f32_32x32x16_bf16 v[144:159], a[248:251], v[80:83], v[144:159]\n"
      "ds_read_b128 a[232:235], v224 offset:30720\n"
      "v_mul_f32 v139, v139, v203\n"
      "v_rcp_f32 v205, v205\n"
      "v_mfma_f32_32x32x16_bf16 v[144:159], a[252:255], v[84:87], v[144:159]\n"
      "ds_read_b128 a[236:239], v224 offset:31744\n"
      "v_mul_f32 v140, v140, v204\n"
      "v_rcp_f32 v206, v206\n"
      "v_mfma_f32_32x32x16_bf16 v[144:159], v[160:163], v[88:91], v[144:159]\n"
      "ds_read_b128 a[240:243], v224 offset:32768\n"
      "v_mul_f32 v141, v141, v205\n"
      "v_rcp_f32 v207, v207\n"
      "v_mfma_f32_32x32x16_bf16 v[144:159], v[164:167], v[92:95], v[144:159]\n"
      "ds_read_b128 a[244:247], v224 offset:33792\n"
      "v_mul_f32 v142, v142, v206\n"
      "s_add_u32 s87, s73, 0xc00\n"
      "s_waitcnt lgkmcnt(6)\n"
      "v_mfma_f32_32x32x16_bf16 v[144:159], v[168:171], v[96:99], v[144:159]\n"
      "ds_read_b128 a[248:251], v224 offset:34816\n"
      "v_mul_f32 v143, v143, v207\n"
      "v_cvt_pk_bf16_f32 v208, v136, v137\n"
      "v_mfma_f32_32x32x16_bf16 v[144:159], v[172:175], v[100:103], v[144:159]\n"
      "ds_read_b128 a[252:255], v224 offset:35840\n"
      "v_cvt_pk_bf16_f32 v209, v138, v139\n"
      "v_cvt_pk_bf16_f32 v210, v140, v141\n"
      "v_cvt_pk_bf16_f32 v211, v142, v143\n"
      "v_mfma_f32_32x32x16_bf16 v[144:159], v[176:179], v[104:107], v[144:159]\n"
      "ds_read_b128 v[160:163], v224 offset:36864\n"
      "v_and_or_b32 v208, v208, -2, s97\n"
      "v_and_or_b32 v209, v209, -2, s97\n"
      "v_and_or_b32 v210, v210, -2, s97\n"
      "v_mfma_f32_32x32x16_bf16 v[144:159], v[180:183], v[108:111], v[144:159]\n"
      "ds_read_b128 v[164:167], v224 offset:37888\n"
      "v_and_or_b32 v211, v211, -2, s97\n"
      "v_mfma_f32_32x32x16_bf16 v[144:159], v[212:215], v[220:223], v[144:159]\n"
      "ds_read_b64 v[212:213], v235 offset:1536\n"
      "buffer_store_dwordx4 v[208:211], v224, s[36:39], s87 offen sc1\n"
      "s_waitcnt lgkmcnt(7)\n"
      "v_mfma_f32_32x32x16_bf16 v[128:143], a[224:227], v[0:3], 0\n"
      "ds_read_b128 v[168:171], v224 offset:38912\n"
      "v_mul_f32 v200, v218, v112\n"
      "v_mul_f32 v201, v218, v113\n"
      "v_mul_f32 v202, v218, v114\n"
      "v_mfma_f32_32x32x16_bf16 v[128:143], a[228:231], v[4:7], v[128:143]\n"
      "ds_read_b128 v[172:175], v224 offset:39936\n"
      "v_mul_f32 v203, v218, v115\n"
      "v_mul_f32 v204, v218, v116\n"
      "v_mfma_f32_32x32x16_bf16 v[128:143], a[232:235], v[8:11], v[128:143]\n"
      "ds_read_b128 v[176:179], v224 offset:40960\n"
      "v_mul_f32 v205, v218, v117\n"
      "v_mul_f32 v206, v218, v118\n"
      "v_mul_f32 v207, v218, v119\n"
      "v_mfma_f32_32x32x16_bf16 v[128:143], a[236:239], v[12:15], v[128:143]\n"
      "ds_read_b128 v[180:183], v224 offset:41984\n"
      "v_exp_f32 v200, v200\n"
      "v_mul_f32 v120, v112, v120\n"
      "s_waitcnt lgkmcnt(7)\n"
      "v_mfma_f32_32x32x16_bf16 v[128:143], a[240:243], v[16:19], v[128:143]\n"
      "ds_read_b128 a[224:227], v224 offset:43008\n"
      "v_exp_f32 v201, v201\n"
      "v_mfma_f32_32x32x16_bf16 v[128:143], a[244:247], v[20:23], v[128:143]\n"
      "ds_read_b128 a[228:231], v224 offset:44032\n"
      "v_mul_f32 v121, v113, v121\n"
      "v_exp_f32 v202, v202\n"
      "v_mfma_f32_32x32x16_bf16 v[128:143], a[248:251], v[24:27], v[128:143]\n"
      "ds_read_b128 a[232:235], v224 offset:45056\n"
      "v_mul_f32 v122, v114, v122\n"
      "v_exp_f32 v203, v203\n"
      "v_mfma_f32_32x32x16_bf16 v[128:143], a[252:255], v[28:31], v[128:143]\n"
      "ds_read_b128 a[236:239], v224 offset:46080\n"
      "buffer_load_dwordx4 v[0:3], v192, s[36:39], s74 offen sc1\n"
      "v_mul_f32 v123, v115, v123\n"
      "v_exp_f32 v204, v204\n"
      "s_waitcnt lgkmcnt(6)\n"
      "v_mfma_f32_32x32x16_bf16 v[128:143], v[160:163], v[32:35], v[128:143]\n"
      "ds_read_b128 a[240:243], v224 offset:47104\n"
      "v_mul_f32 v124, v116, v124\n"
      "v_mfma_f32_32x32x16_bf16 v[128:143], v[164:167], v[36:39], v[128:143]\n"
      "ds_read_b128 a[244:247], v224 offset:48128\n"
      "buffer_load_dwordx4 v[4:7], v193, s[36:39], s74 offen sc1\n"
      "v_exp_f32 v205, v205\n"
      "v_mul_f32 v125, v117, v125\n"
      "v_mfma_f32_32x32x16_bf16 v[128:143], v[168:171], v[40:43], v[128:143]\n"
      "ds_read_b128 a[248:251], v224 offset:49152\n"
      "v_exp_f32 v206, v206\n"
      "v_mul_f32 v126, v118, v126\n"
      "v_mfma_f32_32x32x16_bf16 v[128:143], v[172:175], v[44:47], v[128:143]\n"
      "ds_read_b128 a[252:255], v224 offset:50176\n"
      "buffer_load_dwordx4 v[8:11], v194, s[36:39], s74 offen sc1\n"
      "v_exp_f32 v207, v207\n"
      "v_mul_f32 v127, v119, v127\n"
      "s_waitcnt lgkmcnt(6)\n"
      "v_mfma_f32_32x32x16_bf16 v[128:143], v[176:179], v[48:51], v[128:143]\n"
      "ds_read_b128 v[160:163], v224 offset:51200\n"
      "v_fma_f32 v200, v200, v219, v219\n"
      "v_fma_f32 v201, v201, v219, v219\n"
      "v_fma_f32 v202, v202, v219, v219\n"
      "v_mfma_f32_32x32x16_bf16 v[128:143], v[180:183], v[52:55], v[128:143]\n"
      "ds_read_b128 v[164:167], v224 offset:52224\n"
      "buffer_load_dwordx4 v[12:15], v195, s[36:39], s74 offen sc1\n"
      "v_fma_f32 v203, v203, v219, v219\n"
      "v_fma_f32 v204, v204, v219, v219\n"
      "v_mfma_f32_32x32x16_bf16 v[128:143], a[224:227], v[56:59], v[128:143]\n"
      "ds_read_b128 v[168:171], v224 offset:53248\n"
      "v_fma_f32 v205, v205, v219, v219\n"
      "v_fma_f32 v206, v206, v219, v219\n"
      "v_fma_f32 v207, v207, v219, v219\n"
      "v_mfma_f32_32x32x16_bf16 v[128:143], a[228:231], v[60:63], v[128:143]\n"
      "ds_read_b128 v[172:175], v224 offset:54272\n"
      "buffer_load_dwordx4 v[16:19], v192, s[36:39], s74 offen offset:1024 sc1\n"
      "v_rcp_f32 v200, v200\n"
      "s_waitcnt lgkmcnt(6)\n"
      "v_mfma_f32_32x32x16_bf16 v[128:143], a[232:235], v[64:67], v[128:143]\n"
      "ds_read_b128 v[176:179], v224 offset:55296\n"
      "v_rcp_f32 v201, v201\n"
      "v_mul_f32 v120, v120, v200\n"
      "v_mfma_f32_32x32x16_bf16 v[128:143], a[236:239], v[68:71], v[128:143]\n"
      "ds_read_b128 v[180:183], v224 offset:56320\n"
      "buffer_load_dwordx4 v[20:23], v193, s[36:39], s74 offen offset:1024 sc1\n"
      "v_rcp_f32 v202, v202\n"
      "v_mul_f32 v121, v121, v201\n"
      "v_mfma_f32_32x32x16_bf16 v[128:143], a[240:243], v[72:75], v[128:143]\n"
      "v_rcp_f32 v203, v203\n"
      "v_mfma_f32_32x32x16_bf16 v[128:143], a[244:247], v[76:79], v[128:143]\n"
      "buffer_load_dwordx4 v[24:27], v194, s[36:39], s74 offen offset:1024 sc1\n"
      "v_mul_f32 v122, v122, v202\n"
      "v_rcp_f32 v204, v204\n"
      "s_waitcnt lgkmcnt(4)\n"
      "v_mfma_f32_32x32x16_bf16 v[128:143], a[248:251], v[80:83], v[128:143]\n"
      "v_mul_f32 v123, v123, v203\n"
      "v_rcp_f32 v205, v205\n"
      "v_mfma_f32_32x32x16_bf16 v[128:143], a[252:255], v[84:87], v[128:143]\n"
      "buffer_load_dwordx4 v[28:31], v195, s[36:39], s74 offen offset:1024 sc1\n"
      "v_mul_f32 v124, v124, v204\n"
      "v_rcp_f32 v206, v206\n"
      "v_mfma_f32_32x32x16_bf16 v[128:143], v[160:163], v[88:91], v[128:143]\n"
      "v_mul_f32 v125, v125, v205\n"
      "v_rcp_f32 v207, v207\n"
      "v_mfma_f32_32x32x16_bf16 v[128:143], v[164:167], v[92:95], v[128:143]\n"
      "buffer_load_dwordx4 v[32:35], v192, s[36:39], s74 offen offset:2048 sc1\n"
      "v_mul_f32 v126, v126, v206\n"
      "s_add_u32 s87, s73, 0x1000\n"
      "s_waitcnt lgkmcnt(0)\n"
      "v_mfma_f32_32x32x16_bf16 v[128:143], v[168:171], v[96:99], v[128:143]\n"
      "v_mul_f32 v127, v127, v207\n"
      "v_cvt_pk_bf16_f32 v208, v120, v121\n"
      "v_mfma_f32_32x32x16_bf16 v[128:143], v[172:175], v[100:103], v[128:143]\n"
      "buffer_load_dwordx4 v[36:39], v193, s[36:39], s74 offen offset:2048 sc1\n"
      "v_cvt_pk_bf16_f32 v209, v122, v123\n"
      "v_cvt_pk_bf16_f32 v210, v124, v125\n"
      "v_cvt_pk_bf16_f32 v211, v126, v127\n"
      "v_mfma_f32_32x32x16_bf16 v[128:143], v[176:179], v[104:107], v[128:143]\n"
      "v_and_or_b32 v208, v208, -2, s97\n"
      "v_and_or_b32 v209, v209, -2, s97\n"
      "v_and_or_b32 v210, v210, -2, s97\n"
      "v_mfma_f32_32x32x16_bf16 v[128:143], v[180:183], v[108:111], v[128:143]\n"
      "buffer_load_dwordx4 v[40:43], v194, s[36:39], s74 offen offset:2048 sc1\n"
      "v_and_or_b32 v211, v211, -2, s97\n"
      "v_mfma_f32_32x32x16_bf16 v[128:143], v[212:215], v[220:223], v[128:143]\n"
      "buffer_store_dwordx4 v[208:211], v224, s[36:39], s87 offen sc1\n"
      ".Ljn197:\n"
      "; G4\n"
      "s_waitcnt vmcnt(10)\n"
      "v_bitop3_b32 v217, v0, v1, s97 bitop3:0x7e\n"
      "v_bitop3_b32 v216, v2, v3, s97 bitop3:0x7e\n"
      "v_bitop3_b32 v196, v4, v5, s97 bitop3:0x7e\n"
      "v_or3_b32 v217, v217, v216, v196\n"
      "v_bitop3_b32 v216, v6, v7, s97 bitop3:0x7e\n"
      "v_bitop3_b32 v217, v217, v216, 1 bitop3:0xa8\n"
      "v_cmp_ne_u32 vcc, 0, v217\n"
      "s_cbranch_vccnz .Lst198\n"
      ".Lco199:\n"
      "v_mfma_f32_32x32x16_bf16 v[112:127], a[0:3], v[0:3], 0\n"
      "buffer_load_dwordx4 v[44:47], v195, s[36:39], s74 offen offset:2048 sc1\n"
      "s_waitcnt vmcnt(9)\n"
      "v_bitop3_b32 v217, v8, v9, s97 bitop3:0x7e\n"
      "v_bitop3_b32 v216, v10, v11, s97 bitop3:0x7e\n"
      "v_bitop3_b32 v196, v12, v13, s97 bitop3:0x7e\n"
      "v_mul_f32 v200, v218, v144\n"
      "v_mul_f32 v201, v218, v145\n"
      "v_mul_f32 v202, v218, v146\n"
      "v_mul_f32 v203, v218, v147\n"
      "v_mul_f32 v204, v218, v148\n"
      "v_mfma_f32_32x32x16_bf16 v[160:175], a[4:7], v[0:3], 0\n"
      "v_or3_b32 v217, v217, v216, v196\n"
      "v_bitop3_b32 v216, v14, v15, s97 bitop3:0x7e\n"
      "v_bitop3_b32 v217, v217, v216, 1 bitop3:0xa8\n"
      "v_cmp_ne_u32 vcc, 0, v217\n"
      "v_mul_f32 v205, v218, v149\n"
      "v_mul_f32 v206, v218, v150\n"
      "v_mul_f32 v207, v218, v151\n"
      "v_exp_f32 v200, v200\n"
      "v_mul_f32 v152, v144, v152\n"
      "v_mfma_f32_32x32x16_bf16 v[112:127], a[56:59], v[4:7], v[112:127]\n"
      "buffer_load_dwordx4 v[48:51], v192, s[36:39], s74 offen offset:3072 sc1\n"
      "s_cbranch_vccnz .Lst202\n"
      ".Lco203:\n"
      "v_exp_f32 v201, v201\n"
      "v_mul_f32 v153, v145, v153\n"
      "v_exp_f32 v202, v202\n"
      "v_mfma_f32_32x32x16_bf16 v[160:175], a[60:63], v[4:7], v[160:175]\n"
      "v_mul_f32 v154, v146, v154\n"
      "v_exp_f32 v203, v203\n"
      "v_mul_f32 v155, v147, v155\n"
      "v_exp_f32 v204, v204\n"
      "v_mfma_f32_32x32x16_bf16 v[112:127], a[112:115], v[8:11], v[112:127]\n"
      "buffer_load_dwordx4 v[52:55], v193, s[36:39], s74 offen offset:3072 sc1\n"
      "s_waitcnt vmcnt(9)\n"
      "v_bitop3_b32 v217, v16, v17, s97 bitop3:0x7e\n"
      "v_bitop3_b32 v216, v18, v19, s97 bitop3:0x7e\n"
      "v_bitop3_b32 v196, v20, v21, s97 bitop3:0x7e\n"
      "v_mul_f32 v156, v148, v156\n"
      "v_exp_f32 v205, v205\n"
      "v_mul_f32 v157, v149, v157\n"
      "v_mfma_f32_32x32x16_bf16 v[160:175], a[116:119], v[8:11], v[160:175]\n"
      "v_or3_b32 v217, v217, v216, v196\n"
      "v_bitop3_b32 v216, v22, v23, s97 bitop3:0x7e\n"
      "v_bitop3_b32 v217, v217, v216, 1 bitop3:0xa8\n"
      "v_cmp_ne_u32 vcc, 0, v217\n"
      "v_exp_f32 v206, v206\n"
      "v_mul_f32 v158, v150, v158\n"
      "v_exp_f32 v207, v207\n"
      "v_mul_f32 v159, v151, v159\n"
      "v_mfma_f32_32x32x16_bf16 v[112:127], a[168:171], v[12:15], v[112:127]\n"
      "buffer_load_dwordx4 v[56:59], v194, s[36:39], s74 offen offset:3072 sc1\n"
      "s_cbranch_vccnz .Lst206\n"
      ".Lco207:\n"
      "v_fma_f32 v200, v200, v219, v219\n"
      "v_fma_f32 v201, v201, v219, v219\n"
      "v_fma_f32 v202, v202, v219, v219\n"
      "v_fma_f32 v203, v203, v219, v219\n"
      "v_fma_f32 v204, v204, v219, v219\n"
      "v_mfma_f32_32x32x16_bf16 v[160:175], a[172:175], v[12:15], v[160:175]\n"
      "v_fma_f32 v205, v205, v219, v219\n"
      "v_fma_f32 v206, v206, v219, v219\n"
      "v_fma_f32 v207, v207, v219, v219\n"
      "v_rcp_f32 v200, v200\n"
      "v_mfma_f32_32x32x16_bf16 v[112:127], a[8:11], v[16:19], v[112:127]\n"
      "buffer_load_dwordx4 v[60:63], v195, s[36:39], s74 offen offset:3072 sc1\n"
      "s_waitcnt vmcnt(9)\n"
      "v_bitop3_b32 v217, v24, v25, s97 bitop3:0x7e\n"
      "v_bitop3_b32 v216, v26, v27, s97 bitop3:0x7e\n"
      "v_bitop3_b32 v196, v28, v29, s97 bitop3:0x7e\n"
      "v_rcp_f32 v201, v201\n"
      "v_mul_f32 v152, v152, v200\n"
      "v_rcp_f32 v202, v202\n"
      "v_mul_f32 v153, v153, v201\n"
      "v_mfma_f32_32x32x16_bf16 v[160:175], a[12:15], v[16:19], v[160:175]\n"
      "v_or3_b32 v217, v217, v216, v196\n"
      "v_bitop3_b32 v216, v30, v31, s97 bitop3:0x7e\n"
      "v_bitop3_b32 v217, v217, v216, 1 bitop3:0xa8\n"
      "v_cmp_ne_u32 vcc, 0, v217\n"
      "v_rcp_f32 v203, v203\n"
      "v_mul_f32 v154, v154, v202\n"
      "v_rcp_f32 v204, v204\n"
      "v_mfma_f32_32x32x16_bf16 v[112:127], a[64:67], v[20:23], v[112:127]\n"
      "buffer_load_dwordx4 v[64:67], v192, s[36:39], s89 offen sc1\n"
      "s_cbranch_vccnz .Lst210\n"
      ".Lco211:\n"
      "v_mul_f32 v155, v155, v203\n"
      "v_rcp_f32 v205, v205\n"
      "v_mul_f32 v156, v156, v204\n"
      "v_rcp_f32 v206, v206\n"
      "v_mfma_f32_32x32x16_bf16 v[160:175], a[68:71], v[20:23], v[160:175]\n"
      "v_mul_f32 v157, v157, v205\n"
      "v_rcp_f32 v207, v207\n"
      "v_mul_f32 v158, v158, v206\n"
      "s_add_u32 s87, s73, 0x1400\n"
      "v_mfma_f32_32x32x16_bf16 v[112:127], a[120:123], v[24:27], v[112:127]\n"
      "buffer_load_dwordx4 v[68:71], v193, s[36:39], s89 offen sc1\n"
      "s_waitcnt vmcnt(9)\n"
      "v_bitop3_b32 v217, v32, v33, s97 bitop3:0x7e\n"
      "v_bitop3_b32 v216, v34, v35, s97 bitop3:0x7e\n"
      "v_bitop3_b32 v196, v36, v37, s97 bitop3:0x7e\n"
      "v_mul_f32 v159, v159, v207\n"
      "v_cvt_pk_bf16_f32 v208, v152, v153\n"
      "v_cvt_pk_bf16_f32 v209, v154, v155\n"
      "v_cvt_pk_bf16_f32 v210, v156, v157\n"
      "v_cvt_pk_bf16_f32 v211, v158, v159\n"
      "v_mfma_f32_32x32x16_bf16 v[160:175], a[124:127], v[24:27], v[160:175]\n"
      "v_or3_b32 v217, v217, v216, v196\n"
      "v_bitop3_b32 v216, v38, v39, s97 bitop3:0x7e\n"
      "v_bitop3_b32 v217, v217, v216, 1 bitop3:0xa8\n"
      "v_cmp_ne_u32 vcc, 0, v217\n"
      "v_and_or_b32 v208, v208, -2, s97\n"
      "v_and_or_b32 v209, v209, -2, s97\n"
      "v_and_or_b32 v210, v210, -2, s97\n"
      "v_and_or_b32 v211, v211, -2, s97\n"
      "v_mfma_f32_32x32x16_bf16 v[112:127], a[176:179], v[28:31], v[112:127]\n"
      "buffer_load_dwordx4 v[72:75], v194, s[36:39], s89 offen sc1\n"
      "s_cbranch_vccnz .Lst214\n"
      ".Lco215:\n"
      "buffer_store_dwordx4 v[208:211], v224, s[36:39], s87 offen sc1\n"
      "v_mul_f32 v200, v218, v128\n"
      "v_mul_f32 v201, v218, v129\n"
      "v_mul_f32 v202, v218, v130\n"
      "v_mfma_f32_32x32x16_bf16 v[160:175], a[180:183], v[28:31], v[160:175]\n"
      "v_mul_f32 v203, v218, v131\n"
      "v_mul_f32 v204, v218, v132\n"
      "v_mul_f32 v205, v218, v133\n"
      "v_mul_f32 v206, v218, v134\n"
      "v_mul_f32 v207, v218, v135\n"
      "v_mfma_f32_32x32x16_bf16 v[112:127], a[16:19], v[32:35], v[112:127]\n"
      "buffer_load_dwordx4 v[76:79], v195, s[36:39], s89 offen sc1\n"
      "s_waitcnt vmcnt(9)\n"
      "v_bitop3_b32 v217, v40, v41, s97 bitop3:0x7e\n"
      "v_bitop3_b32 v216, v42, v43, s97 bitop3:0x7e\n"
      "v_bitop3_b32 v196, v44, v45, s97 bitop3:0x7e\n"
      "v_exp_f32 v200, v200\n"
      "v_mul_f32 v136, v128, v136\n"
      "v_exp_f32 v201, v201\n"
      "v_mfma_f32_32x32x16_bf16 v[160:175], a[20:23], v[32:35], v[160:175]\n"
      "v_or3_b32 v217, v217, v216, v196\n"
      "v_bitop3_b32 v216, v46, v47, s97 bitop3:0x7e\n"
      "v_bitop3_b32 v217, v217, v216, 1 bitop3:0xa8\n"
      "v_cmp_ne_u32 vcc, 0, v217\n"
      "v_mul_f32 v137, v129, v137\n"
      "v_exp_f32 v202, v202\n"
      "v_mul_f32 v138, v130, v138\n"
      "v_exp_f32 v203, v203\n"
      "v_mfma_f32_32x32x16_bf16 v[112:127], a[72:75], v[36:39], v[112:127]\n"
      "buffer_load_dwordx4 v[80:83], v192, s[36:39], s89 offen offset:1024 sc1\n"
      "s_cbranch_vccnz .Lst218\n"
      ".Lco219:\n"
      "v_mul_f32 v139, v131, v139\n"
      "v_exp_f32 v204, v204\n"
      "v_mul_f32 v140, v132, v140\n"
      "v_mfma_f32_32x32x16_bf16 v[160:175], a[76:79], v[36:39], v[160:175]\n"
      "v_exp_f32 v205, v205\n"
      "v_mul_f32 v141, v133, v141\n"
      "v_exp_f32 v206, v206\n"
      "v_mul_f32 v142, v134, v142\n"
      "v_mfma_f32_32x32x16_bf16 v[112:127], a[128:131], v[40:43], v[112:127]\n"
      "buffer_load_dwordx4 v[84:87], v193, s[36:39], s89 offen offset:1024 sc1\n"
      "s_waitcnt vmcnt(9)\n"
      "v_bitop3_b32 v217, v48, v49, s97 bitop3:0x7e\n"
      "v_bitop3_b32 v216, v50, v51, s97 bitop3:0x7e\n"
      "v_bitop3_b32 v196, v52, v53, s97 bitop3:0x7e\n"
      "v_exp_f32 v207, v207\n"
      "v_mul_f32 v143, v135, v143\n"
      "v_fma_f32 v200, v200, v219, v219\n"
      "v_fma_f32 v201, v201, v219, v219\n"
      "v_fma_f32 v202, v202, v219, v219\n"
      "v_mfma_f32_32x32x16_bf16 v[160:175], a[132:135], v[40:43], v[160:175]\n"
      "v_or3_b32 v217, v217, v216, v196\n"
      "v_bitop3_b32 v216, v54, v55, s97 bitop3:0x7e\n"
      "v_bitop3_b32 v217, v217, v216, 1 bitop3:0xa8\n"
      "v_cmp_ne_u32 vcc, 0, v217\n"
      "v_fma_f32 v203, v203, v219, v219\n"
      "v_fma_f32 v204, v204, v219, v219\n"
      "v_fma_f32 v205, v205, v219, v219\n"
      "v_fma_f32 v206, v206, v219, v219\n"
      "v_fma_f32 v207, v207, v219, v219\n"
      "v_mfma_f32_32x32x16_bf16 v[112:127], a[184:187], v[44:47], v[112:127]\n"
      "buffer_load_dwordx4 v[88:91], v194, s[36:39], s89 offen offset:1024 sc1\n"
      "s_cbranch_vccnz .Lst222\n"
      ".Lco223:\n"
      "v_rcp_f32 v200, v200\n"
      "v_rcp_f32 v201, v201\n"
      "v_mul_f32 v136, v136, v200\n"
      "v_mfma_f32_32x32x16_bf16 v[160:175], a[188:191], v[44:47], v[160:175]\n"
      "v_rcp_f32 v202, v202\n"
      "v_mul_f32 v137, v137, v201\n"
      "v_rcp_f32 v203, v203\n"
      "v_mul_f32 v138, v138, v202\n"
      "v_mfma_f32_32x32x16_bf16 v[112:127], a[24:27], v[48:51], v[112:127]\n"
      "buffer_load_dwordx4 v[92:95], v195, s[36:39], s89 offen offset:1024 sc1\n"
      "s_waitcnt vmcnt(9)\n"
      "v_bitop3_b32 v217, v56, v57, s97 bitop3:0x7e\n"
      "v_bitop3_b32 v216, v58, v59, s97 bitop3:0x7e\n"
      "v_bitop3_b32 v196, v60, v61, s97 bitop3:0x7e\n"
      "v_rcp_f32 v204, v204\n"
      "v_mul_f32 v139, v139, v203\n"
      "v_rcp_f32 v205, v205\n"
      "v_mfma_f32_32x32x16_bf16 v[160:175], a[28:31], v[48:51], v[160:175]\n"
      "v_or3_b32 v217, v217, v216, v196\n"
      "v_bitop3_b32 v216, v62, v63, s97 bitop3:0x7e\n"
      "v_bitop3_b32 v217, v217, v216, 1 bitop3:0xa8\n"
      "v_cmp_ne_u32 vcc, 0, v217\n"
      "v_mul_f32 v140, v140, v204\n"
      "v_rcp_f32 v206, v206\n"
      "v_mul_f32 v141, v141, v205\n"
      "v_rcp_f32 v207, v207\n"
      "v_mfma_f32_32x32x16_bf16 v[112:127], a[80:83], v[52:55], v[112:127]\n"
      "buffer_load_dwordx4 v[96:99], v192, s[36:39], s89 offen offset:2048 sc1\n"
      "s_cbranch_vccnz .Lst226\n"
      ".Lco227:\n"
      "v_mul_f32 v142, v142, v206\n"
      "s_add_u32 s87, s73, 0x1800\n"
      "v_mul_f32 v143, v143, v207\n"
      "v_cvt_pk_bf16_f32 v208, v136, v137\n"
      "v_mfma_f32_32x32x16_bf16 v[160:175], a[84:87], v[52:55], v[160:175]\n"
      "v_cvt_pk_bf16_f32 v209, v138, v139\n"
      "v_cvt_pk_bf16_f32 v210, v140, v141\n"
      "v_cvt_pk_bf16_f32 v211, v142, v143\n"
      "v_and_or_b32 v208, v208, -2, s97\n"
      "v_and_or_b32 v209, v209, -2, s97\n"
      "v_and_or_b32 v210, v210, -2, s97\n"
      "v_mfma_f32_32x32x16_bf16 v[112:127], a[136:139], v[56:59], v[112:127]\n"
      "buffer_load_dwordx4 v[100:103], v193, s[36:39], s89 offen offset:2048 sc1\n"
      "s_waitcnt vmcnt(9)\n"
      "v_bitop3_b32 v217, v64, v65, s97 bitop3:0x7e\n"
      "v_bitop3_b32 v216, v66, v67, s97 bitop3:0x7e\n"
      "v_bitop3_b32 v196, v68, v69, s97 bitop3:0x7e\n"
      "v_and_or_b32 v211, v211, -2, s97\n"
      "buffer_store_dwordx4 v[208:211], v224, s[36:39], s87 offen sc1\n"
      "v_mfma_f32_32x32x16_bf16 v[160:175], a[140:143], v[56:59], v[160:175]\n"
      "v_or3_b32 v217, v217, v216, v196\n"
      "v_bitop3_b32 v216, v70, v71, s97 bitop3:0x7e\n"
      "v_bitop3_b32 v217, v217, v216, 1 bitop3:0xa8\n"
      "v_cmp_ne_u32 vcc, 0, v217\n"
      "v_lshlrev_b32 v128, 16, v240\n"
      "v_and_b32 v129, 0xffff0000, v240\n"
      "v_lshlrev_b32 v130, 16, v241\n"
      "v_and_b32 v131, 0xffff0000, v241\n"
      "v_lshlrev_b32 v132, 16, v242\n"
      "v_mfma_f32_32x32x16_bf16 v[112:127], a[192:195], v[60:63], v[112:127]\n"
      "buffer_load_dwordx4 v[104:107], v194, s[36:39], s89 offen offset:2048 sc1\n"
      "s_cbranch_vccnz .Lst230\n"
      ".Lco231:\n"
      "v_and_b32 v133, 0xffff0000, v242\n"
      "v_lshlrev_b32 v134, 16, v243\n"
      "v_and_b32 v135, 0xffff0000, v243\n"
      "v_lshlrev_b32 v136, 16, v244\n"
      "v_and_b32 v137, 0xffff0000, v244\n"
      "v_lshlrev_b32 v138, 16, v245\n"
      "v_mfma_f32_32x32x16_bf16 v[160:175], a[196:199], v[60:63], v[160:175]\n"
      "v_and_b32 v139, 0xffff0000, v245\n"
      "v_lshlrev_b32 v140, 16, v246\n"
      "v_and_b32 v141, 0xffff0000, v246\n"
      "v_lshlrev_b32 v142, 16, v247\n"
      "v_and_b32 v143, 0xffff0000, v247\n"
      "s_nop 1\n"
      "v_mfma_f32_32x32x16_bf16 v[128:143], a[0:3], v[64:67], v[128:143]\n"
      "buffer_load_dwordx4 v[108:111], v195, s[36:39], s89 offen offset:2048 sc1\n"
      "s_waitcnt vmcnt(9)\n"
      "v_bitop3_b32 v217, v72, v73, s97 bitop3:0x7e\n"
      "v_bitop3_b32 v216, v74, v75, s97 bitop3:0x7e\n"
      "v_bitop3_b32 v196, v76, v77, s97 bitop3:0x7e\n"
      "v_mfma_f32_32x32x16_bf16 v[176:191], a[4:7], v[64:67], 0\n"
      "v_or3_b32 v217, v217, v216, v196\n"
      "v_bitop3_b32 v216, v78, v79, s97 bitop3:0x7e\n"
      "v_bitop3_b32 v217, v217, v216, 1 bitop3:0xa8\n"
      "v_cmp_ne_u32 vcc, 0, v217\n"
      "v_mfma_f32_32x32x16_bf16 v[128:143], a[56:59], v[68:71], v[128:143]\n"
      "buffer_load_dwordx4 v[0:3], v192, s[36:39], s89 offen offset:3072 sc1\n"
      "s_cbranch_vccnz .Lst234\n"
      ".Lco235:\n"
      "v_mfma_f32_32x32x16_bf16 v[176:191], a[60:63], v[68:71], v[176:191]\n"
      "s_barrier\n"
      "v_mfma_f32_32x32x16_bf16 v[128:143], a[112:115], v[72:75], v[128:143]\n"
      "buffer_load_dwordx4 v[4:7], v193, s[36:39], s89 offen offset:3072 sc1\n"
      "s_waitcnt vmcnt(9)\n"
      "v_bitop3_b32 v217, v80, v81, s97 bitop3:0x7e\n"
      "v_bitop3_b32 v216, v82, v83, s97 bitop3:0x7e\n"
      "v_bitop3_b32 v196, v84, v85, s97 bitop3:0x7e\n"
      "v_mfma_f32_32x32x16_bf16 v[176:191], a[116:119], v[72:75], v[176:191]\n"
      "v_or3_b32 v217, v217, v216, v196\n"
      "v_bitop3_b32 v216, v86, v87, s97 bitop3:0x7e\n"
      "v_bitop3_b32 v217, v217, v216, 1 bitop3:0xa8\n"
      "v_cmp_ne_u32 vcc, 0, v217\n"
      "v_mfma_f32_32x32x16_bf16 v[128:143], a[168:171], v[76:79], v[128:143]\n"
      "buffer_load_dwordx4 v[8:11], v194, s[36:39], s89 offen offset:3072 sc1\n"
      "s_cbranch_vccnz .Lst238\n"
      ".Lco239:\n"
      "v_mfma_f32_32x32x16_bf16 v[176:191], a[172:175], v[76:79], v[176:191]\n"
      "v_mfma_f32_32x32x16_bf16 v[128:143], a[8:11], v[80:83], v[128:143]\n"
      "s_waitcnt vmcnt(8)\n"
      "v_bitop3_b32 v217, v88, v89, s97 bitop3:0x7e\n"
      "v_bitop3_b32 v216, v90, v91, s97 bitop3:0x7e\n"
      "v_bitop3_b32 v196, v92, v93, s97 bitop3:0x7e\n"
      "v_mfma_f32_32x32x16_bf16 v[176:191], a[12:15], v[80:83], v[176:191]\n"
      "buffer_load_dwordx4 v[12:15], v195, s[36:39], s89 offen offset:3072 sc1\n"
      "v_or3_b32 v217, v217, v216, v196\n"
      "v_bitop3_b32 v216, v94, v95, s97 bitop3:0x7e\n"
      "v_bitop3_b32 v217, v217, v216, 1 bitop3:0xa8\n"
      "v_cmp_ne_u32 vcc, 0, v217\n"
      "v_mfma_f32_32x32x16_bf16 v[128:143], a[64:67], v[84:87], v[128:143]\n"
      "s_cbranch_vccnz .Lst242\n"
      ".Lco243:\n"
      "v_mfma_f32_32x32x16_bf16 v[176:191], a[68:71], v[84:87], v[176:191]\n"
      "buffer_load_dwordx4 v[16:19], v192, s[36:39], s78 offen sc1\n"
      "v_mfma_f32_32x32x16_bf16 v[128:143], a[120:123], v[88:91], v[128:143]\n"
      "s_waitcnt vmcnt(8)\n"
      "v_bitop3_b32 v217, v96, v97, s97 bitop3:0x7e\n"
      "v_bitop3_b32 v216, v98, v99, s97 bitop3:0x7e\n"
      "v_bitop3_b32 v196, v100, v101, s97 bitop3:0x7e\n"
      "v_mfma_f32_32x32x16_bf16 v[176:191], a[124:127], v[88:91], v[176:191]\n"
      "buffer_load_dwordx4 v[20:23], v193, s[36:39], s78 offen sc1\n"
      "v_or3_b32 v217, v217, v216, v196\n"
      "v_bitop3_b32 v216, v102, v103, s97 bitop3:0x7e\n"
      "v_bitop3_b32 v217, v217, v216, 1 bitop3:0xa8\n"
      "v_cmp_ne_u32 vcc, 0, v217\n"
      "v_mfma_f32_32x32x16_bf16 v[128:143], a[176:179], v[92:95], v[128:143]\n"
      "s_cbranch_vccnz .Lst246\n"
      ".Lco247:\n"
      "v_mfma_f32_32x32x16_bf16 v[176:191], a[180:183], v[92:95], v[176:191]\n"
      "buffer_load_dwordx4 v[24:27], v194, s[36:39], s78 offen sc1\n"
      "v_mfma_f32_32x32x16_bf16 v[128:143], a[16:19], v[96:99], v[128:143]\n"
      "s_waitcnt vmcnt(7)\n"
      "v_bitop3_b32 v217, v104, v105, s97 bitop3:0x7e\n"
      "v_bitop3_b32 v216, v106, v107, s97 bitop3:0x7e\n"
      "v_bitop3_b32 v196, v108, v109, s97 bitop3:0x7e\n"
      "v_mfma_f32_32x32x16_bf16 v[176:191], a[20:23], v[96:99], v[176:191]\n"
      "buffer_load_dwordx4 v[28:31], v195, s[36:39], s78 offen sc1\n"
      "v_or3_b32 v217, v217, v216, v196\n"
      "v_bitop3_b32 v216, v110, v111, s97 bitop3:0x7e\n"
      "v_bitop3_b32 v217, v217, v216, 1 bitop3:0xa8\n"
      "v_cmp_ne_u32 vcc, 0, v217\n"
      "v_mfma_f32_32x32x16_bf16 v[128:143], a[72:75], v[100:103], v[128:143]\n"
      "s_cbranch_vccnz .Lst250\n"
      ".Lco251:\n"
      "v_mfma_f32_32x32x16_bf16 v[176:191], a[76:79], v[100:103], v[176:191]\n"
      "buffer_load_dwordx4 v[32:35], v192, s[36:39], s79 offen sc1\n"
      "v_mfma_f32_32x32x16_bf16 v[128:143], a[128:131], v[104:107], v[128:143]\n"
      "s_waitcnt vmcnt(7)\n"
      "v_bitop3_b32 v217, v0, v1, s97 bitop3:0x7e\n"
      "v_bitop3_b32 v216, v2, v3, s97 bitop3:0x7e\n"
      "v_bitop3_b32 v196, v4, v5, s97 bitop3:0x7e\n"
      "v_mfma_f32_32x32x16_bf16 v[176:191], a[132:135], v[104:107], v[176:191]\n"
      "buffer_load_dwordx4 v[36:39], v193, s[36:39], s79 offen sc1\n"
      "v_or3_b32 v217, v217, v216, v196\n"
      "v_bitop3_b32 v216, v6, v7, s97 bitop3:0x7e\n"
      "v_bitop3_b32 v217, v217, v216, 1 bitop3:0xa8\n"
      "v_cmp_ne_u32 vcc, 0, v217\n"
      "v_mfma_f32_32x32x16_bf16 v[128:143], a[184:187], v[108:111], v[128:143]\n"
      "s_cbranch_vccnz .Lst254\n"
      ".Lco255:\n"
      "v_mfma_f32_32x32x16_bf16 v[176:191], a[188:191], v[108:111], v[176:191]\n"
      "buffer_load_dwordx4 v[40:43], v194, s[36:39], s79 offen sc1\n"
      "v_mfma_f32_32x32x16_bf16 v[128:143], a[24:27], v[0:3], v[128:143]\n"
      "s_waitcnt vmcnt(7)\n"
      "v_bitop3_b32 v217, v8, v9, s97 bitop3:0x7e\n"
      "v_bitop3_b32 v216, v10, v11, s97 bitop3:0x7e\n"
      "v_bitop3_b32 v196, v12, v13, s97 bitop3:0x7e\n"
      "v_mfma_f32_32x32x16_bf16 v[176:191], a[28:31], v[0:3], v[176:191]\n"
      "buffer_load_dwordx4 v[44:47], v195, s[36:39], s79 offen sc1\n"
      "v_or3_b32 v217, v217, v216, v196\n"
      "v_bitop3_b32 v216, v14, v15, s97 bitop3:0x7e\n"
      "v_bitop3_b32 v217, v217, v216, 1 bitop3:0xa8\n"
      "v_cmp_ne_u32 vcc, 0, v217\n"
      "v_mfma_f32_32x32x16_bf16 v[128:143], a[80:83], v[4:7], v[128:143]\n"
      "s_cbranch_vccnz .Lst258\n"
      ".Lco259:\n"
      "v_mfma_f32_32x32x16_bf16 v[176:191], a[84:87], v[4:7], v[176:191]\n"
      "buffer_load_dwordx4 v[48:51], v192, s[36:39], s78 offen offset:1024 sc1\n"
      "v_mfma_f32_32x32x16_bf16 v[128:143], a[136:139], v[8:11], v[128:143]\n"
      "s_waitcnt vmcnt(7)\n"
      "v_bitop3_b32 v217, v16, v17, s97 bitop3:0x7e\n"
      "v_bitop3_b32 v216, v18, v19, s97 bitop3:0x7e\n"
      "v_bitop3_b32 v196, v20, v21, s97 bitop3:0x7e\n"
      "v_mfma_f32_32x32x16_bf16 v[176:191], a[140:143], v[8:11], v[176:191]\n"
      "buffer_load_dwordx4 v[52:55], v193, s[36:39], s78 offen offset:1024 sc1\n"
      "v_or3_b32 v217, v217, v216, v196\n"
      "v_bitop3_b32 v216, v22, v23, s97 bitop3:0x7e\n"
      "v_bitop3_b32 v217, v217, v216, 1 bitop3:0xa8\n"
      "v_cmp_ne_u32 vcc, 0, v217\n"
      "v_mfma_f32_32x32x16_bf16 v[128:143], a[192:195], v[12:15], v[128:143]\n"
      "s_cbranch_vccnz .Lst262\n"
      ".Lco263:\n"
      "v_mfma_f32_32x32x16_bf16 v[176:191], a[196:199], v[12:15], v[176:191]\n"
      "buffer_load_dwordx4 v[56:59], v194, s[36:39], s78 offen offset:1024 sc1\n"
      "v_mfma_f32_32x32x16_bf16 v[112:127], a[32:35], v[16:19], v[112:127]\n"
      "s_waitcnt vmcnt(7)\n"
      "v_bitop3_b32 v217, v24, v25, s97 bitop3:0x7e\n"
      "v_bitop3_b32 v216, v26, v27, s97 bitop3:0x7e\n"
      "v_bitop3_b32 v196, v28, v29, s97 bitop3:0x7e\n"
      "v_mfma_f32_32x32x16_bf16 v[160:175], a[36:39], v[16:19], v[160:175]\n"
      "buffer_load_dwordx4 v[60:63], v195, s[36:39], s78 offen offset:1024 sc1\n"
      "v_or3_b32 v217, v217, v216, v196\n"
      "v_bitop3_b32 v216, v30, v31, s97 bitop3:0x7e\n"
      "v_bitop3_b32 v217, v217, v216, 1 bitop3:0xa8\n"
      "v_cmp_ne_u32 vcc, 0, v217\n"
      "v_mfma_f32_32x32x16_bf16 v[112:127], a[88:91], v[20:23], v[112:127]\n"
      "s_cbranch_vccnz .Lst266\n"
      ".Lco267:\n"
      "v_mfma_f32_32x32x16_bf16 v[160:175], a[92:95], v[20:23], v[160:175]\n"
      "buffer_load_dwordx4 v[64:67], v192, s[36:39], s78 offen offset:2048 sc1\n"
      "v_mfma_f32_32x32x16_bf16 v[112:127], a[144:147], v[24:27], v[112:127]\n"
      "s_waitcnt vmcnt(7)\n"
      "v_bitop3_b32 v217, v32, v33, s97 bitop3:0x7e\n"
      "v_bitop3_b32 v216, v34, v35, s97 bitop3:0x7e\n"
      "v_bitop3_b32 v196, v36, v37, s97 bitop3:0x7e\n"
      "v_mfma_f32_32x32x16_bf16 v[160:175], a[148:151], v[24:27], v[160:175]\n"
      "buffer_load_dwordx4 v[68:71], v193, s[36:39], s78 offen offset:2048 sc1\n"
      "v_or3_b32 v217, v217, v216, v196\n"
      "v_bitop3_b32 v216, v38, v39, s97 bitop3:0x7e\n"
      "v_bitop3_b32 v217, v217, v216, 1 bitop3:0xa8\n"
      "v_cmp_ne_u32 vcc, 0, v217\n"
      "v_mfma_f32_32x32x16_bf16 v[112:127], a[200:203], v[28:31], v[112:127]\n"
      "s_cbranch_vccnz .Lst270\n"
      ".Lco271:\n"
      "v_mfma_f32_32x32x16_bf16 v[160:175], a[204:207], v[28:31], v[160:175]\n"
      "buffer_load_dwordx4 v[72:75], v194, s[36:39], s78 offen offset:2048 sc1\n"
      "v_mfma_f32_32x32x16_bf16 v[128:143], a[32:35], v[32:35], v[128:143]\n"
      "s_waitcnt vmcnt(7)\n"
      "v_bitop3_b32 v217, v40, v41, s97 bitop3:0x7e\n"
      "v_bitop3_b32 v216, v42, v43, s97 bitop3:0x7e\n"
      "v_bitop3_b32 v196, v44, v45, s97 bitop3:0x7e\n"
      "v_mfma_f32_32x32x16_bf16 v[176:191], a[36:39], v[32:35], v[176:191]\n"
      "buffer_load_dwordx4 v[76:79], v195, s[36:39], s78 offen offset:2048 sc1\n"
      "v_or3_b32 v217, v217, v216, v196\n"
      "v_bitop3_b32 v216, v46, v47, s97 bitop3:0x7e\n"
      "v_bitop3_b32 v217, v217, v216, 1 bitop3:0xa8\n"
      "v_cmp_ne_u32 vcc, 0, v217\n"
      "v_mfma_f32_32x32x16_bf16 v[128:143], a[88:91], v[36:39], v[128:143]\n"
      "s_cbranch_vccnz .Lst274\n"
      ".Lco275:\n"
      "v_mfma_f32_32x32x16_bf16 v[176:191], a[92:95], v[36:39], v[176:191]\n"
      "buffer_load_dwordx4 v[80:83], v192, s[36:39], s79 offen offset:1024 sc1\n"
      "v_mfma_f32_32x32x16_bf16 v[128:143], a[144:147], v[40:43], v[128:143]\n"
      "s_waitcnt vmcnt(7)\n"
      "v_bitop3_b32 v217, v48, v49, s97 bitop3:0x7e\n"
      "v_bitop3_b32 v216, v50, v51, s97 bitop3:0x7e\n"
      "v_bitop3_b32 v196, v52, v53, s97 bitop3:0x7e\n"
      "v_mfma_f32_32x32x16_bf16 v[176:191], a[148:151], v[40:43], v[176:191]\n"
      "buffer_load_dwordx4 v[84:87], v193, s[36:39], s79 offen offset:1024 sc1\n"
      "v_or3_b32 v217, v217, v216, v196\n"
      "v_bitop3_b32 v216, v54, v55, s97 bitop3:0x7e\n"
      "v_bitop3_b32 v217, v217, v216, 1 bitop3:0xa8\n"
      "v_cmp_ne_u32 vcc, 0, v217\n"
      "v_mfma_f32_32x32x16_bf16 v[128:143], a[200:203], v[44:47], v[128:143]\n"
      "s_cbranch_vccnz .Lst278\n"
      ".Lco279:\n"
      "v_mfma_f32_32x32x16_bf16 v[176:191], a[204:207], v[44:47], v[176:191]\n"
      "buffer_load_dwordx4 v[88:91], v194, s[36:39], s79 offen offset:1024 sc1\n"
      "v_mfma_f32_32x32x16_bf16 v[112:127], a[40:43], v[48:51], v[112:127]\n"
      "s_waitcnt vmcnt(7)\n"
      "v_bitop3_b32 v217, v56, v57, s97 bitop3:0x7e\n"
      "v_bitop3_b32 v216, v58, v59, s97 bitop3:0x7e\n"
      "v_bitop3_b32 v196, v60, v61, s97 bitop3:0x7e\n"
      "v_mfma_f32_32x32x16_bf16 v[160:175], a[44:47], v[48:51], v[160:175]\n"
      "buffer_load_dwordx4 v[92:95], v195, s[36:39], s79 offen offset:1024 sc1\n"
      "v_or3_b32 v217, v217, v216, v196\n"
      "v_bitop3_b32 v216, v62, v63, s97 bitop3:0x7e\n"
      "v_bitop3_b32 v217, v217, v216, 1 bitop3:0xa8\n"
      "v_cmp_ne_u32 vcc, 0, v217\n"
      "v_mfma_f32_32x32x16_bf16 v[112:127], a[96:99], v[52:55], v[112:127]\n"
      "s_cbranch_vccnz .Lst282\n"
      ".Lco283:\n"
      "v_mfma_f32_32x32x16_bf16 v[160:175], a[100:103], v[52:55], v[160:175]\n"
      "buffer_load_dwordx4 v[96:99], v192, s[36:39], s79 offen offset:2048 sc1\n"
      "v_mfma_f32_32x32x16_bf16 v[112:127], a[152:155], v[56:59], v[112:127]\n"
      "s_waitcnt vmcnt(7)\n"
      "v_bitop3_b32 v217, v64, v65, s97 bitop3:0x7e\n"
      "v_bitop3_b32 v216, v66, v67, s97 bitop3:0x7e\n"
      "v_bitop3_b32 v196, v68, v69, s97 bitop3:0x7e\n"
      "v_mfma_f32_32x32x16_bf16 v[160:175], a[156:159], v[56:59], v[160:175]\n"
      "buffer_load_dwordx4 v[100:103], v193, s[36:39], s79 offen offset:2048 sc1\n"
      "v_or3_b32 v217, v217, v216, v196\n"
      "v_bitop3_b32 v216, v70, v71, s97 bitop3:0x7e\n"
      "v_bitop3_b32 v217, v217, v216, 1 bitop3:0xa8\n"
      "v_cmp_ne_u32 vcc, 0, v217\n"
      "v_mfma_f32_32x32x16_bf16 v[112:127], a[208:211], v[60:63], v[112:127]\n"
      "s_cbranch_vccnz .Lst286\n"
      ".Lco287:\n"
      "v_mfma_f32_32x32x16_bf16 v[160:175], a[212:215], v[60:63], v[160:175]\n"
      "buffer_load_dwordx4 v[104:107], v194, s[36:39], s79 offen offset:2048 sc1\n"
      "v_mfma_f32_32x32x16_bf16 v[112:127], a[48:51], v[64:67], v[112:127]\n"
      "s_waitcnt vmcnt(7)\n"
      "v_bitop3_b32 v217, v72, v73, s97 bitop3:0x7e\n"
      "v_bitop3_b32 v216, v74, v75, s97 bitop3:0x7e\n"
      "v_bitop3_b32 v196, v76, v77, s97 bitop3:0x7e\n"
      "v_mfma_f32_32x32x16_bf16 v[160:175], a[52:55], v[64:67], v[160:175]\n"
      "buffer_load_dwordx4 v[108:111], v195, s[36:39], s79 offen offset:2048 sc1\n"
      "v_or3_b32 v217, v217, v216, v196\n"
      "v_bitop3_b32 v216, v78, v79, s97 bitop3:0x7e\n"
      "v_bitop3_b32 v217, v217, v216, 1 bitop3:0xa8\n"
      "v_cmp_ne_u32 vcc, 0, v217\n"
      "v_mfma_f32_32x32x16_bf16 v[112:127], a[104:107], v[68:71], v[112:127]\n"
      "s_cbranch_vccnz .Lst290\n"
      ".Lco291:\n"
      "v_mfma_f32_32x32x16_bf16 v[160:175], a[108:111], v[68:71], v[160:175]\n"
      "v_mfma_f32_32x32x16_bf16 v[112:127], a[160:163], v[72:75], v[112:127]\n"
      "s_waitcnt vmcnt(6)\n"
      "v_bitop3_b32 v217, v80, v81, s97 bitop3:0x7e\n"
      "v_bitop3_b32 v216, v82, v83, s97 bitop3:0x7e\n"
      "v_bitop3_b32 v196, v84, v85, s97 bitop3:0x7e\n"
      "v_mfma_f32_32x32x16_bf16 v[160:175], a[164:167], v[72:75], v[160:175]\n"
      "v_or3_b32 v217, v217, v216, v196\n"
      "v_bitop3_b32 v216, v86, v87, s97 bitop3:0x7e\n"
      "v_bitop3_b32 v217, v217, v216, 1 bitop3:0xa8\n"
      "v_cmp_ne_u32 vcc, 0, v217\n"
      "v_mfma_f32_32x32x16_bf16 v[112:127], a[216:219], v[76:79], v[112:127]\n"
      "s_cbranch_vccnz .Lst294\n"
      ".Lco295:\n"
      "v_mfma_f32_32x32x16_bf16 v[160:175], a[220:223], v[76:79], v[160:175]\n"
      "v_mfma_f32_32x32x16_bf16 v[128:143], a[40:43], v[80:83], v[128:143]\n"
      "s_waitcnt vmcnt(4)\n"
      "v_bitop3_b32 v217, v88, v89, s97 bitop3:0x7e\n"
      "v_bitop3_b32 v216, v90, v91, s97 bitop3:0x7e\n"
      "v_bitop3_b32 v196, v92, v93, s97 bitop3:0x7e\n"
      "v_mfma_f32_32x32x16_bf16 v[176:191], a[44:47], v[80:83], v[176:191]\n"
      "s_nop 3\n"
      "ds_write_b128 v250, v[112:115]\n"
      "v_or3_b32 v217, v217, v216, v196\n"
      "v_bitop3_b32 v216, v94, v95, s97 bitop3:0x7e\n"
      "v_bitop3_b32 v217, v217, v216, 1 bitop3:0xa8\n"
      "v_cmp_ne_u32 vcc, 0, v217\n"
      "v_mfma_f32_32x32x16_bf16 v[128:143], a[96:99], v[84:87], v[128:143]\n"
      "s_cbranch_vccnz .Lst298\n"
      ".Lco299:\n"
      "v_mfma_f32_32x32x16_bf16 v[176:191], a[100:103], v[84:87], v[176:191]\n"
      "ds_write_b128 v250, v[116:119] offset:1024\n"
      "v_mfma_f32_32x32x16_bf16 v[128:143], a[152:155], v[88:91], v[128:143]\n"
      "s_waitcnt vmcnt(2)\n"
      "v_bitop3_b32 v217, v96, v97, s97 bitop3:0x7e\n"
      "v_bitop3_b32 v216, v98, v99, s97 bitop3:0x7e\n"
      "v_bitop3_b32 v196, v100, v101, s97 bitop3:0x7e\n"
      "v_mfma_f32_32x32x16_bf16 v[176:191], a[156:159], v[88:91], v[176:191]\n"
      "ds_write_b128 v250, v[120:123] offset:2048\n"
      "v_or3_b32 v217, v217, v216, v196\n"
      "v_bitop3_b32 v216, v102, v103, s97 bitop3:0x7e\n"
      "v_bitop3_b32 v217, v217, v216, 1 bitop3:0xa8\n"
      "v_cmp_ne_u32 vcc, 0, v217\n"
      "v_mfma_f32_32x32x16_bf16 v[128:143], a[208:211], v[92:95], v[128:143]\n"
      "s_cbranch_vccnz .Lst302\n"
      ".Lco303:\n"
      "v_mfma_f32_32x32x16_bf16 v[176:191], a[212:215], v[92:95], v[176:191]\n"
      "ds_write_b128 v250, v[124:127] offset:3072\n"
      "v_mfma_f32_32x32x16_bf16 v[128:143], a[48:51], v[96:99], v[128:143]\n"
      "s_waitcnt vmcnt(0)\n"
      "v_bitop3_b32 v217, v104, v105, s97 bitop3:0x7e\n"
      "v_bitop3_b32 v216, v106, v107, s97 bitop3:0x7e\n"
      "v_bitop3_b32 v196, v108, v109, s97 bitop3:0x7e\n"
      "v_mfma_f32_32x32x16_bf16 v[176:191], a[52:55], v[96:99], v[176:191]\n"
      "ds_write_b128 v251, v[160:163]\n"
      "v_or3_b32 v217, v217, v216, v196\n"
      "v_bitop3_b32 v216, v110, v111, s97 bitop3:0x7e\n"
      "v_bitop3_b32 v217, v217, v216, 1 bitop3:0xa8\n"
      "v_cmp_ne_u32 vcc, 0, v217\n"
      "v_mfma_f32_32x32x16_bf16 v[128:143], a[104:107], v[100:103], v[128:143]\n"
      "s_cbranch_vccnz .Lst306\n"
      ".Lco307:\n"
      "v_mfma_f32_32x32x16_bf16 v[176:191], a[108:111], v[100:103], v[176:191]\n"
      "ds_write_b128 v251, v[164:167] offset:1024\n"
      "v_mfma_f32_32x32x16_bf16 v[176:191], a[164:167], v[104:107], v[176:191]\n"
      "ds_write_b128 v251, v[168:171] offset:2048\n"
      "v_mfma_f32_32x32x16_bf16 v[176:191], a[220:223], v[108:111], v[176:191]\n"
      "ds_write_b128 v251, v[172:175] offset:3072\n"
      "v_mfma_f32_32x32x16_bf16 v[128:143], a[160:163], v[104:107], v[128:143]\n"
      "v_mfma_f32_32x32x16_bf16 v[128:143], a[216:219], v[108:111], v[128:143]\n"
      "s_nop 8\n"
      "ds_write_b128 v249, v[176:179]\n"
      "ds_write_b128 v249, v[180:183] offset:1024\n"
      "ds_write_b128 v249, v[184:187] offset:2048\n"
      "ds_write_b128 v249, v[188:191] offset:3072\n"
      "s_mov_b32 s78, 0x1e0000\n"
      "s_mov_b32 s79, 0x3e0000\n"
      "s_mov_b32 s80, 0x7060302\n"
      "s_xor_b32 s81, s97, 1\n"
      "s_cmp_lg_u32 s48, 0\n"
      "s_cbranch_scc1 .Lsk310\n"
      ".Lsk310:\n"
      "s_waitcnt lgkmcnt(0)\n"
      "s_barrier\n"
      "ds_read_b128 v[16:19], v252 offset:4096\n"
      "ds_read_b128 v[20:23], v252 offset:5120\n"
      "ds_read_b128 v[24:27], v252 offset:6144\n"
      "ds_read_b128 v[28:31], v252 offset:7168\n"
      "ds_read_b128 v[32:35], v252 offset:8192\n"
      "ds_read_b128 v[36:39], v252 offset:9216\n"
      "ds_read_b128 v[40:43], v252 offset:10240\n"
      "ds_read_b128 v[44:47], v252 offset:11264\n"
      "ds_read_b128 v[0:3], v252\n"
      "ds_read_b128 v[4:7], v252 offset:1024\n"
      "ds_read_b128 v[8:11], v252 offset:2048\n"
      "ds_read_b128 v[12:15], v252 offset:3072\n"
      "s_waitcnt lgkmcnt(11)\n"
      "v_pk_add_f32 v[128:129], v[128:129], v[16:17]\n"
      "v_pk_add_f32 v[130:131], v[130:131], v[18:19]\n"
      "s_waitcnt lgkmcnt(10)\n"
      "v_pk_add_f32 v[132:133], v[132:133], v[20:21]\n"
      "v_pk_add_f32 v[134:135], v[134:135], v[22:23]\n"
      "s_waitcnt lgkmcnt(9)\n"
      "v_pk_add_f32 v[136:137], v[136:137], v[24:25]\n"
      "v_pk_add_f32 v[138:139], v[138:139], v[26:27]\n"
      "s_waitcnt lgkmcnt(8)\n"
      "v_pk_add_f32 v[140:141], v[140:141], v[28:29]\n"
      "v_pk_add_f32 v[142:143], v[142:143], v[30:31]\n"
      "s_waitcnt lgkmcnt(7)\n"
      "v_pk_add_f32 v[128:129], v[128:129], v[32:33]\n"
      "v_pk_add_f32 v[130:131], v[130:131], v[34:35]\n"
      "s_waitcnt lgkmcnt(6)\n"
      "v_pk_add_f32 v[132:133], v[132:133], v[36:37]\n"
      "v_pk_add_f32 v[134:135], v[134:135], v[38:39]\n"
      "s_waitcnt lgkmcnt(5)\n"
      "v_pk_add_f32 v[136:137], v[136:137], v[40:41]\n"
      "v_pk_add_f32 v[138:139], v[138:139], v[42:43]\n"
      "s_waitcnt lgkmcnt(4)\n"
      "v_pk_add_f32 v[140:141], v[140:141], v[44:45]\n"
      "v_pk_add_f32 v[142:143], v[142:143], v[46:47]\n"
      "s_waitcnt lgkmcnt(3)\n"
      "v_pk_add_f32 v[128:129], v[128:129], v[0:1]\n"
      "v_pk_add_f32 v[130:131], v[130:131], v[2:3]\n"
      "s_waitcnt lgkmcnt(2)\n"
      "v_pk_add_f32 v[132:133], v[132:133], v[4:5]\n"
      "v_pk_add_f32 v[134:135], v[134:135], v[6:7]\n"
      "s_waitcnt lgkmcnt(1)\n"
      "v_pk_add_f32 v[136:137], v[136:137], v[8:9]\n"
      "v_pk_add_f32 v[138:139], v[138:139], v[10:11]\n"
      "s_waitcnt lgkmcnt(0)\n"
      "v_pk_add_f32 v[140:141], v[140:141], v[12:13]\n"
      "v_pk_add_f32 v[142:143], v[142:143], v[14:15]\n"
      "v_cvt_pk_bf16_f32 v240, v128, v129\n"
      "v_cvt_pk_bf16_f32 v241, v130, v131\n"
      "v_cvt_pk_bf16_f32 v242, v132, v133\n"
      "v_cvt_pk_bf16_f32 v243, v134, v135\n"
      "v_cvt_pk_bf16_f32 v244, v136, v137\n"
      "v_cvt_pk_bf16_f32 v245, v138, v139\n"
      "v_cvt_pk_bf16_f32 v246, v140, v141\n"
      "v_cvt_pk_bf16_f32 v247, v142, v143\n"
      "s_cmp_lg_u32 s48, 0\n"
      "s_cbranch_scc1 .Lfinal\n"
      "v_perm_b32 v48, v240, v128, s80\n"
      "v_perm_b32 v49, v241, v130, s80\n"
      "v_perm_b32 v50, v242, v132, s80\n"
      "v_perm_b32 v51, v243, v134, s80\n"
      "v_or_b32 v48, 1, v48\n"
      "v_or_b32 v49, 1, v49\n"
      "v_or_b32 v50, 1, v50\n"
      "v_or_b32 v51, 1, v51\n"
      "s_nop 0\n"
      "v_permlane32_swap_b32 v48, v50\n"
      "v_permlane32_swap_b32 v49, v51\n"
      "buffer_store_dwordx4 v[48:51], v229, s[36:39], s78 offen sc1\n"
      "v_perm_b32 v52, v244, v136, s80\n"
      "v_perm_b32 v53, v245, v138, s80\n"
      "v_perm_b32 v54, v246, v140, s80\n"
      "v_perm_b32 v55, v247, v142, s80\n"
      "v_or_b32 v52, 1, v52\n"
      "v_or_b32 v53, 1, v53\n"
      "v_or_b32 v54, 1, v54\n"
      "v_or_b32 v55, 1, v55\n"
      "s_nop 0\n"
      "v_permlane32_swap_b32 v52, v54\n"
      "v_permlane32_swap_b32 v53, v55\n"
      "buffer_store_dwordx4 v[52:55], v230, s[36:39], s78 offen sc1\n"
      "s_memtime s[90:91]\n"
      "s_waitcnt lgkmcnt(0)\n"
      "s_mov_b32 s96, s90\n"
      "v_cndmask_b32 v140, v140, 0, s[60:61]\n"
      "v_cndmask_b32 v141, v141, 0, s[60:61]\n"
      "v_cndmask_b32 v142, v142, 0, s[60:61]\n"
      "v_cndmask_b32 v143, v143, 0, s[60:61]\n"
      "v_pk_add_f32 v[0:1], v[128:129], v[130:131]\n"
      "v_pk_mul_f32 v[2:3], v[128:129], v[128:129]\n"
      "v_pk_fma_f32 v[2:3], v[130:131], v[130:131], v[2:3]\n"
      "v_pk_add_f32 v[0:1], v[0:1], v[132:133]\n"
      "v_pk_fma_f32 v[2:3], v[132:133], v[132:133], v[2:3]\n"
      "v_pk_add_f32 v[0:1], v[0:1], v[134:135]\n"
      "v_pk_fma_f32 v[2:3], v[134:135], v[134:135], v[2:3]\n"
      "v_pk_add_f32 v[0:1], v[0:1], v[136:137]\n"
      "v_pk_fma_f32 v[2:3], v[136:137], v[136:137], v[2:3]\n"
      "v_pk_add_f32 v[0:1], v[0:1], v[138:139]\n"
      "v_pk_fma_f32 v[2:3], v[138:139], v[138:139], v[2:3]\n"
      "v_pk_add_f32 v[0:1], v[0:1], v[140:141]\n"
      "v_pk_fma_f32 v[2:3], v[140:141], v[140:141], v[2:3]\n"
      "v_pk_add_f32 v[0:1], v[0:1], v[142:143]\n"
      "v_pk_fma_f32 v[2:3], v[142:143], v[142:143], v[2:3]\n"
      "v_add_f32 v6, v0, v1\n"
      "v_add_f32 v7, v2, v3\n"
      "v_mov_b32 v4, v6\n"
      "v_mov_b32 v5, v7\n"
      "s_nop 0\n"
      "v_permlane32_swap_b32 v6, v4\n"
      "v_permlane32_swap_b32 v7, v5\n"
      "v_pk_add_f32 v[6:7], v[6:7], v[4:5]\n"
      "v_or_b32 v6, 1, v6\n"
      "v_or_b32 v7, 1, v7\n"
      "buffer_store_dwordx2 v[6:7], v231, s[36:39], s79 offen sc1\n"
      "s_add_u32 s66, s66, 0x100000\n"
      "s_add_u32 s67, s67, 0x100000\n"
      "s_mov_b32 s48, 1\n"
      "s_mov_b32 s97, 0\n"
      "s_mov_b32 s63, 0x18000\n"
      "s_mov_b32 s42, 0\n"
      "s_mov_b32 s30, 0\n"
      "s_barrier\n"
      "s_branch .Lloop\n"
      ".Lfinal:\n"
      "s_and_b32 s80, s50, 7\n"
      "s_lshr_b32 s81, s50, 3\n"
      "s_lshl_b32 s78, s49, 2\n"
      "s_lshl_b32 s81, s81, 1\n"
      "s_add_u32 s78, s78, s81\n"
      "s_lshr_b32 s81, s51, 1\n"
      "s_add_u32 s78, s78, s81\n"
      "s_mul_i32 s78, s78, 0x7000\n"
      "s_mul_i32 s79, s80, 0x70\n"
      "s_add_u32 s78, s78, s79\n"
      "s_and_b32 s79, s51, 1\n"
      "s_lshl_b32 s79, s79, 6\n"
      "s_add_u32 s78, s78, s79\n"
      "v_and_b32 v0, 31, v238\n"
      "v_lshrrev_b32 v1, 5, v238\n"
      "v_mul_u32_u24 v0, 0x380, v0\n"
      "v_lshl_add_u32 v0, v1, 4, v0\n"
      "s_and_b64 s[86:87], s[56:57], s[60:61]\n"
      "v_mov_b32 v2, 0x80000000\n"
      "v_cndmask_b32 v2, v0, v2, s[86:87]\n"
      "v_permlane32_swap_b32 v240, v242\n"
      "v_permlane32_swap_b32 v241, v243\n"
      "v_permlane32_swap_b32 v244, v246\n"
      "v_permlane32_swap_b32 v245, v247\n"
      "buffer_store_dwordx4 v[240:243], v0, s[44:47], s78 offen\n"
      "buffer_store_dwordx4 v[244:247], v2, s[44:47], s78 offen offset:32\n"
      "s_endpgm\n"
      ".Lst9:\n"
      ".Llp11:\n"
      "s_nop 1\n"
      "buffer_load_dwordx4 v[184:187], v232, s[36:39], s85 offen sc1\n"
      "buffer_load_dwordx4 v[188:191], v232, s[36:39], s85 offen offset:512 sc1\n"
      "buffer_load_dwordx4 v[192:195], v232, s[36:39], s85 offen offset:1024 sc1\n"
      "buffer_load_dwordx4 v[196:199], v232, s[36:39], s85 offen offset:1536 sc1\n"
      "s_waitcnt vmcnt(0)\n"
      "v_bitop3_b32 v200, v184, v185, v186 bitop3:0x80\n"
      "v_bitop3_b32 v200, v200, v187, v188 bitop3:0x80\n"
      "v_bitop3_b32 v200, v200, v189, v190 bitop3:0x80\n"
      "v_bitop3_b32 v200, v200, v191, v192 bitop3:0x80\n"
      "v_bitop3_b32 v200, v200, v193, v194 bitop3:0x80\n"
      "v_bitop3_b32 v200, v200, v195, v196 bitop3:0x80\n"
      "v_bitop3_b32 v200, v200, v197, v198 bitop3:0x80\n"
      "v_bitop3_b32 v200, v200, v199, 1 bitop3:0x2a\n"
      "v_cmp_ne_u32 vcc, 0, v200\n"
      "s_cbranch_vccz .Lok12\n"
      "s_sub_u32 s77, s77, 1\n"
      "s_cmp_gt_i32 s77, 0\n"
      "s_cbranch_scc1 .Llp11\n"
      "s_branch .Lco10\n"
      ".Lok12:\n"
      "s_nop 1\n"
      "s_waitcnt vmcnt(0)\n"
      "s_branch .Lco10\n"
      ".Lst21:\n"
      ".Llp23:\n"
      "buffer_load_dword v217, v216, s[36:39], s78 offen offset:496 sc1\n"
      "s_waitcnt vmcnt(0)\n"
      "v_bitop3_b32 v217, v217, v217, 1 bitop3:0x2a\n"
      "v_cmp_ne_u32 vcc, 0, v217\n"
      "s_cbranch_vccz .Lok24\n"
      "s_sub_u32 s77, s77, 1\n"
      "s_cmp_gt_i32 s77, 0\n"
      "s_cbranch_scc1 .Llp23\n"
      "s_branch .Lco22\n"
      ".Lok24:\n"
      "s_nop 1\n"
      "buffer_load_dwordx4 v[0:3], v224, s[36:39], s78 offen sc1\n"
      "buffer_load_dwordx4 v[4:7], v224, s[36:39], s78 offen offset:1024 sc1\n"
      "buffer_load_dwordx4 v[8:11], v224, s[36:39], s78 offen offset:2048 sc1\n"
      "buffer_load_dwordx4 v[12:15], v224, s[36:39], s78 offen offset:3072 sc1\n"
      "buffer_load_dwordx4 v[16:19], v224, s[36:39], s79 offen sc1\n"
      "buffer_load_dwordx4 v[20:23], v224, s[36:39], s79 offen offset:1024 sc1\n"
      "buffer_load_dwordx4 v[24:27], v224, s[36:39], s79 offen offset:2048 sc1\n"
      "buffer_load_dwordx4 v[28:31], v224, s[36:39], s79 offen offset:3072 sc1\n"
      "s_waitcnt vmcnt(0)\n"
      "v_bitop3_b32 v217, v0, v1, v2 bitop3:0x80\n"
      "v_bitop3_b32 v217, v217, v3, v4 bitop3:0x80\n"
      "v_bitop3_b32 v217, v217, v5, v6 bitop3:0x80\n"
      "v_bitop3_b32 v217, v217, v7, 1 bitop3:0x2a\n"
      "v_cmp_ne_u32 vcc, 0, v217\n"
      "s_cbranch_vccz .Lco22\n"
      "s_sub_u32 s77, s77, 1\n"
      "s_cmp_gt_i32 s77, 0\n"
      "s_cbranch_scc1 .Llp23\n"
      "s_branch .Lco22\n"
      ".Lst25:\n"
      ".Llp27:\n"
      "buffer_load_dword v217, v216, s[36:39], s78 offen offset:496 sc1\n"
      "s_waitcnt vmcnt(0)\n"
      "v_bitop3_b32 v217, v217, v217, 1 bitop3:0x2a\n"
      "v_cmp_ne_u32 vcc, 0, v217\n"
      "s_cbranch_vccz .Lok28\n"
      "s_sub_u32 s77, s77, 1\n"
      "s_cmp_gt_i32 s77, 0\n"
      "s_cbranch_scc1 .Llp27\n"
      "s_branch .Lco26\n"
      ".Lok28:\n"
      "s_nop 1\n"
      "buffer_load_dwordx4 v[8:11], v224, s[36:39], s78 offen offset:2048 sc1\n"
      "buffer_load_dwordx4 v[12:15], v224, s[36:39], s78 offen offset:3072 sc1\n"
      "buffer_load_dwordx4 v[16:19], v224, s[36:39], s79 offen sc1\n"
      "buffer_load_dwordx4 v[20:23], v224, s[36:39], s79 offen offset:1024 sc1\n"
      "buffer_load_dwordx4 v[24:27], v224, s[36:39], s79 offen offset:2048 sc1\n"
      "buffer_load_dwordx4 v[28:31], v224, s[36:39], s79 offen offset:3072 sc1\n"
      "buffer_load_dwordx4 v[32:35], v224, s[36:39], s80 offen sc1\n"
      "buffer_load_dwordx4 v[36:39], v224, s[36:39], s80 offen offset:1024 sc1\n"
      "buffer_load_dwordx4 v[40:43], v224, s[36:39], s80 offen offset:2048 sc1\n"
      "buffer_load_dwordx4 v[44:47], v224, s[36:39], s80 offen offset:3072 sc1\n"
      "s_waitcnt vmcnt(0)\n"
      "v_bitop3_b32 v217, v8, v9, v10 bitop3:0x80\n"
      "v_bitop3_b32 v217, v217, v11, v12 bitop3:0x80\n"
      "v_bitop3_b32 v217, v217, v13, v14 bitop3:0x80\n"
      "v_bitop3_b32 v217, v217, v15, 1 bitop3:0x2a\n"
      "v_cmp_ne_u32 vcc, 0, v217\n"
      "s_cbranch_vccz .Lco26\n"
      "s_sub_u32 s77, s77, 1\n"
      "s_cmp_gt_i32 s77, 0\n"
      "s_cbranch_scc1 .Llp27\n"
      "s_branch .Lco26\n"
      ".Lst29:\n"
      ".Llp31:\n"
      "buffer_load_dword v217, v216, s[36:39], s78 offen offset:496 sc1\n"
      "s_waitcnt vmcnt(0)\n"
      "v_bitop3_b32 v217, v217, v217, 1 bitop3:0x2a\n"
      "v_cmp_ne_u32 vcc, 0, v217\n"
      "s_cbranch_vccz .Lok32\n"
      "s_sub_u32 s77, s77, 1\n"
      "s_cmp_gt_i32 s77, 0\n"
      "s_cbranch_scc1 .Llp31\n"
      "s_branch .Lco30\n"
      ".Lok32:\n"
      "s_nop 1\n"
      "buffer_load_dwordx4 v[16:19], v224, s[36:39], s79 offen sc1\n"
      "buffer_load_dwordx4 v[20:23], v224, s[36:39], s79 offen offset:1024 sc1\n"
      "buffer_load_dwordx4 v[24:27], v224, s[36:39], s79 offen offset:2048 sc1\n"
      "buffer_load_dwordx4 v[28:31], v224, s[36:39], s79 offen offset:3072 sc1\n"
      "buffer_load_dwordx4 v[32:35], v224, s[36:39], s80 offen sc1\n"
      "buffer_load_dwordx4 v[36:39], v224, s[36:39], s80 offen offset:1024 sc1\n"
      "buffer_load_dwordx4 v[40:43], v224, s[36:39], s80 offen offset:2048 sc1\n"
      "buffer_load_dwordx4 v[44:47], v224, s[36:39], s80 offen offset:3072 sc1\n"
      "s_waitcnt vmcnt(0)\n"
      "v_bitop3_b32 v217, v16, v17, v18 bitop3:0x80\n"
      "v_bitop3_b32 v217, v217, v19, v20 bitop3:0x80\n"
      "v_bitop3_b32 v217, v217, v21, v22 bitop3:0x80\n"
      "v_bitop3_b32 v217, v217, v23, 1 bitop3:0x2a\n"
      "v_cmp_ne_u32 vcc, 0, v217\n"
      "s_cbranch_vccz .Lco30\n"
      "s_sub_u32 s77, s77, 1\n"
      "s_cmp_gt_i32 s77, 0\n"
      "s_cbranch_scc1 .Llp31\n"
      "s_branch .Lco30\n"
      ".Lst33:\n"
      ".Llp35:\n"
      "buffer_load_dword v217, v216, s[36:39], s78 offen offset:496 sc1\n"
      "s_waitcnt vmcnt(0)\n"
      "v_bitop3_b32 v217, v217, v217, 1 bitop3:0x2a\n"
      "v_cmp_ne_u32 vcc, 0, v217\n"
      "s_cbranch_vccz .Lok36\n"
      "s_sub_u32 s77, s77, 1\n"
      "s_cmp_gt_i32 s77, 0\n"
      "s_cbranch_scc1 .Llp35\n"
      "s_branch .Lco34\n"
      ".Lok36:\n"
      "s_nop 1\n"
      "buffer_load_dwordx4 v[24:27], v224, s[36:39], s79 offen offset:2048 sc1\n"
      "buffer_load_dwordx4 v[28:31], v224, s[36:39], s79 offen offset:3072 sc1\n"
      "buffer_load_dwordx4 v[32:35], v224, s[36:39], s80 offen sc1\n"
      "buffer_load_dwordx4 v[36:39], v224, s[36:39], s80 offen offset:1024 sc1\n"
      "buffer_load_dwordx4 v[40:43], v224, s[36:39], s80 offen offset:2048 sc1\n"
      "buffer_load_dwordx4 v[44:47], v224, s[36:39], s80 offen offset:3072 sc1\n"
      "buffer_load_dwordx4 v[48:51], v224, s[36:39], s81 offen sc1\n"
      "buffer_load_dwordx4 v[52:55], v224, s[36:39], s81 offen offset:1024 sc1\n"
      "buffer_load_dwordx4 v[56:59], v224, s[36:39], s81 offen offset:2048 sc1\n"
      "buffer_load_dwordx4 v[60:63], v224, s[36:39], s81 offen offset:3072 sc1\n"
      "s_waitcnt vmcnt(0)\n"
      "v_bitop3_b32 v217, v24, v25, v26 bitop3:0x80\n"
      "v_bitop3_b32 v217, v217, v27, v28 bitop3:0x80\n"
      "v_bitop3_b32 v217, v217, v29, v30 bitop3:0x80\n"
      "v_bitop3_b32 v217, v217, v31, 1 bitop3:0x2a\n"
      "v_cmp_ne_u32 vcc, 0, v217\n"
      "s_cbranch_vccz .Lco34\n"
      "s_sub_u32 s77, s77, 1\n"
      "s_cmp_gt_i32 s77, 0\n"
      "s_cbranch_scc1 .Llp35\n"
      "s_branch .Lco34\n"
      ".Lst37:\n"
      ".Llp39:\n"
      "buffer_load_dword v217, v216, s[36:39], s78 offen offset:496 sc1\n"
      "s_waitcnt vmcnt(0)\n"
      "v_bitop3_b32 v217, v217, v217, 1 bitop3:0x2a\n"
      "v_cmp_ne_u32 vcc, 0, v217\n"
      "s_cbranch_vccz .Lok40\n"
      "s_sub_u32 s77, s77, 1\n"
      "s_cmp_gt_i32 s77, 0\n"
      "s_cbranch_scc1 .Llp39\n"
      "s_branch .Lco38\n"
      ".Lok40:\n"
      "s_nop 1\n"
      "buffer_load_dwordx4 v[32:35], v224, s[36:39], s80 offen sc1\n"
      "buffer_load_dwordx4 v[36:39], v224, s[36:39], s80 offen offset:1024 sc1\n"
      "buffer_load_dwordx4 v[40:43], v224, s[36:39], s80 offen offset:2048 sc1\n"
      "buffer_load_dwordx4 v[44:47], v224, s[36:39], s80 offen offset:3072 sc1\n"
      "buffer_load_dwordx4 v[48:51], v224, s[36:39], s81 offen sc1\n"
      "buffer_load_dwordx4 v[52:55], v224, s[36:39], s81 offen offset:1024 sc1\n"
      "buffer_load_dwordx4 v[56:59], v224, s[36:39], s81 offen offset:2048 sc1\n"
      "buffer_load_dwordx4 v[60:63], v224, s[36:39], s81 offen offset:3072 sc1\n"
      "buffer_load_dwordx4 v[64:67], v224, s[36:39], s82 offen sc1\n"
      "buffer_load_dwordx4 v[68:71], v224, s[36:39], s82 offen offset:1024 sc1\n"
      "buffer_load_dwordx4 v[72:75], v224, s[36:39], s82 offen offset:2048 sc1\n"
      "s_waitcnt vmcnt(0)\n"
      "v_bitop3_b32 v217, v32, v33, v34 bitop3:0x80\n"
      "v_bitop3_b32 v217, v217, v35, v36 bitop3:0x80\n"
      "v_bitop3_b32 v217, v217, v37, v38 bitop3:0x80\n"
      "v_bitop3_b32 v217, v217, v39, 1 bitop3:0x2a\n"
      "v_cmp_ne_u32 vcc, 0, v217\n"
      "s_cbranch_vccz .Lco38\n"
      "s_sub_u32 s77, s77, 1\n"
      "s_cmp_gt_i32 s77, 0\n"
      "s_cbranch_scc1 .Llp39\n"
      "s_branch .Lco38\n"
      ".Lst41:\n"
      ".Llp43:\n"
      "buffer_load_dword v217, v216, s[36:39], s78 offen offset:496 sc1\n"
      "s_waitcnt vmcnt(0)\n"
      "v_bitop3_b32 v217, v217, v217, 1 bitop3:0x2a\n"
      "v_cmp_ne_u32 vcc, 0, v217\n"
      "s_cbranch_vccz .Lok44\n"
      "s_sub_u32 s77, s77, 1\n"
      "s_cmp_gt_i32 s77, 0\n"
      "s_cbranch_scc1 .Llp43\n"
      "s_branch .Lco42\n"
      ".Lok44:\n"
      "s_nop 1\n"
      "buffer_load_dwordx4 v[40:43], v224, s[36:39], s80 offen offset:2048 sc1\n"
      "buffer_load_dwordx4 v[44:47], v224, s[36:39], s80 offen offset:3072 sc1\n"
      "buffer_load_dwordx4 v[48:51], v224, s[36:39], s81 offen sc1\n"
      "buffer_load_dwordx4 v[52:55], v224, s[36:39], s81 offen offset:1024 sc1\n"
      "buffer_load_dwordx4 v[56:59], v224, s[36:39], s81 offen offset:2048 sc1\n"
      "buffer_load_dwordx4 v[60:63], v224, s[36:39], s81 offen offset:3072 sc1\n"
      "buffer_load_dwordx4 v[64:67], v224, s[36:39], s82 offen sc1\n"
      "buffer_load_dwordx4 v[68:71], v224, s[36:39], s82 offen offset:1024 sc1\n"
      "buffer_load_dwordx4 v[72:75], v224, s[36:39], s82 offen offset:2048 sc1\n"
      "buffer_load_dwordx4 v[76:79], v224, s[36:39], s82 offen offset:3072 sc1\n"
      "buffer_load_dwordx4 v[80:83], v224, s[36:39], s83 offen sc1\n"
      "s_waitcnt vmcnt(0)\n"
      "v_bitop3_b32 v217, v40, v41, v42 bitop3:0x80\n"
      "v_bitop3_b32 v217, v217, v43, v44 bitop3:0x80\n"
      "v_bitop3_b32 v217, v217, v45, v46 bitop3:0x80\n"
      "v_bitop3_b32 v217, v217, v47, 1 bitop3:0x2a\n"
      "v_cmp_ne_u32 vcc, 0, v217\n"
      "s_cbranch_vccz .Lco42\n"
      "s_sub_u32 s77, s77, 1\n"
      "s_cmp_gt_i32 s77, 0\n"
      "s_cbranch_scc1 .Llp43\n"
      "s_branch .Lco42\n"
      ".Lst45:\n"
      ".Llp47:\n"
      "buffer_load_dword v217, v216, s[36:39], s78 offen offset:496 sc1\n"
      "s_waitcnt vmcnt(0)\n"
      "v_bitop3_b32 v217, v217, v217, 1 bitop3:0x2a\n"
      "v_cmp_ne_u32 vcc, 0, v217\n"
      "s_cbranch_vccz .Lok48\n"
      "s_sub_u32 s77, s77, 1\n"
      "s_cmp_gt_i32 s77, 0\n"
      "s_cbranch_scc1 .Llp47\n"
      "s_branch .Lco46\n"
      ".Lok48:\n"
      "s_nop 1\n"
      "buffer_load_dwordx4 v[48:51], v224, s[36:39], s81 offen sc1\n"
      "buffer_load_dwordx4 v[52:55], v224, s[36:39], s81 offen offset:1024 sc1\n"
      "buffer_load_dwordx4 v[56:59], v224, s[36:39], s81 offen offset:2048 sc1\n"
      "buffer_load_dwordx4 v[60:63], v224, s[36:39], s81 offen offset:3072 sc1\n"
      "buffer_load_dwordx4 v[64:67], v224, s[36:39], s82 offen sc1\n"
      "buffer_load_dwordx4 v[68:71], v224, s[36:39], s82 offen offset:1024 sc1\n"
      "buffer_load_dwordx4 v[72:75], v224, s[36:39], s82 offen offset:2048 sc1\n"
      "buffer_load_dwordx4 v[76:79], v224, s[36:39], s82 offen offset:3072 sc1\n"
      "buffer_load_dwordx4 v[80:83], v224, s[36:39], s83 offen sc1\n"
      "buffer_load_dwordx4 v[84:87], v224, s[36:39], s83 offen offset:1024 sc1\n"
      "buffer_load_dwordx4 v[88:91], v224, s[36:39], s83 offen offset:2048 sc1\n"
      "s_waitcnt vmcnt(0)\n"
      "v_bitop3_b32 v217, v48, v49, v50 bitop3:0x80\n"
      "v_bitop3_b32 v217, v217, v51, v52 bitop3:0x80\n"
      "v_bitop3_b32 v217, v217, v53, v54 bitop3:0x80\n"
      "v_bitop3_b32 v217, v217, v55, 1 bitop3:0x2a\n"
      "v_cmp_ne_u32 vcc, 0, v217\n"
      "s_cbranch_vccz .Lco46\n"
      "s_sub_u32 s77, s77, 1\n"
      "s_cmp_gt_i32 s77, 0\n"
      "s_cbranch_scc1 .Llp47\n"
      "s_branch .Lco46\n"
      ".Lst49:\n"
      ".Llp51:\n"
      "buffer_load_dword v217, v216, s[36:39], s78 offen offset:496 sc1\n"
      "s_waitcnt vmcnt(0)\n"
      "v_bitop3_b32 v217, v217, v217, 1 bitop3:0x2a\n"
      "v_cmp_ne_u32 vcc, 0, v217\n"
      "s_cbranch_vccz .Lok52\n"
      "s_sub_u32 s77, s77, 1\n"
      "s_cmp_gt_i32 s77, 0\n"
      "s_cbranch_scc1 .Llp51\n"
      "s_branch .Lco50\n"
      ".Lok52:\n"
      "s_nop 1\n"
      "buffer_load_dwordx4 v[56:59], v224, s[36:39], s81 offen offset:2048 sc1\n"
      "buffer_load_dwordx4 v[60:63], v224, s[36:39], s81 offen offset:3072 sc1\n"
      "buffer_load_dwordx4 v[64:67], v224, s[36:39], s82 offen sc1\n"
      "buffer_load_dwordx4 v[68:71], v224, s[36:39], s82 offen offset:1024 sc1\n"
      "buffer_load_dwordx4 v[72:75], v224, s[36:39], s82 offen offset:2048 sc1\n"
      "buffer_load_dwordx4 v[76:79], v224, s[36:39], s82 offen offset:3072 sc1\n"
      "buffer_load_dwordx4 v[80:83], v224, s[36:39], s83 offen sc1\n"
      "buffer_load_dwordx4 v[84:87], v224, s[36:39], s83 offen offset:1024 sc1\n"
      "buffer_load_dwordx4 v[88:91], v224, s[36:39], s83 offen offset:2048 sc1\n"
      "buffer_load_dwordx4 v[92:95], v224, s[36:39], s83 offen offset:3072 sc1\n"
      "buffer_load_dwordx4 v[96:99], v224, s[36:39], s84 offen sc1\n"
      "s_waitcnt vmcnt(0)\n"
      "v_bitop3_b32 v217, v56, v57, v58 bitop3:0x80\n"
      "v_bitop3_b32 v217, v217, v59, v60 bitop3:0x80\n"
      "v_bitop3_b32 v217, v217, v61, v62 bitop3:0x80\n"
      "v_bitop3_b32 v217, v217, v63, 1 bitop3:0x2a\n"
      "v_cmp_ne_u32 vcc, 0, v217\n"
      "s_cbranch_vccz .Lco50\n"
      "s_sub_u32 s77, s77, 1\n"
      "s_cmp_gt_i32 s77, 0\n"
      "s_cbranch_scc1 .Llp51\n"
      "s_branch .Lco50\n"
      ".Lst53:\n"
      ".Llp55:\n"
      "buffer_load_dword v217, v216, s[36:39], s78 offen offset:496 sc1\n"
      "s_waitcnt vmcnt(0)\n"
      "v_bitop3_b32 v217, v217, v217, 1 bitop3:0x2a\n"
      "v_cmp_ne_u32 vcc, 0, v217\n"
      "s_cbranch_vccz .Lok56\n"
      "s_sub_u32 s77, s77, 1\n"
      "s_cmp_gt_i32 s77, 0\n"
      "s_cbranch_scc1 .Llp55\n"
      "s_branch .Lco54\n"
      ".Lok56:\n"
      "s_nop 1\n"
      "buffer_load_dwordx4 v[64:67], v224, s[36:39], s82 offen sc1\n"
      "buffer_load_dwordx4 v[68:71], v224, s[36:39], s82 offen offset:1024 sc1\n"
      "buffer_load_dwordx4 v[72:75], v224, s[36:39], s82 offen offset:2048 sc1\n"
      "buffer_load_dwordx4 v[76:79], v224, s[36:39], s82 offen offset:3072 sc1\n"
      "buffer_load_dwordx4 v[80:83], v224, s[36:39], s83 offen sc1\n"
      "buffer_load_dwordx4 v[84:87], v224, s[36:39], s83 offen offset:1024 sc1\n"
      "buffer_load_dwordx4 v[88:91], v224, s[36:39], s83 offen offset:2048 sc1\n"
      "buffer_load_dwordx4 v[92:95], v224, s[36:39], s83 offen offset:3072 sc1\n"
      "buffer_load_dwordx4 v[96:99], v224, s[36:39], s84 offen sc1\n"
      "buffer_load_dwordx4 v[100:103], v224, s[36:39], s84 offen offset:1024 sc1\n"
      "buffer_load_dwordx4 v[104:107], v224, s[36:39], s84 offen offset:2048 sc1\n"
      "s_waitcnt vmcnt(0)\n"
      "v_bitop3_b32 v217, v64, v65, v66 bitop3:0x80\n"
      "v_bitop3_b32 v217, v217, v67, v68 bitop3:0x80\n"
      "v_bitop3_b32 v217, v217, v69, v70 bitop3:0x80\n"
      "v_bitop3_b32 v217, v217, v71, 1 bitop3:0x2a\n"
      "v_cmp_ne_u32 vcc, 0, v217\n"
      "s_cbranch_vccz .Lco54\n"
      "s_sub_u32 s77, s77, 1\n"
      "s_cmp_gt_i32 s77, 0\n"
      "s_cbranch_scc1 .Llp55\n"
      "s_branch .Lco54\n"
      ".Lst57:\n"
      ".Llp59:\n"
      "buffer_load_dword v217, v216, s[36:39], s78 offen offset:496 sc1\n"
      "s_waitcnt vmcnt(0)\n"
      "v_bitop3_b32 v217, v217, v217, 1 bitop3:0x2a\n"
      "v_cmp_ne_u32 vcc, 0, v217\n"
      "s_cbranch_vccz .Lok60\n"
      "s_sub_u32 s77, s77, 1\n"
      "s_cmp_gt_i32 s77, 0\n"
      "s_cbranch_scc1 .Llp59\n"
      "s_branch .Lco58\n"
      ".Lok60:\n"
      "s_nop 1\n"
      "buffer_load_dwordx4 v[72:75], v224, s[36:39], s82 offen offset:2048 sc1\n"
      "buffer_load_dwordx4 v[76:79], v224, s[36:39], s82 offen offset:3072 sc1\n"
      "buffer_load_dwordx4 v[80:83], v224, s[36:39], s83 offen sc1\n"
      "buffer_load_dwordx4 v[84:87], v224, s[36:39], s83 offen offset:1024 sc1\n"
      "buffer_load_dwordx4 v[88:91], v224, s[36:39], s83 offen offset:2048 sc1\n"
      "buffer_load_dwordx4 v[92:95], v224, s[36:39], s83 offen offset:3072 sc1\n"
      "buffer_load_dwordx4 v[96:99], v224, s[36:39], s84 offen sc1\n"
      "buffer_load_dwordx4 v[100:103], v224, s[36:39], s84 offen offset:1024 sc1\n"
      "buffer_load_dwordx4 v[104:107], v224, s[36:39], s84 offen offset:2048 sc1\n"
      "buffer_load_dwordx4 v[108:111], v224, s[36:39], s84 offen offset:3072 sc1\n"
      "s_waitcnt vmcnt(0)\n"
      "v_bitop3_b32 v217, v72, v73, v74 bitop3:0x80\n"
      "v_bitop3_b32 v217, v217, v75, v76 bitop3:0x80\n"
      "v_bitop3_b32 v217, v217, v77, v78 bitop3:0x80\n"
      "v_bitop3_b32 v217, v217, v79, 1 bitop3:0x2a\n"
      "v_cmp_ne_u32 vcc, 0, v217\n"
      "s_cbranch_vccz .Lco58\n"
      "s_sub_u32 s77, s77, 1\n"
      "s_cmp_gt_i32 s77, 0\n"
      "s_cbranch_scc1 .Llp59\n"
      "s_branch .Lco58\n"
      ".Lst61:\n"
      ".Llp63:\n"
      "buffer_load_dword v217, v216, s[36:39], s78 offen offset:496 sc1\n"
      "s_waitcnt vmcnt(0)\n"
      "v_bitop3_b32 v217, v217, v217, 1 bitop3:0x2a\n"
      "v_cmp_ne_u32 vcc, 0, v217\n"
      "s_cbranch_vccz .Lok64\n"
      "s_sub_u32 s77, s77, 1\n"
      "s_cmp_gt_i32 s77, 0\n"
      "s_cbranch_scc1 .Llp63\n"
      "s_branch .Lco62\n"
      ".Lok64:\n"
      "s_nop 1\n"
      "buffer_load_dwordx4 v[80:83], v224, s[36:39], s83 offen sc1\n"
      "buffer_load_dwordx4 v[84:87], v224, s[36:39], s83 offen offset:1024 sc1\n"
      "buffer_load_dwordx4 v[88:91], v224, s[36:39], s83 offen offset:2048 sc1\n"
      "buffer_load_dwordx4 v[92:95], v224, s[36:39], s83 offen offset:3072 sc1\n"
      "buffer_load_dwordx4 v[96:99], v224, s[36:39], s84 offen sc1\n"
      "buffer_load_dwordx4 v[100:103], v224, s[36:39], s84 offen offset:1024 sc1\n"
      "buffer_load_dwordx4 v[104:107], v224, s[36:39], s84 offen offset:2048 sc1\n"
      "buffer_load_dwordx4 v[108:111], v224, s[36:39], s84 offen offset:3072 sc1\n"
      "s_waitcnt vmcnt(0)\n"
      "v_bitop3_b32 v217, v80, v81, v82 bitop3:0x80\n"
      "v_bitop3_b32 v217, v217, v83, v84 bitop3:0x80\n"
      "v_bitop3_b32 v217, v217, v85, v86 bitop3:0x80\n"
      "v_bitop3_b32 v217, v217, v87, 1 bitop3:0x2a\n"
      "v_cmp_ne_u32 vcc, 0, v217\n"
      "s_cbranch_vccz .Lco62\n"
      "s_sub_u32 s77, s77, 1\n"
      "s_cmp_gt_i32 s77, 0\n"
      "s_cbranch_scc1 .Llp63\n"
      "s_branch .Lco62\n"
      ".Lst65:\n"
      ".Llp67:\n"
      "buffer_load_dword v217, v216, s[36:39], s78 offen offset:496 sc1\n"
      "s_waitcnt vmcnt(0)\n"
      "v_bitop3_b32 v217, v217, v217, 1 bitop3:0x2a\n"
      "v_cmp_ne_u32 vcc, 0, v217\n"
      "s_cbranch_vccz .Lok68\n"
      "s_sub_u32 s77, s77, 1\n"
      "s_cmp_gt_i32 s77, 0\n"
      "s_cbranch_scc1 .Llp67\n"
      "s_branch .Lco66\n"
      ".Lok68:\n"
      "s_nop 1\n"
      "buffer_load_dwordx4 v[88:91], v224, s[36:39], s83 offen offset:2048 sc1\n"
      "buffer_load_dwordx4 v[92:95], v224, s[36:39], s83 offen offset:3072 sc1\n"
      "buffer_load_dwordx4 v[96:99], v224, s[36:39], s84 offen sc1\n"
      "buffer_load_dwordx4 v[100:103], v224, s[36:39], s84 offen offset:1024 sc1\n"
      "buffer_load_dwordx4 v[104:107], v224, s[36:39], s84 offen offset:2048 sc1\n"
      "buffer_load_dwordx4 v[108:111], v224, s[36:39], s84 offen offset:3072 sc1\n"
      "s_waitcnt vmcnt(0)\n"
      "v_bitop3_b32 v217, v88, v89, v90 bitop3:0x80\n"
      "v_bitop3_b32 v217, v217, v91, v92 bitop3:0x80\n"
      "v_bitop3_b32 v217, v217, v93, v94 bitop3:0x80\n"
      "v_bitop3_b32 v217, v217, v95, 1 bitop3:0x2a\n"
      "v_cmp_ne_u32 vcc, 0, v217\n"
      "s_cbranch_vccz .Lco66\n"
      "s_sub_u32 s77, s77, 1\n"
      "s_cmp_gt_i32 s77, 0\n"
      "s_cbranch_scc1 .Llp67\n"
      "s_branch .Lco66\n"
      ".Lst69:\n"
      ".Llp71:\n"
      "buffer_load_dword v217, v216, s[36:39], s78 offen offset:496 sc1\n"
      "s_waitcnt vmcnt(0)\n"
      "v_bitop3_b32 v217, v217, v217, 1 bitop3:0x2a\n"
      "v_cmp_ne_u32 vcc, 0, v217\n"
      "s_cbranch_vccz .Lok72\n"
      "s_sub_u32 s77, s77, 1\n"
      "s_cmp_gt_i32 s77, 0\n"
      "s_cbranch_scc1 .Llp71\n"
      "s_branch .Lco70\n"
      ".Lok72:\n"
      "s_nop 1\n"
      "buffer_load_dwordx4 v[96:99], v224, s[36:39], s84 offen sc1\n"
      "buffer_load_dwordx4 v[100:103], v224, s[36:39], s84 offen offset:1024 sc1\n"
      "buffer_load_dwordx4 v[104:107], v224, s[36:39], s84 offen offset:2048 sc1\n"
      "buffer_load_dwordx4 v[108:111], v224, s[36:39], s84 offen offset:3072 sc1\n"
      "s_waitcnt vmcnt(0)\n"
      "v_bitop3_b32 v217, v96, v97, v98 bitop3:0x80\n"
      "v_bitop3_b32 v217, v217, v99, v100 bitop3:0x80\n"
      "v_bitop3_b32 v217, v217, v101, v102 bitop3:0x80\n"
      "v_bitop3_b32 v217, v217, v103, 1 bitop3:0x2a\n"
      "v_cmp_ne_u32 vcc, 0, v217\n"
      "s_cbranch_vccz .Lco70\n"
      "s_sub_u32 s77, s77, 1\n"
      "s_cmp_gt_i32 s77, 0\n"
      "s_cbranch_scc1 .Llp71\n"
      "s_branch .Lco70\n"
      ".Lst73:\n"
      ".Llp75:\n"
      "buffer_load_dword v217, v216, s[36:39], s78 offen offset:496 sc1\n"
      "s_waitcnt vmcnt(0)\n"
      "v_bitop3_b32 v217, v217, v217, 1 bitop3:0x2a\n"
      "v_cmp_ne_u32 vcc, 0, v217\n"
      "s_cbranch_vccz .Lok76\n"
      "s_sub_u32 s77, s77, 1\n"
      "s_cmp_gt_i32 s77, 0\n"
      "s_cbranch_scc1 .Llp75\n"
      "s_branch .Lco74\n"
      ".Lok76:\n"
      "s_nop 1\n"
      "buffer_load_dwordx4 v[104:107], v224, s[36:39], s84 offen offset:2048 sc1\n"
      "buffer_load_dwordx4 v[108:111], v224, s[36:39], s84 offen offset:3072 sc1\n"
      "s_waitcnt vmcnt(0)\n"
      "v_bitop3_b32 v217, v104, v105, v106 bitop3:0x80\n"
      "v_bitop3_b32 v217, v217, v107, v108 bitop3:0x80\n"
      "v_bitop3_b32 v217, v217, v109, v110 bitop3:0x80\n"
      "v_bitop3_b32 v217, v217, v111, 1 bitop3:0x2a\n"
      "v_cmp_ne_u32 vcc, 0, v217\n"
      "s_cbranch_vccz .Lco74\n"
      "s_sub_u32 s77, s77, 1\n"
      "s_cmp_gt_i32 s77, 0\n"
      "s_cbranch_scc1 .Llp75\n"
      "s_branch .Lco74\n"
      ".Lst79:\n"
      ".Llp81:\n"
      "v_max3_u32 v132, v128, v129, v130\n"
      "v_max_u32 v132, v132, v131\n"
      "v_cmp_eq_u32 vcc, 0xffbfffbf, v132\n"
      "s_cbranch_vccz .Lco80\n"
      "buffer_load_dwordx4 v[128:131], v248, s[36:39], s67 offen sc1\n"
      "s_waitcnt vmcnt(0)\n"
      "s_sub_u32 s77, s77, 1\n"
      "s_cmp_gt_i32 s77, 0\n"
      "s_cbranch_scc1 .Llp81\n"
      "s_branch .Lco80\n"
      ".Lfl83:\n"
      ".Llp85:\n"
      "s_mov_b32 s87, 0x1800\n"
      "buffer_load_dwordx4 v[160:163], v224, s[36:39], s87 offen sc1\n"
      "buffer_load_dwordx4 v[164:167], v224, s[36:39], s87 offen offset:1024 sc1\n"
      "s_waitcnt vmcnt(0)\n"
      "v_cmp_ne_u64 vcc, s[54:55], v[160:161]\n"
      "v_cmp_ne_u64 s[86:87], s[54:55], v[162:163]\n"
      "s_or_b64 vcc, vcc, s[86:87]\n"
      "v_cmp_ne_u64 s[86:87], s[54:55], v[164:165]\n"
      "s_or_b64 vcc, vcc, s[86:87]\n"
      "v_cmp_ne_u64 s[86:87], s[54:55], v[166:167]\n"
      "s_or_b64 vcc, vcc, s[86:87]\n"
      "s_cbranch_vccz .Lne84\n"
      "s_sub_u32 s77, s77, 1\n"
      "s_cmp_gt_i32 s77, 0\n"
      "s_cbranch_scc1 .Llp85\n"
      "s_branch .Lne84\n"
      ".Lst89:\n"
      ".Llp91:\n"
      "buffer_load_dword v217, v216, s[36:39], s82 offen offset:1520 sc1\n"
      "s_waitcnt vmcnt(0)\n"
      "v_bitop3_b32 v217, v217, s97, 1 bitop3:0x28\n"
      "v_cmp_ne_u32 vcc, 0, v217\n"
      "s_cbranch_vccz .Lok92\n"
      "s_sub_u32 s77, s77, 1\n"
      "s_cmp_gt_i32 s77, 0\n"
      "s_cbranch_scc1 .Llp91\n"
      "s_branch .Lco90\n"
      ".Lok92:\n"
      "s_nop 1\n"
      "buffer_load_dwordx4 v[0:3], v224, s[36:39], s70 offen sc1\n"
      "buffer_load_dwordx4 v[4:7], v224, s[36:39], s70 offen offset:1024 sc1\n"
      "buffer_load_dwordx4 v[8:11], v224, s[36:39], s70 offen offset:2048 sc1\n"
      "buffer_load_dwordx4 v[12:15], v224, s[36:39], s70 offen offset:3072 sc1\n"
      "buffer_load_dwordx4 v[16:19], v224, s[36:39], s80 offen sc1\n"
      "buffer_load_dwordx4 v[20:23], v224, s[36:39], s80 offen offset:1024 sc1\n"
      "buffer_load_dwordx4 v[24:27], v224, s[36:39], s80 offen offset:2048 sc1\n"
      "buffer_load_dwordx4 v[28:31], v224, s[36:39], s80 offen offset:3072 sc1\n"
      "buffer_load_dwordx4 v[32:35], v224, s[36:39], s88 offen sc1\n"
      "buffer_load_dwordx4 v[36:39], v224, s[36:39], s88 offen offset:1024 sc1\n"
      "buffer_load_dwordx4 v[40:43], v224, s[36:39], s88 offen offset:2048 sc1\n"
      "buffer_load_dwordx4 v[44:47], v224, s[36:39], s88 offen offset:3072 sc1\n"
      "buffer_load_dwordx4 v[48:51], v224, s[36:39], s81 offen sc1\n"
      "buffer_load_dwordx4 v[52:55], v224, s[36:39], s81 offen offset:1024 sc1\n"
      "buffer_load_dwordx4 v[56:59], v224, s[36:39], s81 offen offset:2048 sc1\n"
      "buffer_load_dwordx4 v[60:63], v224, s[36:39], s81 offen offset:3072 sc1\n"
      "s_waitcnt vmcnt(0)\n"
      "v_bitop3_b32 v217, v0, v1, s97 bitop3:0x7e\n"
      "v_bitop3_b32 v219, v2, v3, s97 bitop3:0x7e\n"
      "v_bitop3_b32 v176, v4, v5, s97 bitop3:0x7e\n"
      "v_or3_b32 v217, v217, v219, v176\n"
      "v_bitop3_b32 v219, v6, v7, s97 bitop3:0x7e\n"
      "v_bitop3_b32 v217, v217, v219, 1 bitop3:0xa8\n"
      "v_cmp_ne_u32 vcc, 0, v217\n"
      "s_cbranch_vccz .Lco90\n"
      "s_sub_u32 s77, s77, 1\n"
      "s_cmp_gt_i32 s77, 0\n"
      "s_cbranch_scc1 .Llp91\n"
      "s_branch .Lco90\n"
      ".Lst93:\n"
      ".Llp95:\n"
      "buffer_load_dword v217, v216, s[36:39], s82 offen offset:1520 sc1\n"
      "s_waitcnt vmcnt(0)\n"
      "v_bitop3_b32 v217, v217, s97, 1 bitop3:0x28\n"
      "v_cmp_ne_u32 vcc, 0, v217\n"
      "s_cbranch_vccz .Lok96\n"
      "s_sub_u32 s77, s77, 1\n"
      "s_cmp_gt_i32 s77, 0\n"
      "s_cbranch_scc1 .Llp95\n"
      "s_branch .Lco94\n"
      ".Lok96:\n"
      "s_nop 1\n"
      "buffer_load_dwordx4 v[8:11], v224, s[36:39], s70 offen offset:2048 sc1\n"
      "buffer_load_dwordx4 v[12:15], v224, s[36:39], s70 offen offset:3072 sc1\n"
      "buffer_load_dwordx4 v[16:19], v224, s[36:39], s80 offen sc1\n"
      "buffer_load_dwordx4 v[20:23], v224, s[36:39], s80 offen offset:1024 sc1\n"
      "buffer_load_dwordx4 v[24:27], v224, s[36:39], s80 offen offset:2048 sc1\n"
      "buffer_load_dwordx4 v[28:31], v224, s[36:39], s80 offen offset:3072 sc1\n"
      "buffer_load_dwordx4 v[32:35], v224, s[36:39], s88 offen sc1\n"
      "buffer_load_dwordx4 v[36:39], v224, s[36:39], s88 offen offset:1024 sc1\n"
      "buffer_load_dwordx4 v[40:43], v224, s[36:39], s88 offen offset:2048 sc1\n"
      "buffer_load_dwordx4 v[44:47], v224, s[36:39], s88 offen offset:3072 sc1\n"
      "buffer_load_dwordx4 v[48:51], v224, s[36:39], s81 offen sc1\n"
      "buffer_load_dwordx4 v[52:55], v224, s[36:39], s81 offen offset:1024 sc1\n"
      "buffer_load_dwordx4 v[56:59], v224, s[36:39], s81 offen offset:2048 sc1\n"
      "buffer_load_dwordx4 v[60:63], v224, s[36:39], s81 offen offset:3072 sc1\n"
      "s_waitcnt vmcnt(0)\n"
      "v_bitop3_b32 v217, v8, v9, s97 bitop3:0x7e\n"
      "v_bitop3_b32 v219, v10, v11, s97 bitop3:0x7e\n"
      "v_bitop3_b32 v176, v12, v13, s97 bitop3:0x7e\n"
      "v_or3_b32 v217, v217, v219, v176\n"
      "v_bitop3_b32 v219, v14, v15, s97 bitop3:0x7e\n"
      "v_bitop3_b32 v217, v217, v219, 1 bitop3:0xa8\n"
      "v_cmp_ne_u32 vcc, 0, v217\n"
      "s_cbranch_vccz .Lco94\n"
      "s_sub_u32 s77, s77, 1\n"
      "s_cmp_gt_i32 s77, 0\n"
      "s_cbranch_scc1 .Llp95\n"
      "s_branch .Lco94\n"
      ".Lst97:\n"
      ".Llp99:\n"
      "buffer_load_dword v217, v216, s[36:39], s82 offen offset:1520 sc1\n"
      "s_waitcnt vmcnt(0)\n"
      "v_bitop3_b32 v217, v217, s97, 1 bitop3:0x28\n"
      "v_cmp_ne_u32 vcc, 0, v217\n"
      "s_cbranch_vccz .Lok100\n"
      "s_sub_u32 s77, s77, 1\n"
      "s_cmp_gt_i32 s77, 0\n"
      "s_cbranch_scc1 .Llp99\n"
      "s_branch .Lco98\n"
      ".Lok100:\n"
      "s_nop 1\n"
      "buffer_load_dwordx4 v[16:19], v224, s[36:39], s80 offen sc1\n"
      "buffer_load_dwordx4 v[20:23], v224, s[36:39], s80 offen offset:1024 sc1\n"
      "buffer_load_dwordx4 v[24:27], v224, s[36:39], s80 offen offset:2048 sc1\n"
      "buffer_load_dwordx4 v[28:31], v224, s[36:39], s80 offen offset:3072 sc1\n"
      "buffer_load_dwordx4 v[32:35], v224, s[36:39], s88 offen sc1\n"
      "buffer_load_dwordx4 v[36:39], v224, s[36:39], s88 offen offset:1024 sc1\n"
      "buffer_load_dwordx4 v[40:43], v224, s[36:39], s88 offen offset:2048 sc1\n"
      "buffer_load_dwordx4 v[44:47], v224, s[36:39], s88 offen offset:3072 sc1\n"
      "buffer_load_dwordx4 v[48:51], v224, s[36:39], s81 offen sc1\n"
      "buffer_load_dwordx4 v[52:55], v224, s[36:39], s81 offen offset:1024 sc1\n"
      "buffer_load_dwordx4 v[56:59], v224, s[36:39], s81 offen offset:2048 sc1\n"
      "buffer_load_dwordx4 v[60:63], v224, s[36:39], s81 offen offset:3072 sc1\n"
      "s_waitcnt vmcnt(0)\n"
      "v_bitop3_b32 v217, v16, v17, s97 bitop3:0x7e\n"
      "v_bitop3_b32 v219, v18, v19, s97 bitop3:0x7e\n"
      "v_bitop3_b32 v176, v20, v21, s97 bitop3:0x7e\n"
      "v_or3_b32 v217, v217, v219, v176\n"
      "v_bitop3_b32 v219, v22, v23, s97 bitop3:0x7e\n"
      "v_bitop3_b32 v217, v217, v219, 1 bitop3:0xa8\n"
      "v_cmp_ne_u32 vcc, 0, v217\n"
      "s_cbranch_vccz .Lco98\n"
      "s_sub_u32 s77, s77, 1\n"
      "s_cmp_gt_i32 s77, 0\n"
      "s_cbranch_scc1 .Llp99\n"
      "s_branch .Lco98\n"
      ".Lst101:\n"
      ".Llp103:\n"
      "buffer_load_dword v217, v216, s[36:39], s82 offen offset:1520 sc1\n"
      "s_waitcnt vmcnt(0)\n"
      "v_bitop3_b32 v217, v217, s97, 1 bitop3:0x28\n"
      "v_cmp_ne_u32 vcc, 0, v217\n"
      "s_cbranch_vccz .Lok104\n"
      "s_sub_u32 s77, s77, 1\n"
      "s_cmp_gt_i32 s77, 0\n"
      "s_cbranch_scc1 .Llp103\n"
      "s_branch .Lco102\n"
      ".Lok104:\n"
      "s_nop 1\n"
      "buffer_load_dwordx4 v[24:27], v224, s[36:39], s80 offen offset:2048 sc1\n"
      "buffer_load_dwordx4 v[28:31], v224, s[36:39], s80 offen offset:3072 sc1\n"
      "buffer_load_dwordx4 v[32:35], v224, s[36:39], s88 offen sc1\n"
      "buffer_load_dwordx4 v[36:39], v224, s[36:39], s88 offen offset:1024 sc1\n"
      "buffer_load_dwordx4 v[40:43], v224, s[36:39], s88 offen offset:2048 sc1\n"
      "buffer_load_dwordx4 v[44:47], v224, s[36:39], s88 offen offset:3072 sc1\n"
      "buffer_load_dwordx4 v[48:51], v224, s[36:39], s81 offen sc1\n"
      "buffer_load_dwordx4 v[52:55], v224, s[36:39], s81 offen offset:1024 sc1\n"
      "buffer_load_dwordx4 v[56:59], v224, s[36:39], s81 offen offset:2048 sc1\n"
      "buffer_load_dwordx4 v[60:63], v224, s[36:39], s81 offen offset:3072 sc1\n"
      "s_waitcnt vmcnt(0)\n"
      "v_bitop3_b32 v217, v24, v25, s97 bitop3:0x7e\n"
      "v_bitop3_b32 v219, v26, v27, s97 bitop3:0x7e\n"
      "v_bitop3_b32 v176, v28, v29, s97 bitop3:0x7e\n"
      "v_or3_b32 v217, v217, v219, v176\n"
      "v_bitop3_b32 v219, v30, v31, s97 bitop3:0x7e\n"
      "v_bitop3_b32 v217, v217, v219, 1 bitop3:0xa8\n"
      "v_cmp_ne_u32 vcc, 0, v217\n"
      "s_cbranch_vccz .Lco102\n"
      "s_sub_u32 s77, s77, 1\n"
      "s_cmp_gt_i32 s77, 0\n"
      "s_cbranch_scc1 .Llp103\n"
      "s_branch .Lco102\n"
      ".Lst105:\n"
      ".Llp107:\n"
      "buffer_load_dword v217, v216, s[36:39], s82 offen offset:1520 sc1\n"
      "s_waitcnt vmcnt(0)\n"
      "v_bitop3_b32 v217, v217, s97, 1 bitop3:0x28\n"
      "v_cmp_ne_u32 vcc, 0, v217\n"
      "s_cbranch_vccz .Lok108\n"
      "s_sub_u32 s77, s77, 1\n"
      "s_cmp_gt_i32 s77, 0\n"
      "s_cbranch_scc1 .Llp107\n"
      "s_branch .Lco106\n"
      ".Lok108:\n"
      "s_nop 1\n"
      "buffer_load_dwordx4 v[32:35], v224, s[36:39], s88 offen sc1\n"
      "buffer_load_dwordx4 v[36:39], v224, s[36:39], s88 offen offset:1024 sc1\n"
      "buffer_load_dwordx4 v[40:43], v224, s[36:39], s88 offen offset:2048 sc1\n"
      "buffer_load_dwordx4 v[44:47], v224, s[36:39], s88 offen offset:3072 sc1\n"
      "buffer_load_dwordx4 v[48:51], v224, s[36:39], s81 offen sc1\n"
      "buffer_load_dwordx4 v[52:55], v224, s[36:39], s81 offen offset:1024 sc1\n"
      "buffer_load_dwordx4 v[56:59], v224, s[36:39], s81 offen offset:2048 sc1\n"
      "buffer_load_dwordx4 v[60:63], v224, s[36:39], s81 offen offset:3072 sc1\n"
      "s_waitcnt vmcnt(0)\n"
      "v_bitop3_b32 v217, v32, v33, s97 bitop3:0x7e\n"
      "v_bitop3_b32 v219, v34, v35, s97 bitop3:0x7e\n"
      "v_bitop3_b32 v176, v36, v37, s97 bitop3:0x7e\n"
      "v_or3_b32 v217, v217, v219, v176\n"
      "v_bitop3_b32 v219, v38, v39, s97 bitop3:0x7e\n"
      "v_bitop3_b32 v217, v217, v219, 1 bitop3:0xa8\n"
      "v_cmp_ne_u32 vcc, 0, v217\n"
      "s_cbranch_vccz .Lco106\n"
      "s_sub_u32 s77, s77, 1\n"
      "s_cmp_gt_i32 s77, 0\n"
      "s_cbranch_scc1 .Llp107\n"
      "s_branch .Lco106\n"
      ".Lst109:\n"
      ".Llp111:\n"
      "buffer_load_dword v217, v216, s[36:39], s82 offen offset:1520 sc1\n"
      "s_waitcnt vmcnt(0)\n"
      "v_bitop3_b32 v217, v217, s97, 1 bitop3:0x28\n"
      "v_cmp_ne_u32 vcc, 0, v217\n"
      "s_cbranch_vccz .Lok112\n"
      "s_sub_u32 s77, s77, 1\n"
      "s_cmp_gt_i32 s77, 0\n"
      "s_cbranch_scc1 .Llp111\n"
      "s_branch .Lco110\n"
      ".Lok112:\n"
      "s_nop 1\n"
      "buffer_load_dwordx4 v[40:43], v224, s[36:39], s88 offen offset:2048 sc1\n"
      "buffer_load_dwordx4 v[44:47], v224, s[36:39], s88 offen offset:3072 sc1\n"
      "buffer_load_dwordx4 v[48:51], v224, s[36:39], s81 offen sc1\n"
      "buffer_load_dwordx4 v[52:55], v224, s[36:39], s81 offen offset:1024 sc1\n"
      "buffer_load_dwordx4 v[56:59], v224, s[36:39], s81 offen offset:2048 sc1\n"
      "buffer_load_dwordx4 v[60:63], v224, s[36:39], s81 offen offset:3072 sc1\n"
      "s_waitcnt vmcnt(0)\n"
      "v_bitop3_b32 v217, v40, v41, s97 bitop3:0x7e\n"
      "v_bitop3_b32 v219, v42, v43, s97 bitop3:0x7e\n"
      "v_bitop3_b32 v176, v44, v45, s97 bitop3:0x7e\n"
      "v_or3_b32 v217, v217, v219, v176\n"
      "v_bitop3_b32 v219, v46, v47, s97 bitop3:0x7e\n"
      "v_bitop3_b32 v217, v217, v219, 1 bitop3:0xa8\n"
      "v_cmp_ne_u32 vcc, 0, v217\n"
      "s_cbranch_vccz .Lco110\n"
      "s_sub_u32 s77, s77, 1\n"
      "s_cmp_gt_i32 s77, 0\n"
      "s_cbranch_scc1 .Llp111\n"
      "s_branch .Lco110\n"
      ".Lst113:\n"
      ".Llp115:\n"
      "buffer_load_dword v217, v216, s[36:39], s82 offen offset:1520 sc1\n"
      "s_waitcnt vmcnt(0)\n"
      "v_bitop3_b32 v217, v217, s97, 1 bitop3:0x28\n"
      "v_cmp_ne_u32 vcc, 0, v217\n"
      "s_cbranch_vccz .Lok116\n"
      "s_sub_u32 s77, s77, 1\n"
      "s_cmp_gt_i32 s77, 0\n"
      "s_cbranch_scc1 .Llp115\n"
      "s_branch .Lco114\n"
      ".Lok116:\n"
      "s_nop 1\n"
      "buffer_load_dwordx4 v[48:51], v224, s[36:39], s81 offen sc1\n"
      "buffer_load_dwordx4 v[52:55], v224, s[36:39], s81 offen offset:1024 sc1\n"
      "buffer_load_dwordx4 v[56:59], v224, s[36:39], s81 offen offset:2048 sc1\n"
      "buffer_load_dwordx4 v[60:63], v224, s[36:39], s81 offen offset:3072 sc1\n"
      "s_waitcnt vmcnt(0)\n"
      "v_bitop3_b32 v217, v48, v49, s97 bitop3:0x7e\n"
      "v_bitop3_b32 v219, v50, v51, s97 bitop3:0x7e\n"
      "v_bitop3_b32 v176, v52, v53, s97 bitop3:0x7e\n"
      "v_or3_b32 v217, v217, v219, v176\n"
      "v_bitop3_b32 v219, v54, v55, s97 bitop3:0x7e\n"
      "v_bitop3_b32 v217, v217, v219, 1 bitop3:0xa8\n"
      "v_cmp_ne_u32 vcc, 0, v217\n"
      "s_cbranch_vccz .Lco114\n"
      "s_sub_u32 s77, s77, 1\n"
      "s_cmp_gt_i32 s77, 0\n"
      "s_cbranch_scc1 .Llp115\n"
      "s_branch .Lco114\n"
      ".Lst117:\n"
      ".Llp119:\n"
      "buffer_load_dword v217, v216, s[36:39], s82 offen offset:1520 sc1\n"
      "s_waitcnt vmcnt(0)\n"
      "v_bitop3_b32 v217, v217, s97, 1 bitop3:0x28\n"
      "v_cmp_ne_u32 vcc, 0, v217\n"
      "s_cbranch_vccz .Lok120\n"
      "s_sub_u32 s77, s77, 1\n"
      "s_cmp_gt_i32 s77, 0\n"
      "s_cbranch_scc1 .Llp119\n"
      "s_branch .Lco118\n"
      ".Lok120:\n"
      "s_nop 1\n"
      "buffer_load_dwordx4 v[56:59], v224, s[36:39], s81 offen offset:2048 sc1\n"
      "buffer_load_dwordx4 v[60:63], v224, s[36:39], s81 offen offset:3072 sc1\n"
      "s_waitcnt vmcnt(0)\n"
      "v_bitop3_b32 v217, v56, v57, s97 bitop3:0x7e\n"
      "v_bitop3_b32 v219, v58, v59, s97 bitop3:0x7e\n"
      "v_bitop3_b32 v176, v60, v61, s97 bitop3:0x7e\n"
      "v_or3_b32 v217, v217, v219, v176\n"
      "v_bitop3_b32 v219, v62, v63, s97 bitop3:0x7e\n"
      "v_bitop3_b32 v217, v217, v219, 1 bitop3:0xa8\n"
      "v_cmp_ne_u32 vcc, 0, v217\n"
      "s_cbranch_vccz .Lco118\n"
      "s_sub_u32 s77, s77, 1\n"
      "s_cmp_gt_i32 s77, 0\n"
      "s_cbranch_scc1 .Llp119\n"
      "s_branch .Lco118\n"
      ".Lst121:\n"
      ".Llp123:\n"
      "buffer_load_dwordx4 v[128:131], v224, s[36:39], s83 offen sc1\n"
      "buffer_load_dwordx4 v[132:135], v224, s[36:39], s83 offen offset:1024 sc1\n"
      "buffer_load_dwordx4 v[136:139], v224, s[36:39], s83 offen offset:2048 sc1\n"
      "buffer_load_dwordx4 v[140:143], v224, s[36:39], s83 offen offset:3072 sc1\n"
      "buffer_load_dwordx4 v[144:147], v224, s[36:39], s84 offen sc1\n"
      "buffer_load_dwordx4 v[148:151], v224, s[36:39], s84 offen offset:1024 sc1\n"
      "buffer_load_dwordx4 v[152:155], v224, s[36:39], s84 offen offset:2048 sc1\n"
      "buffer_load_dwordx4 v[156:159], v224, s[36:39], s84 offen offset:3072 sc1\n"
      "s_waitcnt vmcnt(0)\n"
      "v_max3_u32 v178, v128, v129, v130\n"
      "v_max3_u32 v178, v178, v131, v132\n"
      "v_max3_u32 v178, v178, v133, v134\n"
      "v_max3_u32 v178, v178, v135, v136\n"
      "v_max3_u32 v178, v178, v137, v138\n"
      "v_max3_u32 v178, v178, v139, v140\n"
      "v_max3_u32 v178, v178, v141, v142\n"
      "v_max3_u32 v178, v178, v143, v144\n"
      "v_max3_u32 v178, v178, v145, v146\n"
      "v_max3_u32 v178, v178, v147, v148\n"
      "v_max3_u32 v178, v178, v149, v150\n"
      "v_max3_u32 v178, v178, v151, v152\n"
      "v_max3_u32 v178, v178, v153, v154\n"
      "v_max3_u32 v178, v178, v155, v156\n"
      "v_max3_u32 v178, v178, v157, v158\n"
      "v_max_u32 v178, v178, v159\n"
      "v_cmp_eq_u32 vcc, 0xffbfffbf, v178\n"
      "s_cbranch_vccz .Lco122\n"
      "s_sub_u32 s77, s77, 1\n"
      "s_cmp_gt_i32 s77, 0\n"
      "s_cbranch_scc1 .Llp123\n"
      "s_branch .Lco122\n"
      ".Lst131:\n"
      ".Llp133:\n"
      "s_nop 1\n"
      "buffer_load_dwordx4 v[184:187], v232, s[36:39], s85 offen sc1\n"
      "buffer_load_dwordx4 v[188:191], v232, s[36:39], s85 offen offset:512 sc1\n"
      "buffer_load_dwordx4 v[192:195], v232, s[36:39], s85 offen offset:1024 sc1\n"
      "buffer_load_dwordx4 v[196:199], v232, s[36:39], s85 offen offset:1536 sc1\n"
      "s_waitcnt vmcnt(0)\n"
      "v_bitop3_b32 v200, v184, v185, s97 bitop3:0x7e\n"
      "v_bitop3_b32 v201, v186, v187, s97 bitop3:0x7e\n"
      "v_bitop3_b32 v202, v188, v189, s97 bitop3:0x7e\n"
      "v_or3_b32 v200, v200, v201, v202\n"
      "v_bitop3_b32 v201, v190, v191, s97 bitop3:0x7e\n"
      "v_bitop3_b32 v202, v192, v193, s97 bitop3:0x7e\n"
      "v_or3_b32 v200, v200, v201, v202\n"
      "v_bitop3_b32 v201, v194, v195, s97 bitop3:0x7e\n"
      "v_bitop3_b32 v202, v196, v197, s97 bitop3:0x7e\n"
      "v_or3_b32 v200, v200, v201, v202\n"
      "v_bitop3_b32 v201, v198, v199, s97 bitop3:0x7e\n"
      "v_bitop3_b32 v200, v200, v201, 1 bitop3:0xa8\n"
      "v_cmp_ne_u32 vcc, 0, v200\n"
      "s_cbranch_vccz .Lok134\n"
      "s_sub_u32 s77, s77, 1\n"
      "s_cmp_gt_i32 s77, 0\n"
      "s_cbranch_scc1 .Llp133\n"
      "s_branch .Lco132\n"
      ".Lok134:\n"
      "s_nop 1\n"
      "s_waitcnt vmcnt(0)\n"
      "s_branch .Lco132\n"
      ".Lst139:\n"
      ".Llp141:\n"
      "buffer_load_dword v217, v216, s[36:39], s78 offen offset:496 sc1\n"
      "s_waitcnt vmcnt(0)\n"
      "v_bitop3_b32 v217, v217, s97, 1 bitop3:0x28\n"
      "v_cmp_ne_u32 vcc, 0, v217\n"
      "s_cbranch_vccz .Lok142\n"
      "s_sub_u32 s77, s77, 1\n"
      "s_cmp_gt_i32 s77, 0\n"
      "s_cbranch_scc1 .Llp141\n"
      "s_branch .Lco140\n"
      ".Lok142:\n"
      "s_nop 1\n"
      "buffer_load_dwordx4 v[0:3], v224, s[36:39], s78 offen sc1\n"
      "buffer_load_dwordx4 v[4:7], v224, s[36:39], s78 offen offset:1024 sc1\n"
      "buffer_load_dwordx4 v[8:11], v224, s[36:39], s78 offen offset:2048 sc1\n"
      "buffer_load_dwordx4 v[12:15], v224, s[36:39], s78 offen offset:3072 sc1\n"
      "buffer_load_dwordx4 v[16:19], v224, s[36:39], s79 offen sc1\n"
      "buffer_load_dwordx4 v[20:23], v224, s[36:39], s79 offen offset:1024 sc1\n"
      "buffer_load_dwordx4 v[24:27], v224, s[36:39], s79 offen offset:2048 sc1\n"
      "buffer_load_dwordx4 v[28:31], v224, s[36:39], s79 offen offset:3072 sc1\n"
      "s_waitcnt vmcnt(0)\n"
      "v_bitop3_b32 v217, v0, v1, s97 bitop3:0x7e\n"
      "v_bitop3_b32 v254, v2, v3, s97 bitop3:0x7e\n"
      "v_bitop3_b32 v211, v4, v5, s97 bitop3:0x7e\n"
      "v_or3_b32 v217, v217, v254, v211\n"
      "v_bitop3_b32 v254, v6, v7, s97 bitop3:0x7e\n"
      "v_bitop3_b32 v217, v217, v254, 1 bitop3:0xa8\n"
      "v_cmp_ne_u32 vcc, 0, v217\n"
      "s_cbranch_vccz .Lco140\n"
      "s_sub_u32 s77, s77, 1\n"
      "s_cmp_gt_i32 s77, 0\n"
      "s_cbranch_scc1 .Llp141\n"
      "s_branch .Lco140\n"
      ".Lst143:\n"
      ".Llp145:\n"
      "buffer_load_dword v217, v216, s[36:39], s78 offen offset:496 sc1\n"
      "s_waitcnt vmcnt(0)\n"
      "v_bitop3_b32 v217, v217, s97, 1 bitop3:0x28\n"
      "v_cmp_ne_u32 vcc, 0, v217\n"
      "s_cbranch_vccz .Lok146\n"
      "s_sub_u32 s77, s77, 1\n"
      "s_cmp_gt_i32 s77, 0\n"
      "s_cbranch_scc1 .Llp145\n"
      "s_branch .Lco144\n"
      ".Lok146:\n"
      "s_nop 1\n"
      "buffer_load_dwordx4 v[8:11], v224, s[36:39], s78 offen offset:2048 sc1\n"
      "buffer_load_dwordx4 v[12:15], v224, s[36:39], s78 offen offset:3072 sc1\n"
      "buffer_load_dwordx4 v[16:19], v224, s[36:39], s79 offen sc1\n"
      "buffer_load_dwordx4 v[20:23], v224, s[36:39], s79 offen offset:1024 sc1\n"
      "buffer_load_dwordx4 v[24:27], v224, s[36:39], s79 offen offset:2048 sc1\n"
      "buffer_load_dwordx4 v[28:31], v224, s[36:39], s79 offen offset:3072 sc1\n"
      "buffer_load_dwordx4 v[32:35], v224, s[36:39], s80 offen sc1\n"
      "buffer_load_dwordx4 v[36:39], v224, s[36:39], s80 offen offset:1024 sc1\n"
      "buffer_load_dwordx4 v[40:43], v224, s[36:39], s80 offen offset:2048 sc1\n"
      "buffer_load_dwordx4 v[44:47], v224, s[36:39], s80 offen offset:3072 sc1\n"
      "s_waitcnt vmcnt(0)\n"
      "v_bitop3_b32 v217, v8, v9, s97 bitop3:0x7e\n"
      "v_bitop3_b32 v254, v10, v11, s97 bitop3:0x7e\n"
      "v_bitop3_b32 v211, v12, v13, s97 bitop3:0x7e\n"
      "v_or3_b32 v217, v217, v254, v211\n"
      "v_bitop3_b32 v254, v14, v15, s97 bitop3:0x7e\n"
      "v_bitop3_b32 v217, v217, v254, 1 bitop3:0xa8\n"
      "v_cmp_ne_u32 vcc, 0, v217\n"
      "s_cbranch_vccz .Lco144\n"
      "s_sub_u32 s77, s77, 1\n"
      "s_cmp_gt_i32 s77, 0\n"
      "s_cbranch_scc1 .Llp145\n"
      "s_branch .Lco144\n"
      ".Lst147:\n"
      ".Llp149:\n"
      "buffer_load_dword v217, v216, s[36:39], s78 offen offset:496 sc1\n"
      "s_waitcnt vmcnt(0)\n"
      "v_bitop3_b32 v217, v217, s97, 1 bitop3:0x28\n"
      "v_cmp_ne_u32 vcc, 0, v217\n"
      "s_cbranch_vccz .Lok150\n"
      "s_sub_u32 s77, s77, 1\n"
      "s_cmp_gt_i32 s77, 0\n"
      "s_cbranch_scc1 .Llp149\n"
      "s_branch .Lco148\n"
      ".Lok150:\n"
      "s_nop 1\n"
      "buffer_load_dwordx4 v[16:19], v224, s[36:39], s79 offen sc1\n"
      "buffer_load_dwordx4 v[20:23], v224, s[36:39], s79 offen offset:1024 sc1\n"
      "buffer_load_dwordx4 v[24:27], v224, s[36:39], s79 offen offset:2048 sc1\n"
      "buffer_load_dwordx4 v[28:31], v224, s[36:39], s79 offen offset:3072 sc1\n"
      "buffer_load_dwordx4 v[32:35], v224, s[36:39], s80 offen sc1\n"
      "buffer_load_dwordx4 v[36:39], v224, s[36:39], s80 offen offset:1024 sc1\n"
      "buffer_load_dwordx4 v[40:43], v224, s[36:39], s80 offen offset:2048 sc1\n"
      "buffer_load_dwordx4 v[44:47], v224, s[36:39], s80 offen offset:3072 sc1\n"
      "s_waitcnt vmcnt(0)\n"
      "v_bitop3_b32 v217, v16, v17, s97 bitop3:0x7e\n"
      "v_bitop3_b32 v254, v18, v19, s97 bitop3:0x7e\n"
      "v_bitop3_b32 v211, v20, v21, s97 bitop3:0x7e\n"
      "v_or3_b32 v217, v217, v254, v211\n"
      "v_bitop3_b32 v254, v22, v23, s97 bitop3:0x7e\n"
      "v_bitop3_b32 v217, v217, v254, 1 bitop3:0xa8\n"
      "v_cmp_ne_u32 vcc, 0, v217\n"
      "s_cbranch_vccz .Lco148\n"
      "s_sub_u32 s77, s77, 1\n"
      "s_cmp_gt_i32 s77, 0\n"
      "s_cbranch_scc1 .Llp149\n"
      "s_branch .Lco148\n"
      ".Lst151:\n"
      ".Llp153:\n"
      "buffer_load_dword v217, v216, s[36:39], s78 offen offset:496 sc1\n"
      "s_waitcnt vmcnt(0)\n"
      "v_bitop3_b32 v217, v217, s97, 1 bitop3:0x28\n"
      "v_cmp_ne_u32 vcc, 0, v217\n"
      "s_cbranch_vccz .Lok154\n"
      "s_sub_u32 s77, s77, 1\n"
      "s_cmp_gt_i32 s77, 0\n"
      "s_cbranch_scc1 .Llp153\n"
      "s_branch .Lco152\n"
      ".Lok154:\n"
      "s_nop 1\n"
      "buffer_load_dwordx4 v[24:27], v224, s[36:39], s79 offen offset:2048 sc1\n"
      "buffer_load_dwordx4 v[28:31], v224, s[36:39], s79 offen offset:3072 sc1\n"
      "buffer_load_dwordx4 v[32:35], v224, s[36:39], s80 offen sc1\n"
      "buffer_load_dwordx4 v[36:39], v224, s[36:39], s80 offen offset:1024 sc1\n"
      "buffer_load_dwordx4 v[40:43], v224, s[36:39], s80 offen offset:2048 sc1\n"
      "buffer_load_dwordx4 v[44:47], v224, s[36:39], s80 offen offset:3072 sc1\n"
      "buffer_load_dwordx4 v[48:51], v224, s[36:39], s81 offen sc1\n"
      "buffer_load_dwordx4 v[52:55], v224, s[36:39], s81 offen offset:1024 sc1\n"
      "buffer_load_dwordx4 v[56:59], v224, s[36:39], s81 offen offset:2048 sc1\n"
      "buffer_load_dwordx4 v[60:63], v224, s[36:39], s81 offen offset:3072 sc1\n"
      "s_waitcnt vmcnt(0)\n"
      "v_bitop3_b32 v217, v24, v25, s97 bitop3:0x7e\n"
      "v_bitop3_b32 v254, v26, v27, s97 bitop3:0x7e\n"
      "v_bitop3_b32 v211, v28, v29, s97 bitop3:0x7e\n"
      "v_or3_b32 v217, v217, v254, v211\n"
      "v_bitop3_b32 v254, v30, v31, s97 bitop3:0x7e\n"
      "v_bitop3_b32 v217, v217, v254, 1 bitop3:0xa8\n"
      "v_cmp_ne_u32 vcc, 0, v217\n"
      "s_cbranch_vccz .Lco152\n"
      "s_sub_u32 s77, s77, 1\n"
      "s_cmp_gt_i32 s77, 0\n"
      "s_cbranch_scc1 .Llp153\n"
      "s_branch .Lco152\n"
      ".Lst155:\n"
      ".Llp157:\n"
      "buffer_load_dword v217, v216, s[36:39], s78 offen offset:496 sc1\n"
      "s_waitcnt vmcnt(0)\n"
      "v_bitop3_b32 v217, v217, s97, 1 bitop3:0x28\n"
      "v_cmp_ne_u32 vcc, 0, v217\n"
      "s_cbranch_vccz .Lok158\n"
      "s_sub_u32 s77, s77, 1\n"
      "s_cmp_gt_i32 s77, 0\n"
      "s_cbranch_scc1 .Llp157\n"
      "s_branch .Lco156\n"
      ".Lok158:\n"
      "s_nop 1\n"
      "buffer_load_dwordx4 v[32:35], v224, s[36:39], s80 offen sc1\n"
      "buffer_load_dwordx4 v[36:39], v224, s[36:39], s80 offen offset:1024 sc1\n"
      "buffer_load_dwordx4 v[40:43], v224, s[36:39], s80 offen offset:2048 sc1\n"
      "buffer_load_dwordx4 v[44:47], v224, s[36:39], s80 offen offset:3072 sc1\n"
      "buffer_load_dwordx4 v[48:51], v224, s[36:39], s81 offen sc1\n"
      "buffer_load_dwordx4 v[52:55], v224, s[36:39], s81 offen offset:1024 sc1\n"
      "buffer_load_dwordx4 v[56:59], v224, s[36:39], s81 offen offset:2048 sc1\n"
      "buffer_load_dwordx4 v[60:63], v224, s[36:39], s81 offen offset:3072 sc1\n"
      "buffer_load_dwordx4 v[64:67], v224, s[36:39], s82 offen sc1\n"
      "buffer_load_dwordx4 v[68:71], v224, s[36:39], s82 offen offset:1024 sc1\n"
      "s_waitcnt vmcnt(0)\n"
      "v_bitop3_b32 v217, v32, v33, s97 bitop3:0x7e\n"
      "v_bitop3_b32 v254, v34, v35, s97 bitop3:0x7e\n"
      "v_bitop3_b32 v211, v36, v37, s97 bitop3:0x7e\n"
      "v_or3_b32 v217, v217, v254, v211\n"
      "v_bitop3_b32 v254, v38, v39, s97 bitop3:0x7e\n"
      "v_bitop3_b32 v217, v217, v254, 1 bitop3:0xa8\n"
      "v_cmp_ne_u32 vcc, 0, v217\n"
      "s_cbranch_vccz .Lco156\n"
      "s_sub_u32 s77, s77, 1\n"
      "s_cmp_gt_i32 s77, 0\n"
      "s_cbranch_scc1 .Llp157\n"
      "s_branch .Lco156\n"
      ".Lst159:\n"
      ".Llp161:\n"
      "buffer_load_dword v217, v216, s[36:39], s78 offen offset:496 sc1\n"
      "s_waitcnt vmcnt(0)\n"
      "v_bitop3_b32 v217, v217, s97, 1 bitop3:0x28\n"
      "v_cmp_ne_u32 vcc, 0, v217\n"
      "s_cbranch_vccz .Lok162\n"
      "s_sub_u32 s77, s77, 1\n"
      "s_cmp_gt_i32 s77, 0\n"
      "s_cbranch_scc1 .Llp161\n"
      "s_branch .Lco160\n"
      ".Lok162:\n"
      "s_nop 1\n"
      "buffer_load_dwordx4 v[40:43], v224, s[36:39], s80 offen offset:2048 sc1\n"
      "buffer_load_dwordx4 v[44:47], v224, s[36:39], s80 offen offset:3072 sc1\n"
      "buffer_load_dwordx4 v[48:51], v224, s[36:39], s81 offen sc1\n"
      "buffer_load_dwordx4 v[52:55], v224, s[36:39], s81 offen offset:1024 sc1\n"
      "buffer_load_dwordx4 v[56:59], v224, s[36:39], s81 offen offset:2048 sc1\n"
      "buffer_load_dwordx4 v[60:63], v224, s[36:39], s81 offen offset:3072 sc1\n"
      "buffer_load_dwordx4 v[64:67], v224, s[36:39], s82 offen sc1\n"
      "buffer_load_dwordx4 v[68:71], v224, s[36:39], s82 offen offset:1024 sc1\n"
      "buffer_load_dwordx4 v[72:75], v224, s[36:39], s82 offen offset:2048 sc1\n"
      "buffer_load_dwordx4 v[76:79], v224, s[36:39], s82 offen offset:3072 sc1\n"
      "s_waitcnt vmcnt(0)\n"
      "v_bitop3_b32 v217, v40, v41, s97 bitop3:0x7e\n"
      "v_bitop3_b32 v254, v42, v43, s97 bitop3:0x7e\n"
      "v_bitop3_b32 v211, v44, v45, s97 bitop3:0x7e\n"
      "v_or3_b32 v217, v217, v254, v211\n"
      "v_bitop3_b32 v254, v46, v47, s97 bitop3:0x7e\n"
      "v_bitop3_b32 v217, v217, v254, 1 bitop3:0xa8\n"
      "v_cmp_ne_u32 vcc, 0, v217\n"
      "s_cbranch_vccz .Lco160\n"
      "s_sub_u32 s77, s77, 1\n"
      "s_cmp_gt_i32 s77, 0\n"
      "s_cbranch_scc1 .Llp161\n"
      "s_branch .Lco160\n"
      ".Lst164:\n"
      ".Llp166:\n"
      "buffer_load_dword v217, v216, s[36:39], s78 offen offset:496 sc1\n"
      "s_waitcnt vmcnt(0)\n"
      "v_bitop3_b32 v217, v217, s97, 1 bitop3:0x28\n"
      "v_cmp_ne_u32 vcc, 0, v217\n"
      "s_cbranch_vccz .Lok167\n"
      "s_sub_u32 s77, s77, 1\n"
      "s_cmp_gt_i32 s77, 0\n"
      "s_cbranch_scc1 .Llp166\n"
      "s_branch .Lco165\n"
      ".Lok167:\n"
      "s_nop 1\n"
      "buffer_load_dwordx4 v[48:51], v224, s[36:39], s81 offen sc1\n"
      "buffer_load_dwordx4 v[52:55], v224, s[36:39], s81 offen offset:1024 sc1\n"
      "buffer_load_dwordx4 v[56:59], v224, s[36:39], s81 offen offset:2048 sc1\n"
      "buffer_load_dwordx4 v[60:63], v224, s[36:39], s81 offen offset:3072 sc1\n"
      "buffer_load_dwordx4 v[64:67], v224, s[36:39], s82 offen sc1\n"
      "buffer_load_dwordx4 v[68:71], v224, s[36:39], s82 offen offset:1024 sc1\n"
      "buffer_load_dwordx4 v[72:75], v224, s[36:39], s82 offen offset:2048 sc1\n"
      "buffer_load_dwordx4 v[76:79], v224, s[36:39], s82 offen offset:3072 sc1\n"
      "buffer_load_dwordx4 v[80:83], v224, s[36:39], s83 offen sc1\n"
      "buffer_load_dwordx4 v[84:87], v224, s[36:39], s83 offen offset:1024 sc1\n"
      "s_waitcnt vmcnt(0)\n"
      "v_bitop3_b32 v217, v48, v49, s97 bitop3:0x7e\n"
      "v_bitop3_b32 v254, v50, v51, s97 bitop3:0x7e\n"
      "v_bitop3_b32 v211, v52, v53, s97 bitop3:0x7e\n"
      "v_or3_b32 v217, v217, v254, v211\n"
      "v_bitop3_b32 v254, v54, v55, s97 bitop3:0x7e\n"
      "v_bitop3_b32 v217, v217, v254, 1 bitop3:0xa8\n"
      "v_cmp_ne_u32 vcc, 0, v217\n"
      "s_cbranch_vccz .Lco165\n"
      "s_sub_u32 s77, s77, 1\n"
      "s_cmp_gt_i32 s77, 0\n"
      "s_cbranch_scc1 .Llp166\n"
      "s_branch .Lco165\n"
      ".Lst168:\n"
      ".Llp170:\n"
      "buffer_load_dword v217, v216, s[36:39], s78 offen offset:496 sc1\n"
      "s_waitcnt vmcnt(0)\n"
      "v_bitop3_b32 v217, v217, s97, 1 bitop3:0x28\n"
      "v_cmp_ne_u32 vcc, 0, v217\n"
      "s_cbranch_vccz .Lok171\n"
      "s_sub_u32 s77, s77, 1\n"
      "s_cmp_gt_i32 s77, 0\n"
      "s_cbranch_scc1 .Llp170\n"
      "s_branch .Lco169\n"
      ".Lok171:\n"
      "s_nop 1\n"
      "buffer_load_dwordx4 v[56:59], v224, s[36:39], s81 offen offset:2048 sc1\n"
      "buffer_load_dwordx4 v[60:63], v224, s[36:39], s81 offen offset:3072 sc1\n"
      "buffer_load_dwordx4 v[64:67], v224, s[36:39], s82 offen sc1\n"
      "buffer_load_dwordx4 v[68:71], v224, s[36:39], s82 offen offset:1024 sc1\n"
      "buffer_load_dwordx4 v[72:75], v224, s[36:39], s82 offen offset:2048 sc1\n"
      "buffer_load_dwordx4 v[76:79], v224, s[36:39], s82 offen offset:3072 sc1\n"
      "buffer_load_dwordx4 v[80:83], v224, s[36:39], s83 offen sc1\n"
      "buffer_load_dwordx4 v[84:87], v224, s[36:39], s83 offen offset:1024 sc1\n"
      "buffer_load_dwordx4 v[88:91], v224, s[36:39], s83 offen offset:2048 sc1\n"
      "buffer_load_dwordx4 v[92:95], v224, s[36:39], s83 offen offset:3072 sc1\n"
      "s_waitcnt vmcnt(0)\n"
      "v_bitop3_b32 v217, v56, v57, s97 bitop3:0x7e\n"
      "v_bitop3_b32 v254, v58, v59, s97 bitop3:0x7e\n"
      "v_bitop3_b32 v211, v60, v61, s97 bitop3:0x7e\n"
      "v_or3_b32 v217, v217, v254, v211\n"
      "v_bitop3_b32 v254, v62, v63, s97 bitop3:0x7e\n"
      "v_bitop3_b32 v217, v217, v254, 1 bitop3:0xa8\n"
      "v_cmp_ne_u32 vcc, 0, v217\n"
      "s_cbranch_vccz .Lco169\n"
      "s_sub_u32 s77, s77, 1\n"
      "s_cmp_gt_i32 s77, 0\n"
      "s_cbranch_scc1 .Llp170\n"
      "s_branch .Lco169\n"
      ".Lst172:\n"
      ".Llp174:\n"
      "buffer_load_dword v217, v216, s[36:39], s78 offen offset:496 sc1\n"
      "s_waitcnt vmcnt(0)\n"
      "v_bitop3_b32 v217, v217, s97, 1 bitop3:0x28\n"
      "v_cmp_ne_u32 vcc, 0, v217\n"
      "s_cbranch_vccz .Lok175\n"
      "s_sub_u32 s77, s77, 1\n"
      "s_cmp_gt_i32 s77, 0\n"
      "s_cbranch_scc1 .Llp174\n"
      "s_branch .Lco173\n"
      ".Lok175:\n"
      "s_nop 1\n"
      "buffer_load_dwordx4 v[64:67], v224, s[36:39], s82 offen sc1\n"
      "buffer_load_dwordx4 v[68:71], v224, s[36:39], s82 offen offset:1024 sc1\n"
      "buffer_load_dwordx4 v[72:75], v224, s[36:39], s82 offen offset:2048 sc1\n"
      "buffer_load_dwordx4 v[76:79], v224, s[36:39], s82 offen offset:3072 sc1\n"
      "buffer_load_dwordx4 v[80:83], v224, s[36:39], s83 offen sc1\n"
      "buffer_load_dwordx4 v[84:87], v224, s[36:39], s83 offen offset:1024 sc1\n"
      "buffer_load_dwordx4 v[88:91], v224, s[36:39], s83 offen offset:2048 sc1\n"
      "buffer_load_dwordx4 v[92:95], v224, s[36:39], s83 offen offset:3072 sc1\n"
      "buffer_load_dwordx4 v[96:99], v224, s[36:39], s84 offen sc1\n"
      "buffer_load_dwordx4 v[100:103], v224, s[36:39], s84 offen offset:1024 sc1\n"
      "s_waitcnt vmcnt(0)\n"
      "v_bitop3_b32 v217, v64, v65, s97 bitop3:0x7e\n"
      "v_bitop3_b32 v254, v66, v67, s97 bitop3:0x7e\n"
      "v_bitop3_b32 v211, v68, v69, s97 bitop3:0x7e\n"
      "v_or3_b32 v217, v217, v254, v211\n"
      "v_bitop3_b32 v254, v70, v71, s97 bitop3:0x7e\n"
      "v_bitop3_b32 v217, v217, v254, 1 bitop3:0xa8\n"
      "v_cmp_ne_u32 vcc, 0, v217\n"
      "s_cbranch_vccz .Lco173\n"
      "s_sub_u32 s77, s77, 1\n"
      "s_cmp_gt_i32 s77, 0\n"
      "s_cbranch_scc1 .Llp174\n"
      "s_branch .Lco173\n"
      ".Lst176:\n"
      ".Llp178:\n"
      "buffer_load_dword v217, v216, s[36:39], s78 offen offset:496 sc1\n"
      "s_waitcnt vmcnt(0)\n"
      "v_bitop3_b32 v217, v217, s97, 1 bitop3:0x28\n"
      "v_cmp_ne_u32 vcc, 0, v217\n"
      "s_cbranch_vccz .Lok179\n"
      "s_sub_u32 s77, s77, 1\n"
      "s_cmp_gt_i32 s77, 0\n"
      "s_cbranch_scc1 .Llp178\n"
      "s_branch .Lco177\n"
      ".Lok179:\n"
      "s_nop 1\n"
      "buffer_load_dwordx4 v[72:75], v224, s[36:39], s82 offen offset:2048 sc1\n"
      "buffer_load_dwordx4 v[76:79], v224, s[36:39], s82 offen offset:3072 sc1\n"
      "buffer_load_dwordx4 v[80:83], v224, s[36:39], s83 offen sc1\n"
      "buffer_load_dwordx4 v[84:87], v224, s[36:39], s83 offen offset:1024 sc1\n"
      "buffer_load_dwordx4 v[88:91], v224, s[36:39], s83 offen offset:2048 sc1\n"
      "buffer_load_dwordx4 v[92:95], v224, s[36:39], s83 offen offset:3072 sc1\n"
      "buffer_load_dwordx4 v[96:99], v224, s[36:39], s84 offen sc1\n"
      "buffer_load_dwordx4 v[100:103], v224, s[36:39], s84 offen offset:1024 sc1\n"
      "buffer_load_dwordx4 v[104:107], v224, s[36:39], s84 offen offset:2048 sc1\n"
      "buffer_load_dwordx4 v[108:111], v224, s[36:39], s84 offen offset:3072 sc1\n"
      "s_waitcnt vmcnt(0)\n"
      "v_bitop3_b32 v217, v72, v73, s97 bitop3:0x7e\n"
      "v_bitop3_b32 v254, v74, v75, s97 bitop3:0x7e\n"
      "v_bitop3_b32 v211, v76, v77, s97 bitop3:0x7e\n"
      "v_or3_b32 v217, v217, v254, v211\n"
      "v_bitop3_b32 v254, v78, v79, s97 bitop3:0x7e\n"
      "v_bitop3_b32 v217, v217, v254, 1 bitop3:0xa8\n"
      "v_cmp_ne_u32 vcc, 0, v217\n"
      "s_cbranch_vccz .Lco177\n"
      "s_sub_u32 s77, s77, 1\n"
      "s_cmp_gt_i32 s77, 0\n"
      "s_cbranch_scc1 .Llp178\n"
      "s_branch .Lco177\n"
      ".Lst180:\n"
      ".Llp182:\n"
      "buffer_load_dword v217, v216, s[36:39], s78 offen offset:496 sc1\n"
      "s_waitcnt vmcnt(0)\n"
      "v_bitop3_b32 v217, v217, s97, 1 bitop3:0x28\n"
      "v_cmp_ne_u32 vcc, 0, v217\n"
      "s_cbranch_vccz .Lok183\n"
      "s_sub_u32 s77, s77, 1\n"
      "s_cmp_gt_i32 s77, 0\n"
      "s_cbranch_scc1 .Llp182\n"
      "s_branch .Lco181\n"
      ".Lok183:\n"
      "s_nop 1\n"
      "buffer_load_dwordx4 v[80:83], v224, s[36:39], s83 offen sc1\n"
      "buffer_load_dwordx4 v[84:87], v224, s[36:39], s83 offen offset:1024 sc1\n"
      "buffer_load_dwordx4 v[88:91], v224, s[36:39], s83 offen offset:2048 sc1\n"
      "buffer_load_dwordx4 v[92:95], v224, s[36:39], s83 offen offset:3072 sc1\n"
      "buffer_load_dwordx4 v[96:99], v224, s[36:39], s84 offen sc1\n"
      "buffer_load_dwordx4 v[100:103], v224, s[36:39], s84 offen offset:1024 sc1\n"
      "buffer_load_dwordx4 v[104:107], v224, s[36:39], s84 offen offset:2048 sc1\n"
      "buffer_load_dwordx4 v[108:111], v224, s[36:39], s84 offen offset:3072 sc1\n"
      "s_waitcnt vmcnt(0)\n"
      "v_bitop3_b32 v217, v80, v81, s97 bitop3:0x7e\n"
      "v_bitop3_b32 v254, v82, v83, s97 bitop3:0x7e\n"
      "v_bitop3_b32 v211, v84, v85, s97 bitop3:0x7e\n"
      "v_or3_b32 v217, v217, v254, v211\n"
      "v_bitop3_b32 v254, v86, v87, s97 bitop3:0x7e\n"
      "v_bitop3_b32 v217, v217, v254, 1 bitop3:0xa8\n"
      "v_cmp_ne_u32 vcc, 0, v217\n"
      "s_cbranch_vccz .Lco181\n"
      "s_sub_u32 s77, s77, 1\n"
      "s_cmp_gt_i32 s77, 0\n"
      "s_cbranch_scc1 .Llp182\n"
      "s_branch .Lco181\n"
      ".Lst184:\n"
      ".Llp186:\n"
      "buffer_load_dword v217, v216, s[36:39], s78 offen offset:496 sc1\n"
      "s_waitcnt vmcnt(0)\n"
      "v_bitop3_b32 v217, v217, s97, 1 bitop3:0x28\n"
      "v_cmp_ne_u32 vcc, 0, v217\n"
      "s_cbranch_vccz .Lok187\n"
      "s_sub_u32 s77, s77, 1\n"
      "s_cmp_gt_i32 s77, 0\n"
      "s_cbranch_scc1 .Llp186\n"
      "s_branch .Lco185\n"
      ".Lok187:\n"
      "s_nop 1\n"
      "buffer_load_dwordx4 v[88:91], v224, s[36:39], s83 offen offset:2048 sc1\n"
      "buffer_load_dwordx4 v[92:95], v224, s[36:39], s83 offen offset:3072 sc1\n"
      "buffer_load_dwordx4 v[96:99], v224, s[36:39], s84 offen sc1\n"
      "buffer_load_dwordx4 v[100:103], v224, s[36:39], s84 offen offset:1024 sc1\n"
      "buffer_load_dwordx4 v[104:107], v224, s[36:39], s84 offen offset:2048 sc1\n"
      "buffer_load_dwordx4 v[108:111], v224, s[36:39], s84 offen offset:3072 sc1\n"
      "s_waitcnt vmcnt(0)\n"
      "v_bitop3_b32 v217, v88, v89, s97 bitop3:0x7e\n"
      "v_bitop3_b32 v254, v90, v91, s97 bitop3:0x7e\n"
      "v_bitop3_b32 v211, v92, v93, s97 bitop3:0x7e\n"
      "v_or3_b32 v217, v217, v254, v211\n"
      "v_bitop3_b32 v254, v94, v95, s97 bitop3:0x7e\n"
      "v_bitop3_b32 v217, v217, v254, 1 bitop3:0xa8\n"
      "v_cmp_ne_u32 vcc, 0, v217\n"
      "s_cbranch_vccz .Lco185\n"
      "s_sub_u32 s77, s77, 1\n"
      "s_cmp_gt_i32 s77, 0\n"
      "s_cbranch_scc1 .Llp186\n"
      "s_branch .Lco185\n"
      ".Lst188:\n"
      ".Llp190:\n"
      "buffer_load_dword v217, v216, s[36:39], s78 offen offset:496 sc1\n"
      "s_waitcnt vmcnt(0)\n"
      "v_bitop3_b32 v217, v217, s97, 1 bitop3:0x28\n"
      "v_cmp_ne_u32 vcc, 0, v217\n"
      "s_cbranch_vccz .Lok191\n"
      "s_sub_u32 s77, s77, 1\n"
      "s_cmp_gt_i32 s77, 0\n"
      "s_cbranch_scc1 .Llp190\n"
      "s_branch .Lco189\n"
      ".Lok191:\n"
      "s_nop 1\n"
      "buffer_load_dwordx4 v[96:99], v224, s[36:39], s84 offen sc1\n"
      "buffer_load_dwordx4 v[100:103], v224, s[36:39], s84 offen offset:1024 sc1\n"
      "buffer_load_dwordx4 v[104:107], v224, s[36:39], s84 offen offset:2048 sc1\n"
      "buffer_load_dwordx4 v[108:111], v224, s[36:39], s84 offen offset:3072 sc1\n"
      "s_waitcnt vmcnt(0)\n"
      "v_bitop3_b32 v217, v96, v97, s97 bitop3:0x7e\n"
      "v_bitop3_b32 v254, v98, v99, s97 bitop3:0x7e\n"
      "v_bitop3_b32 v211, v100, v101, s97 bitop3:0x7e\n"
      "v_or3_b32 v217, v217, v254, v211\n"
      "v_bitop3_b32 v254, v102, v103, s97 bitop3:0x7e\n"
      "v_bitop3_b32 v217, v217, v254, 1 bitop3:0xa8\n"
      "v_cmp_ne_u32 vcc, 0, v217\n"
      "s_cbranch_vccz .Lco189\n"
      "s_sub_u32 s77, s77, 1\n"
      "s_cmp_gt_i32 s77, 0\n"
      "s_cbranch_scc1 .Llp190\n"
      "s_branch .Lco189\n"
      ".Lst192:\n"
      ".Llp194:\n"
      "buffer_load_dword v217, v216, s[36:39], s78 offen offset:496 sc1\n"
      "s_waitcnt vmcnt(0)\n"
      "v_bitop3_b32 v217, v217, s97, 1 bitop3:0x28\n"
      "v_cmp_ne_u32 vcc, 0, v217\n"
      "s_cbranch_vccz .Lok195\n"
      "s_sub_u32 s77, s77, 1\n"
      "s_cmp_gt_i32 s77, 0\n"
      "s_cbranch_scc1 .Llp194\n"
      "s_branch .Lco193\n"
      ".Lok195:\n"
      "s_nop 1\n"
      "buffer_load_dwordx4 v[104:107], v224, s[36:39], s84 offen offset:2048 sc1\n"
      "buffer_load_dwordx4 v[108:111], v224, s[36:39], s84 offen offset:3072 sc1\n"
      "s_waitcnt vmcnt(0)\n"
      "v_bitop3_b32 v217, v104, v105, s97 bitop3:0x7e\n"
      "v_bitop3_b32 v254, v106, v107, s97 bitop3:0x7e\n"
      "v_bitop3_b32 v211, v108, v109, s97 bitop3:0x7e\n"
      "v_or3_b32 v217, v217, v254, v211\n"
      "v_bitop3_b32 v254, v110, v111, s97 bitop3:0x7e\n"
      "v_bitop3_b32 v217, v217, v254, 1 bitop3:0xa8\n"
      "v_cmp_ne_u32 vcc, 0, v217\n"
      "s_cbranch_vccz .Lco193\n"
      "s_sub_u32 s77, s77, 1\n"
      "s_cmp_gt_i32 s77, 0\n"
      "s_cbranch_scc1 .Llp194\n"
      "s_branch .Lco193\n"
      ".Lst198:\n"
      ".Llp200:\n"
      "s_nop 1\n"
      "buffer_load_dwordx4 v[0:3], v192, s[36:39], s74 offen sc1\n"
      "buffer_load_dwordx4 v[4:7], v193, s[36:39], s74 offen sc1\n"
      "s_waitcnt vmcnt(0)\n"
      "v_bitop3_b32 v217, v0, v1, s97 bitop3:0x7e\n"
      "v_bitop3_b32 v216, v2, v3, s97 bitop3:0x7e\n"
      "v_bitop3_b32 v196, v4, v5, s97 bitop3:0x7e\n"
      "v_or3_b32 v217, v217, v216, v196\n"
      "v_bitop3_b32 v216, v6, v7, s97 bitop3:0x7e\n"
      "v_bitop3_b32 v217, v217, v216, 1 bitop3:0xa8\n"
      "v_cmp_ne_u32 vcc, 0, v217\n"
      "s_cbranch_vccz .Lok201\n"
      "s_sub_u32 s77, s77, 1\n"
      "s_cmp_gt_i32 s77, 0\n"
      "s_cbranch_scc1 .Llp200\n"
      "s_branch .Lco199\n"
      ".Lok201:\n"
      "s_nop 1\n"
      "buffer_load_dwordx4 v[8:11], v194, s[36:39], s74 offen sc1\n"
      "buffer_load_dwordx4 v[12:15], v195, s[36:39], s74 offen sc1\n"
      "buffer_load_dwordx4 v[16:19], v192, s[36:39], s74 offen offset:1024 sc1\n"
      "buffer_load_dwordx4 v[20:23], v193, s[36:39], s74 offen offset:1024 sc1\n"
      "buffer_load_dwordx4 v[24:27], v194, s[36:39], s74 offen offset:1024 sc1\n"
      "buffer_load_dwordx4 v[28:31], v195, s[36:39], s74 offen offset:1024 sc1\n"
      "buffer_load_dwordx4 v[32:35], v192, s[36:39], s74 offen offset:2048 sc1\n"
      "buffer_load_dwordx4 v[36:39], v193, s[36:39], s74 offen offset:2048 sc1\n"
      "buffer_load_dwordx4 v[40:43], v194, s[36:39], s74 offen offset:2048 sc1\n"
      "s_waitcnt vmcnt(0)\n"
      "s_branch .Lco199\n"
      ".Lst202:\n"
      ".Llp204:\n"
      "s_nop 1\n"
      "buffer_load_dwordx4 v[8:11], v194, s[36:39], s74 offen sc1\n"
      "buffer_load_dwordx4 v[12:15], v195, s[36:39], s74 offen sc1\n"
      "s_waitcnt vmcnt(0)\n"
      "v_bitop3_b32 v217, v8, v9, s97 bitop3:0x7e\n"
      "v_bitop3_b32 v216, v10, v11, s97 bitop3:0x7e\n"
      "v_bitop3_b32 v196, v12, v13, s97 bitop3:0x7e\n"
      "v_or3_b32 v217, v217, v216, v196\n"
      "v_bitop3_b32 v216, v14, v15, s97 bitop3:0x7e\n"
      "v_bitop3_b32 v217, v217, v216, 1 bitop3:0xa8\n"
      "v_cmp_ne_u32 vcc, 0, v217\n"
      "s_cbranch_vccz .Lok205\n"
      "s_sub_u32 s77, s77, 1\n"
      "s_cmp_gt_i32 s77, 0\n"
      "s_cbranch_scc1 .Llp204\n"
      "s_branch .Lco203\n"
      ".Lok205:\n"
      "s_nop 1\n"
      "buffer_load_dwordx4 v[16:19], v192, s[36:39], s74 offen offset:1024 sc1\n"
      "buffer_load_dwordx4 v[20:23], v193, s[36:39], s74 offen offset:1024 sc1\n"
      "buffer_load_dwordx4 v[24:27], v194, s[36:39], s74 offen offset:1024 sc1\n"
      "buffer_load_dwordx4 v[28:31], v195, s[36:39], s74 offen offset:1024 sc1\n"
      "buffer_load_dwordx4 v[32:35], v192, s[36:39], s74 offen offset:2048 sc1\n"
      "buffer_load_dwordx4 v[36:39], v193, s[36:39], s74 offen offset:2048 sc1\n"
      "buffer_load_dwordx4 v[40:43], v194, s[36:39], s74 offen offset:2048 sc1\n"
      "buffer_load_dwordx4 v[44:47], v195, s[36:39], s74 offen offset:2048 sc1\n"
      "buffer_load_dwordx4 v[48:51], v192, s[36:39], s74 offen offset:3072 sc1\n"
      "s_waitcnt vmcnt(0)\n"
      "s_branch .Lco203\n"
      ".Lst206:\n"
      ".Llp208:\n"
      "s_nop 1\n"
      "buffer_load_dwordx4 v[16:19], v192, s[36:39], s74 offen offset:1024 sc1\n"
      "buffer_load_dwordx4 v[20:23], v193, s[36:39], s74 offen offset:1024 sc1\n"
      "s_waitcnt vmcnt(0)\n"
      "v_bitop3_b32 v217, v16, v17, s97 bitop3:0x7e\n"
      "v_bitop3_b32 v216, v18, v19, s97 bitop3:0x7e\n"
      "v_bitop3_b32 v196, v20, v21, s97 bitop3:0x7e\n"
      "v_or3_b32 v217, v217, v216, v196\n"
      "v_bitop3_b32 v216, v22, v23, s97 bitop3:0x7e\n"
      "v_bitop3_b32 v217, v217, v216, 1 bitop3:0xa8\n"
      "v_cmp_ne_u32 vcc, 0, v217\n"
      "s_cbranch_vccz .Lok209\n"
      "s_sub_u32 s77, s77, 1\n"
      "s_cmp_gt_i32 s77, 0\n"
      "s_cbranch_scc1 .Llp208\n"
      "s_branch .Lco207\n"
      ".Lok209:\n"
      "s_nop 1\n"
      "buffer_load_dwordx4 v[24:27], v194, s[36:39], s74 offen offset:1024 sc1\n"
      "buffer_load_dwordx4 v[28:31], v195, s[36:39], s74 offen offset:1024 sc1\n"
      "buffer_load_dwordx4 v[32:35], v192, s[36:39], s74 offen offset:2048 sc1\n"
      "buffer_load_dwordx4 v[36:39], v193, s[36:39], s74 offen offset:2048 sc1\n"
      "buffer_load_dwordx4 v[40:43], v194, s[36:39], s74 offen offset:2048 sc1\n"
      "buffer_load_dwordx4 v[44:47], v195, s[36:39], s74 offen offset:2048 sc1\n"
      "buffer_load_dwordx4 v[48:51], v192, s[36:39], s74 offen offset:3072 sc1\n"
      "buffer_load_dwordx4 v[52:55], v193, s[36:39], s74 offen offset:3072 sc1\n"
      "buffer_load_dwordx4 v[56:59], v194, s[36:39], s74 offen offset:3072 sc1\n"
      "s_waitcnt vmcnt(0)\n"
      "s_branch .Lco207\n"
      ".Lst210:\n"
      ".Llp212:\n"
      "s_nop 1\n"
      "buffer_load_dwordx4 v[24:27], v194, s[36:39], s74 offen offset:1024 sc1\n"
      "buffer_load_dwordx4 v[28:31], v195, s[36:39], s74 offen offset:1024 sc1\n"
      "s_waitcnt vmcnt(0)\n"
      "v_bitop3_b32 v217, v24, v25, s97 bitop3:0x7e\n"
      "v_bitop3_b32 v216, v26, v27, s97 bitop3:0x7e\n"
      "v_bitop3_b32 v196, v28, v29, s97 bitop3:0x7e\n"
      "v_or3_b32 v217, v217, v216, v196\n"
      "v_bitop3_b32 v216, v30, v31, s97 bitop3:0x7e\n"
      "v_bitop3_b32 v217, v217, v216, 1 bitop3:0xa8\n"
      "v_cmp_ne_u32 vcc, 0, v217\n"
      "s_cbranch_vccz .Lok213\n"
      "s_sub_u32 s77, s77, 1\n"
      "s_cmp_gt_i32 s77, 0\n"
      "s_cbranch_scc1 .Llp212\n"
      "s_branch .Lco211\n"
      ".Lok213:\n"
      "s_nop 1\n"
      "buffer_load_dwordx4 v[32:35], v192, s[36:39], s74 offen offset:2048 sc1\n"
      "buffer_load_dwordx4 v[36:39], v193, s[36:39], s74 offen offset:2048 sc1\n"
      "buffer_load_dwordx4 v[40:43], v194, s[36:39], s74 offen offset:2048 sc1\n"
      "buffer_load_dwordx4 v[44:47], v195, s[36:39], s74 offen offset:2048 sc1\n"
      "buffer_load_dwordx4 v[48:51], v192, s[36:39], s74 offen offset:3072 sc1\n"
      "buffer_load_dwordx4 v[52:55], v193, s[36:39], s74 offen offset:3072 sc1\n"
      "buffer_load_dwordx4 v[56:59], v194, s[36:39], s74 offen offset:3072 sc1\n"
      "buffer_load_dwordx4 v[60:63], v195, s[36:39], s74 offen offset:3072 sc1\n"
      "buffer_load_dwordx4 v[64:67], v192, s[36:39], s89 offen sc1\n"
      "s_waitcnt vmcnt(0)\n"
      "s_branch .Lco211\n"
      ".Lst214:\n"
      ".Llp216:\n"
      "s_nop 1\n"
      "buffer_load_dwordx4 v[32:35], v192, s[36:39], s74 offen offset:2048 sc1\n"
      "buffer_load_dwordx4 v[36:39], v193, s[36:39], s74 offen offset:2048 sc1\n"
      "s_waitcnt vmcnt(0)\n"
      "v_bitop3_b32 v217, v32, v33, s97 bitop3:0x7e\n"
      "v_bitop3_b32 v216, v34, v35, s97 bitop3:0x7e\n"
      "v_bitop3_b32 v196, v36, v37, s97 bitop3:0x7e\n"
      "v_or3_b32 v217, v217, v216, v196\n"
      "v_bitop3_b32 v216, v38, v39, s97 bitop3:0x7e\n"
      "v_bitop3_b32 v217, v217, v216, 1 bitop3:0xa8\n"
      "v_cmp_ne_u32 vcc, 0, v217\n"
      "s_cbranch_vccz .Lok217\n"
      "s_sub_u32 s77, s77, 1\n"
      "s_cmp_gt_i32 s77, 0\n"
      "s_cbranch_scc1 .Llp216\n"
      "s_branch .Lco215\n"
      ".Lok217:\n"
      "s_nop 1\n"
      "buffer_load_dwordx4 v[40:43], v194, s[36:39], s74 offen offset:2048 sc1\n"
      "buffer_load_dwordx4 v[44:47], v195, s[36:39], s74 offen offset:2048 sc1\n"
      "buffer_load_dwordx4 v[48:51], v192, s[36:39], s74 offen offset:3072 sc1\n"
      "buffer_load_dwordx4 v[52:55], v193, s[36:39], s74 offen offset:3072 sc1\n"
      "buffer_load_dwordx4 v[56:59], v194, s[36:39], s74 offen offset:3072 sc1\n"
      "buffer_load_dwordx4 v[60:63], v195, s[36:39], s74 offen offset:3072 sc1\n"
      "buffer_load_dwordx4 v[64:67], v192, s[36:39], s89 offen sc1\n"
      "buffer_load_dwordx4 v[68:71], v193, s[36:39], s89 offen sc1\n"
      "buffer_load_dwordx4 v[72:75], v194, s[36:39], s89 offen sc1\n"
      "s_waitcnt vmcnt(0)\n"
      "s_branch .Lco215\n"
      ".Lst218:\n"
      ".Llp220:\n"
      "s_nop 1\n"
      "buffer_load_dwordx4 v[40:43], v194, s[36:39], s74 offen offset:2048 sc1\n"
      "buffer_load_dwordx4 v[44:47], v195, s[36:39], s74 offen offset:2048 sc1\n"
      "s_waitcnt vmcnt(0)\n"
      "v_bitop3_b32 v217, v40, v41, s97 bitop3:0x7e\n"
      "v_bitop3_b32 v216, v42, v43, s97 bitop3:0x7e\n"
      "v_bitop3_b32 v196, v44, v45, s97 bitop3:0x7e\n"
      "v_or3_b32 v217, v217, v216, v196\n"
      "v_bitop3_b32 v216, v46, v47, s97 bitop3:0x7e\n"
      "v_bitop3_b32 v217, v217, v216, 1 bitop3:0xa8\n"
      "v_cmp_ne_u32 vcc, 0, v217\n"
      "s_cbranch_vccz .Lok221\n"
      "s_sub_u32 s77, s77, 1\n"
      "s_cmp_gt_i32 s77, 0\n"
      "s_cbranch_scc1 .Llp220\n"
      "s_branch .Lco219\n"
      ".Lok221:\n"
      "s_nop 1\n"
      "buffer_load_dwordx4 v[48:51], v192, s[36:39], s74 offen offset:3072 sc1\n"
      "buffer_load_dwordx4 v[52:55], v193, s[36:39], s74 offen offset:3072 sc1\n"
      "buffer_load_dwordx4 v[56:59], v194, s[36:39], s74 offen offset:3072 sc1\n"
      "buffer_load_dwordx4 v[60:63], v195, s[36:39], s74 offen offset:3072 sc1\n"
      "buffer_load_dwordx4 v[64:67], v192, s[36:39], s89 offen sc1\n"
      "buffer_load_dwordx4 v[68:71], v193, s[36:39], s89 offen sc1\n"
      "buffer_load_dwordx4 v[72:75], v194, s[36:39], s89 offen sc1\n"
      "buffer_load_dwordx4 v[76:79], v195, s[36:39], s89 offen sc1\n"
      "buffer_load_dwordx4 v[80:83], v192, s[36:39], s89 offen offset:1024 sc1\n"
      "s_waitcnt vmcnt(0)\n"
      "s_branch .Lco219\n"
      ".Lst222:\n"
      ".Llp224:\n"
      "s_nop 1\n"
      "buffer_load_dwordx4 v[48:51], v192, s[36:39], s74 offen offset:3072 sc1\n"
      "buffer_load_dwordx4 v[52:55], v193, s[36:39], s74 offen offset:3072 sc1\n"
      "s_waitcnt vmcnt(0)\n"
      "v_bitop3_b32 v217, v48, v49, s97 bitop3:0x7e\n"
      "v_bitop3_b32 v216, v50, v51, s97 bitop3:0x7e\n"
      "v_bitop3_b32 v196, v52, v53, s97 bitop3:0x7e\n"
      "v_or3_b32 v217, v217, v216, v196\n"
      "v_bitop3_b32 v216, v54, v55, s97 bitop3:0x7e\n"
      "v_bitop3_b32 v217, v217, v216, 1 bitop3:0xa8\n"
      "v_cmp_ne_u32 vcc, 0, v217\n"
      "s_cbranch_vccz .Lok225\n"
      "s_sub_u32 s77, s77, 1\n"
      "s_cmp_gt_i32 s77, 0\n"
      "s_cbranch_scc1 .Llp224\n"
      "s_branch .Lco223\n"
      ".Lok225:\n"
      "s_nop 1\n"
      "buffer_load_dwordx4 v[56:59], v194, s[36:39], s74 offen offset:3072 sc1\n"
      "buffer_load_dwordx4 v[60:63], v195, s[36:39], s74 offen offset:3072 sc1\n"
      "buffer_load_dwordx4 v[64:67], v192, s[36:39], s89 offen sc1\n"
      "buffer_load_dwordx4 v[68:71], v193, s[36:39], s89 offen sc1\n"
      "buffer_load_dwordx4 v[72:75], v194, s[36:39], s89 offen sc1\n"
      "buffer_load_dwordx4 v[76:79], v195, s[36:39], s89 offen sc1\n"
      "buffer_load_dwordx4 v[80:83], v192, s[36:39], s89 offen offset:1024 sc1\n"
      "buffer_load_dwordx4 v[84:87], v193, s[36:39], s89 offen offset:1024 sc1\n"
      "buffer_load_dwordx4 v[88:91], v194, s[36:39], s89 offen offset:1024 sc1\n"
      "s_waitcnt vmcnt(0)\n"
      "s_branch .Lco223\n"
      ".Lst226:\n"
      ".Llp228:\n"
      "s_nop 1\n"
      "buffer_load_dwordx4 v[56:59], v194, s[36:39], s74 offen offset:3072 sc1\n"
      "buffer_load_dwordx4 v[60:63], v195, s[36:39], s74 offen offset:3072 sc1\n"
      "s_waitcnt vmcnt(0)\n"
      "v_bitop3_b32 v217, v56, v57, s97 bitop3:0x7e\n"
      "v_bitop3_b32 v216, v58, v59, s97 bitop3:0x7e\n"
      "v_bitop3_b32 v196, v60, v61, s97 bitop3:0x7e\n"
      "v_or3_b32 v217, v217, v216, v196\n"
      "v_bitop3_b32 v216, v62, v63, s97 bitop3:0x7e\n"
      "v_bitop3_b32 v217, v217, v216, 1 bitop3:0xa8\n"
      "v_cmp_ne_u32 vcc, 0, v217\n"
      "s_cbranch_vccz .Lok229\n"
      "s_sub_u32 s77, s77, 1\n"
      "s_cmp_gt_i32 s77, 0\n"
      "s_cbranch_scc1 .Llp228\n"
      "s_branch .Lco227\n"
      ".Lok229:\n"
      "s_nop 1\n"
      "buffer_load_dwordx4 v[64:67], v192, s[36:39], s89 offen sc1\n"
      "buffer_load_dwordx4 v[68:71], v193, s[36:39], s89 offen sc1\n"
      "buffer_load_dwordx4 v[72:75], v194, s[36:39], s89 offen sc1\n"
      "buffer_load_dwordx4 v[76:79], v195, s[36:39], s89 offen sc1\n"
      "buffer_load_dwordx4 v[80:83], v192, s[36:39], s89 offen offset:1024 sc1\n"
      "buffer_load_dwordx4 v[84:87], v193, s[36:39], s89 offen offset:1024 sc1\n"
      "buffer_load_dwordx4 v[88:91], v194, s[36:39], s89 offen offset:1024 sc1\n"
      "buffer_load_dwordx4 v[92:95], v195, s[36:39], s89 offen offset:1024 sc1\n"
      "buffer_load_dwordx4 v[96:99], v192, s[36:39], s89 offen offset:2048 sc1\n"
      "s_waitcnt vmcnt(0)\n"
      "s_branch .Lco227\n"
      ".Lst230:\n"
      ".Llp232:\n"
      "s_nop 1\n"
      "buffer_load_dwordx4 v[64:67], v192, s[36:39], s89 offen sc1\n"
      "buffer_load_dwordx4 v[68:71], v193, s[36:39], s89 offen sc1\n"
      "s_waitcnt vmcnt(0)\n"
      "v_bitop3_b32 v217, v64, v65, s97 bitop3:0x7e\n"
      "v_bitop3_b32 v216, v66, v67, s97 bitop3:0x7e\n"
      "v_bitop3_b32 v196, v68, v69, s97 bitop3:0x7e\n"
      "v_or3_b32 v217, v217, v216, v196\n"
      "v_bitop3_b32 v216, v70, v71, s97 bitop3:0x7e\n"
      "v_bitop3_b32 v217, v217, v216, 1 bitop3:0xa8\n"
      "v_cmp_ne_u32 vcc, 0, v217\n"
      "s_cbranch_vccz .Lok233\n"
      "s_sub_u32 s77, s77, 1\n"
      "s_cmp_gt_i32 s77, 0\n"
      "s_cbranch_scc1 .Llp232\n"
      "s_branch .Lco231\n"
      ".Lok233:\n"
      "s_nop 1\n"
      "buffer_load_dwordx4 v[72:75], v194, s[36:39], s89 offen sc1\n"
      "buffer_load_dwordx4 v[76:79], v195, s[36:39], s89 offen sc1\n"
      "buffer_load_dwordx4 v[80:83], v192, s[36:39], s89 offen offset:1024 sc1\n"
      "buffer_load_dwordx4 v[84:87], v193, s[36:39], s89 offen offset:1024 sc1\n"
      "buffer_load_dwordx4 v[88:91], v194, s[36:39], s89 offen offset:1024 sc1\n"
      "buffer_load_dwordx4 v[92:95], v195, s[36:39], s89 offen offset:1024 sc1\n"
      "buffer_load_dwordx4 v[96:99], v192, s[36:39], s89 offen offset:2048 sc1\n"
      "buffer_load_dwordx4 v[100:103], v193, s[36:39], s89 offen offset:2048 sc1\n"
      "buffer_load_dwordx4 v[104:107], v194, s[36:39], s89 offen offset:2048 sc1\n"
      "s_waitcnt vmcnt(0)\n"
      "s_branch .Lco231\n"
      ".Lst234:\n"
      ".Llp236:\n"
      "s_nop 1\n"
      "buffer_load_dwordx4 v[72:75], v194, s[36:39], s89 offen sc1\n"
      "buffer_load_dwordx4 v[76:79], v195, s[36:39], s89 offen sc1\n"
      "s_waitcnt vmcnt(0)\n"
      "v_bitop3_b32 v217, v72, v73, s97 bitop3:0x7e\n"
      "v_bitop3_b32 v216, v74, v75, s97 bitop3:0x7e\n"
      "v_bitop3_b32 v196, v76, v77, s97 bitop3:0x7e\n"
      "v_or3_b32 v217, v217, v216, v196\n"
      "v_bitop3_b32 v216, v78, v79, s97 bitop3:0x7e\n"
      "v_bitop3_b32 v217, v217, v216, 1 bitop3:0xa8\n"
      "v_cmp_ne_u32 vcc, 0, v217\n"
      "s_cbranch_vccz .Lok237\n"
      "s_sub_u32 s77, s77, 1\n"
      "s_cmp_gt_i32 s77, 0\n"
      "s_cbranch_scc1 .Llp236\n"
      "s_branch .Lco235\n"
      ".Lok237:\n"
      "s_nop 1\n"
      "buffer_load_dwordx4 v[80:83], v192, s[36:39], s89 offen offset:1024 sc1\n"
      "buffer_load_dwordx4 v[84:87], v193, s[36:39], s89 offen offset:1024 sc1\n"
      "buffer_load_dwordx4 v[88:91], v194, s[36:39], s89 offen offset:1024 sc1\n"
      "buffer_load_dwordx4 v[92:95], v195, s[36:39], s89 offen offset:1024 sc1\n"
      "buffer_load_dwordx4 v[96:99], v192, s[36:39], s89 offen offset:2048 sc1\n"
      "buffer_load_dwordx4 v[100:103], v193, s[36:39], s89 offen offset:2048 sc1\n"
      "buffer_load_dwordx4 v[104:107], v194, s[36:39], s89 offen offset:2048 sc1\n"
      "buffer_load_dwordx4 v[108:111], v195, s[36:39], s89 offen offset:2048 sc1\n"
      "buffer_load_dwordx4 v[0:3], v192, s[36:39], s89 offen offset:3072 sc1\n"
      "s_waitcnt vmcnt(0)\n"
      "s_branch .Lco235\n"
      ".Lst238:\n"
      ".Llp240:\n"
      "s_nop 1\n"
      "buffer_load_dwordx4 v[80:83], v192, s[36:39], s89 offen offset:1024 sc1\n"
      "buffer_load_dwordx4 v[84:87], v193, s[36:39], s89 offen offset:1024 sc1\n"
      "s_waitcnt vmcnt(0)\n"
      "v_bitop3_b32 v217, v80, v81, s97 bitop3:0x7e\n"
      "v_bitop3_b32 v216, v82, v83, s97 bitop3:0x7e\n"
      "v_bitop3_b32 v196, v84, v85, s97 bitop3:0x7e\n"
      "v_or3_b32 v217, v217, v216, v196\n"
      "v_bitop3_b32 v216, v86, v87, s97 bitop3:0x7e\n"
      "v_bitop3_b32 v217, v217, v216, 1 bitop3:0xa8\n"
      "v_cmp_ne_u32 vcc, 0, v217\n"
      "s_cbranch_vccz .Lok241\n"
      "s_sub_u32 s77, s77, 1\n"
      "s_cmp_gt_i32 s77, 0\n"
      "s_cbranch_scc1 .Llp240\n"
      "s_branch .Lco239\n"
      ".Lok241:\n"
      "s_nop 1\n"
      "buffer_load_dwordx4 v[88:91], v194, s[36:39], s89 offen offset:1024 sc1\n"
      "buffer_load_dwordx4 v[92:95], v195, s[36:39], s89 offen offset:1024 sc1\n"
      "buffer_load_dwordx4 v[96:99], v192, s[36:39], s89 offen offset:2048 sc1\n"
      "buffer_load_dwordx4 v[100:103], v193, s[36:39], s89 offen offset:2048 sc1\n"
      "buffer_load_dwordx4 v[104:107], v194, s[36:39], s89 offen offset:2048 sc1\n"
      "buffer_load_dwordx4 v[108:111], v195, s[36:39], s89 offen offset:2048 sc1\n"
      "buffer_load_dwordx4 v[0:3], v192, s[36:39], s89 offen offset:3072 sc1\n"
      "buffer_load_dwordx4 v[4:7], v193, s[36:39], s89 offen offset:3072 sc1\n"
      "buffer_load_dwordx4 v[8:11], v194, s[36:39], s89 offen offset:3072 sc1\n"
      "s_waitcnt vmcnt(0)\n"
      "s_branch .Lco239\n"
      ".Lst242:\n"
      ".Llp244:\n"
      "s_nop 1\n"
      "buffer_load_dwordx4 v[88:91], v194, s[36:39], s89 offen offset:1024 sc1\n"
      "buffer_load_dwordx4 v[92:95], v195, s[36:39], s89 offen offset:1024 sc1\n"
      "s_waitcnt vmcnt(0)\n"
      "v_bitop3_b32 v217, v88, v89, s97 bitop3:0x7e\n"
      "v_bitop3_b32 v216, v90, v91, s97 bitop3:0x7e\n"
      "v_bitop3_b32 v196, v92, v93, s97 bitop3:0x7e\n"
      "v_or3_b32 v217, v217, v216, v196\n"
      "v_bitop3_b32 v216, v94, v95, s97 bitop3:0x7e\n"
      "v_bitop3_b32 v217, v217, v216, 1 bitop3:0xa8\n"
      "v_cmp_ne_u32 vcc, 0, v217\n"
      "s_cbranch_vccz .Lok245\n"
      "s_sub_u32 s77, s77, 1\n"
      "s_cmp_gt_i32 s77, 0\n"
      "s_cbranch_scc1 .Llp244\n"
      "s_branch .Lco243\n"
      ".Lok245:\n"
      "s_nop 1\n"
      "buffer_load_dwordx4 v[96:99], v192, s[36:39], s89 offen offset:2048 sc1\n"
      "buffer_load_dwordx4 v[100:103], v193, s[36:39], s89 offen offset:2048 sc1\n"
      "buffer_load_dwordx4 v[104:107], v194, s[36:39], s89 offen offset:2048 sc1\n"
      "buffer_load_dwordx4 v[108:111], v195, s[36:39], s89 offen offset:2048 sc1\n"
      "buffer_load_dwordx4 v[0:3], v192, s[36:39], s89 offen offset:3072 sc1\n"
      "buffer_load_dwordx4 v[4:7], v193, s[36:39], s89 offen offset:3072 sc1\n"
      "buffer_load_dwordx4 v[8:11], v194, s[36:39], s89 offen offset:3072 sc1\n"
      "buffer_load_dwordx4 v[12:15], v195, s[36:39], s89 offen offset:3072 sc1\n"
      "buffer_load_dwordx4 v[16:19], v192, s[36:39], s78 offen sc1\n"
      "s_waitcnt vmcnt(0)\n"
      "s_branch .Lco243\n"
      ".Lst246:\n"
      ".Llp248:\n"
      "s_nop 1\n"
      "buffer_load_dwordx4 v[96:99], v192, s[36:39], s89 offen offset:2048 sc1\n"
      "buffer_load_dwordx4 v[100:103], v193, s[36:39], s89 offen offset:2048 sc1\n"
      "s_waitcnt vmcnt(0)\n"
      "v_bitop3_b32 v217, v96, v97, s97 bitop3:0x7e\n"
      "v_bitop3_b32 v216, v98, v99, s97 bitop3:0x7e\n"
      "v_bitop3_b32 v196, v100, v101, s97 bitop3:0x7e\n"
      "v_or3_b32 v217, v217, v216, v196\n"
      "v_bitop3_b32 v216, v102, v103, s97 bitop3:0x7e\n"
      "v_bitop3_b32 v217, v217, v216, 1 bitop3:0xa8\n"
      "v_cmp_ne_u32 vcc, 0, v217\n"
      "s_cbranch_vccz .Lok249\n"
      "s_sub_u32 s77, s77, 1\n"
      "s_cmp_gt_i32 s77, 0\n"
      "s_cbranch_scc1 .Llp248\n"
      "s_branch .Lco247\n"
      ".Lok249:\n"
      "s_nop 1\n"
      "buffer_load_dwordx4 v[104:107], v194, s[36:39], s89 offen offset:2048 sc1\n"
      "buffer_load_dwordx4 v[108:111], v195, s[36:39], s89 offen offset:2048 sc1\n"
      "buffer_load_dwordx4 v[0:3], v192, s[36:39], s89 offen offset:3072 sc1\n"
      "buffer_load_dwordx4 v[4:7], v193, s[36:39], s89 offen offset:3072 sc1\n"
      "buffer_load_dwordx4 v[8:11], v194, s[36:39], s89 offen offset:3072 sc1\n"
      "buffer_load_dwordx4 v[12:15], v195, s[36:39], s89 offen offset:3072 sc1\n"
      "buffer_load_dwordx4 v[16:19], v192, s[36:39], s78 offen sc1\n"
      "buffer_load_dwordx4 v[20:23], v193, s[36:39], s78 offen sc1\n"
      "buffer_load_dwordx4 v[24:27], v194, s[36:39], s78 offen sc1\n"
      "s_waitcnt vmcnt(0)\n"
      "s_branch .Lco247\n"
      ".Lst250:\n"
      ".Llp252:\n"
      "s_nop 1\n"
      "buffer_load_dwordx4 v[104:107], v194, s[36:39], s89 offen offset:2048 sc1\n"
      "buffer_load_dwordx4 v[108:111], v195, s[36:39], s89 offen offset:2048 sc1\n"
      "s_waitcnt vmcnt(0)\n"
      "v_bitop3_b32 v217, v104, v105, s97 bitop3:0x7e\n"
      "v_bitop3_b32 v216, v106, v107, s97 bitop3:0x7e\n"
      "v_bitop3_b32 v196, v108, v109, s97 bitop3:0x7e\n"
      "v_or3_b32 v217, v217, v216, v196\n"
      "v_bitop3_b32 v216, v110, v111, s97 bitop3:0x7e\n"
      "v_bitop3_b32 v217, v217, v216, 1 bitop3:0xa8\n"
      "v_cmp_ne_u32 vcc, 0, v217\n"
      "s_cbranch_vccz .Lok253\n"
      "s_sub_u32 s77, s77, 1\n"
      "s_cmp_gt_i32 s77, 0\n"
      "s_cbranch_scc1 .Llp252\n"
      "s_branch .Lco251\n"
      ".Lok253:\n"
      "s_nop 1\n"
      "buffer_load_dwordx4 v[0:3], v192, s[36:39], s89 offen offset:3072 sc1\n"
      "buffer_load_dwordx4 v[4:7], v193, s[36:39], s89 offen offset:3072 sc1\n"
      "buffer_load_dwordx4 v[8:11], v194, s[36:39], s89 offen offset:3072 sc1\n"
      "buffer_load_dwordx4 v[12:15], v195, s[36:39], s89 offen offset:3072 sc1\n"
      "buffer_load_dwordx4 v[16:19], v192, s[36:39], s78 offen sc1\n"
      "buffer_load_dwordx4 v[20:23], v193, s[36:39], s78 offen sc1\n"
      "buffer_load_dwordx4 v[24:27], v194, s[36:39], s78 offen sc1\n"
      "buffer_load_dwordx4 v[28:31], v195, s[36:39], s78 offen sc1\n"
      "buffer_load_dwordx4 v[32:35], v192, s[36:39], s79 offen sc1\n"
      "s_waitcnt vmcnt(0)\n"
      "s_branch .Lco251\n"
      ".Lst254:\n"
      ".Llp256:\n"
      "s_nop 1\n"
      "buffer_load_dwordx4 v[0:3], v192, s[36:39], s89 offen offset:3072 sc1\n"
      "buffer_load_dwordx4 v[4:7], v193, s[36:39], s89 offen offset:3072 sc1\n"
      "s_waitcnt vmcnt(0)\n"
      "v_bitop3_b32 v217, v0, v1, s97 bitop3:0x7e\n"
      "v_bitop3_b32 v216, v2, v3, s97 bitop3:0x7e\n"
      "v_bitop3_b32 v196, v4, v5, s97 bitop3:0x7e\n"
      "v_or3_b32 v217, v217, v216, v196\n"
      "v_bitop3_b32 v216, v6, v7, s97 bitop3:0x7e\n"
      "v_bitop3_b32 v217, v217, v216, 1 bitop3:0xa8\n"
      "v_cmp_ne_u32 vcc, 0, v217\n"
      "s_cbranch_vccz .Lok257\n"
      "s_sub_u32 s77, s77, 1\n"
      "s_cmp_gt_i32 s77, 0\n"
      "s_cbranch_scc1 .Llp256\n"
      "s_branch .Lco255\n"
      ".Lok257:\n"
      "s_nop 1\n"
      "buffer_load_dwordx4 v[8:11], v194, s[36:39], s89 offen offset:3072 sc1\n"
      "buffer_load_dwordx4 v[12:15], v195, s[36:39], s89 offen offset:3072 sc1\n"
      "buffer_load_dwordx4 v[16:19], v192, s[36:39], s78 offen sc1\n"
      "buffer_load_dwordx4 v[20:23], v193, s[36:39], s78 offen sc1\n"
      "buffer_load_dwordx4 v[24:27], v194, s[36:39], s78 offen sc1\n"
      "buffer_load_dwordx4 v[28:31], v195, s[36:39], s78 offen sc1\n"
      "buffer_load_dwordx4 v[32:35], v192, s[36:39], s79 offen sc1\n"
      "buffer_load_dwordx4 v[36:39], v193, s[36:39], s79 offen sc1\n"
      "buffer_load_dwordx4 v[40:43], v194, s[36:39], s79 offen sc1\n"
      "s_waitcnt vmcnt(0)\n"
      "s_branch .Lco255\n"
      ".Lst258:\n"
      ".Llp260:\n"
      "s_nop 1\n"
      "buffer_load_dwordx4 v[8:11], v194, s[36:39], s89 offen offset:3072 sc1\n"
      "buffer_load_dwordx4 v[12:15], v195, s[36:39], s89 offen offset:3072 sc1\n"
      "s_waitcnt vmcnt(0)\n"
      "v_bitop3_b32 v217, v8, v9, s97 bitop3:0x7e\n"
      "v_bitop3_b32 v216, v10, v11, s97 bitop3:0x7e\n"
      "v_bitop3_b32 v196, v12, v13, s97 bitop3:0x7e\n"
      "v_or3_b32 v217, v217, v216, v196\n"
      "v_bitop3_b32 v216, v14, v15, s97 bitop3:0x7e\n"
      "v_bitop3_b32 v217, v217, v216, 1 bitop3:0xa8\n"
      "v_cmp_ne_u32 vcc, 0, v217\n"
      "s_cbranch_vccz .Lok261\n"
      "s_sub_u32 s77, s77, 1\n"
      "s_cmp_gt_i32 s77, 0\n"
      "s_cbranch_scc1 .Llp260\n"
      "s_branch .Lco259\n"
      ".Lok261:\n"
      "s_nop 1\n"
      "buffer_load_dwordx4 v[16:19], v192, s[36:39], s78 offen sc1\n"
      "buffer_load_dwordx4 v[20:23], v193, s[36:39], s78 offen sc1\n"
      "buffer_load_dwordx4 v[24:27], v194, s[36:39], s78 offen sc1\n"
      "buffer_load_dwordx4 v[28:31], v195, s[36:39], s78 offen sc1\n"
      "buffer_load_dwordx4 v[32:35], v192, s[36:39], s79 offen sc1\n"
      "buffer_load_dwordx4 v[36:39], v193, s[36:39], s79 offen sc1\n"
      "buffer_load_dwordx4 v[40:43], v194, s[36:39], s79 offen sc1\n"
      "buffer_load_dwordx4 v[44:47], v195, s[36:39], s79 offen sc1\n"
      "buffer_load_dwordx4 v[48:51], v192, s[36:39], s78 offen offset:1024 sc1\n"
      "s_waitcnt vmcnt(0)\n"
      "s_branch .Lco259\n"
      ".Lst262:\n"
      ".Llp264:\n"
      "s_nop 1\n"
      "buffer_load_dwordx4 v[16:19], v192, s[36:39], s78 offen sc1\n"
      "buffer_load_dwordx4 v[20:23], v193, s[36:39], s78 offen sc1\n"
      "s_waitcnt vmcnt(0)\n"
      "v_bitop3_b32 v217, v16, v17, s97 bitop3:0x7e\n"
      "v_bitop3_b32 v216, v18, v19, s97 bitop3:0x7e\n"
      "v_bitop3_b32 v196, v20, v21, s97 bitop3:0x7e\n"
      "v_or3_b32 v217, v217, v216, v196\n"
      "v_bitop3_b32 v216, v22, v23, s97 bitop3:0x7e\n"
      "v_bitop3_b32 v217, v217, v216, 1 bitop3:0xa8\n"
      "v_cmp_ne_u32 vcc, 0, v217\n"
      "s_cbranch_vccz .Lok265\n"
      "s_sub_u32 s77, s77, 1\n"
      "s_cmp_gt_i32 s77, 0\n"
      "s_cbranch_scc1 .Llp264\n"
      "s_branch .Lco263\n"
      ".Lok265:\n"
      "s_nop 1\n"
      "buffer_load_dwordx4 v[24:27], v194, s[36:39], s78 offen sc1\n"
      "buffer_load_dwordx4 v[28:31], v195, s[36:39], s78 offen sc1\n"
      "buffer_load_dwordx4 v[32:35], v192, s[36:39], s79 offen sc1\n"
      "buffer_load_dwordx4 v[36:39], v193, s[36:39], s79 offen sc1\n"
      "buffer_load_dwordx4 v[40:43], v194, s[36:39], s79 offen sc1\n"
      "buffer_load_dwordx4 v[44:47], v195, s[36:39], s79 offen sc1\n"
      "buffer_load_dwordx4 v[48:51], v192, s[36:39], s78 offen offset:1024 sc1\n"
      "buffer_load_dwordx4 v[52:55], v193, s[36:39], s78 offen offset:1024 sc1\n"
      "buffer_load_dwordx4 v[56:59], v194, s[36:39], s78 offen offset:1024 sc1\n"
      "s_waitcnt vmcnt(0)\n"
      "s_branch .Lco263\n"
      ".Lst266:\n"
      ".Llp268:\n"
      "s_nop 1\n"
      "buffer_load_dwordx4 v[24:27], v194, s[36:39], s78 offen sc1\n"
      "buffer_load_dwordx4 v[28:31], v195, s[36:39], s78 offen sc1\n"
      "s_waitcnt vmcnt(0)\n"
      "v_bitop3_b32 v217, v24, v25, s97 bitop3:0x7e\n"
      "v_bitop3_b32 v216, v26, v27, s97 bitop3:0x7e\n"
      "v_bitop3_b32 v196, v28, v29, s97 bitop3:0x7e\n"
      "v_or3_b32 v217, v217, v216, v196\n"
      "v_bitop3_b32 v216, v30, v31, s97 bitop3:0x7e\n"
      "v_bitop3_b32 v217, v217, v216, 1 bitop3:0xa8\n"
      "v_cmp_ne_u32 vcc, 0, v217\n"
      "s_cbranch_vccz .Lok269\n"
      "s_sub_u32 s77, s77, 1\n"
      "s_cmp_gt_i32 s77, 0\n"
      "s_cbranch_scc1 .Llp268\n"
      "s_branch .Lco267\n"
      ".Lok269:\n"
      "s_nop 1\n"
      "buffer_load_dwordx4 v[32:35], v192, s[36:39], s79 offen sc1\n"
      "buffer_load_dwordx4 v[36:39], v193, s[36:39], s79 offen sc1\n"
      "buffer_load_dwordx4 v[40:43], v194, s[36:39], s79 offen sc1\n"
      "buffer_load_dwordx4 v[44:47], v195, s[36:39], s79 offen sc1\n"
      "buffer_load_dwordx4 v[48:51], v192, s[36:39], s78 offen offset:1024 sc1\n"
      "buffer_load_dwordx4 v[52:55], v193, s[36:39], s78 offen offset:1024 sc1\n"
      "buffer_load_dwordx4 v[56:59], v194, s[36:39], s78 offen offset:1024 sc1\n"
      "buffer_load_dwordx4 v[60:63], v195, s[36:39], s78 offen offset:1024 sc1\n"
      "buffer_load_dwordx4 v[64:67], v192, s[36:39], s78 offen offset:2048 sc1\n"
      "s_waitcnt vmcnt(0)\n"
      "s_branch .Lco267\n"
      ".Lst270:\n"
      ".Llp272:\n"
      "s_nop 1\n"
      "buffer_load_dwordx4 v[32:35], v192, s[36:39], s79 offen sc1\n"
      "buffer_load_dwordx4 v[36:39], v193, s[36:39], s79 offen sc1\n"
      "s_waitcnt vmcnt(0)\n"
      "v_bitop3_b32 v217, v32, v33, s97 bitop3:0x7e\n"
      "v_bitop3_b32 v216, v34, v35, s97 bitop3:0x7e\n"
      "v_bitop3_b32 v196, v36, v37, s97 bitop3:0x7e\n"
      "v_or3_b32 v217, v217, v216, v196\n"
      "v_bitop3_b32 v216, v38, v39, s97 bitop3:0x7e\n"
      "v_bitop3_b32 v217, v217, v216, 1 bitop3:0xa8\n"
      "v_cmp_ne_u32 vcc, 0, v217\n"
      "s_cbranch_vccz .Lok273\n"
      "s_sub_u32 s77, s77, 1\n"
      "s_cmp_gt_i32 s77, 0\n"
      "s_cbranch_scc1 .Llp272\n"
      "s_branch .Lco271\n"
      ".Lok273:\n"
      "s_nop 1\n"
      "buffer_load_dwordx4 v[40:43], v194, s[36:39], s79 offen sc1\n"
      "buffer_load_dwordx4 v[44:47], v195, s[36:39], s79 offen sc1\n"
      "buffer_load_dwordx4 v[48:51], v192, s[36:39], s78 offen offset:1024 sc1\n"
      "buffer_load_dwordx4 v[52:55], v193, s[36:39], s78 offen offset:1024 sc1\n"
      "buffer_load_dwordx4 v[56:59], v194, s[36:39], s78 offen offset:1024 sc1\n"
      "buffer_load_dwordx4 v[60:63], v195, s[36:39], s78 offen offset:1024 sc1\n"
      "buffer_load_dwordx4 v[64:67], v192, s[36:39], s78 offen offset:2048 sc1\n"
      "buffer_load_dwordx4 v[68:71], v193, s[36:39], s78 offen offset:2048 sc1\n"
      "buffer_load_dwordx4 v[72:75], v194, s[36:39], s78 offen offset:2048 sc1\n"
      "s_waitcnt vmcnt(0)\n"
      "s_branch .Lco271\n"
      ".Lst274:\n"
      ".Llp276:\n"
      "s_nop 1\n"
      "buffer_load_dwordx4 v[40:43], v194, s[36:39], s79 offen sc1\n"
      "buffer_load_dwordx4 v[44:47], v195, s[36:39], s79 offen sc1\n"
      "s_waitcnt vmcnt(0)\n"
      "v_bitop3_b32 v217, v40, v41, s97 bitop3:0x7e\n"
      "v_bitop3_b32 v216, v42, v43, s97 bitop3:0x7e\n"
      "v_bitop3_b32 v196, v44, v45, s97 bitop3:0x7e\n"
      "v_or3_b32 v217, v217, v216, v196\n"
      "v_bitop3_b32 v216, v46, v47, s97 bitop3:0x7e\n"
      "v_bitop3_b32 v217, v217, v216, 1 bitop3:0xa8\n"
      "v_cmp_ne_u32 vcc, 0, v217\n"
      "s_cbranch_vccz .Lok277\n"
      "s_sub_u32 s77, s77, 1\n"
      "s_cmp_gt_i32 s77, 0\n"
      "s_cbranch_scc1 .Llp276\n"
      "s_branch .Lco275\n"
      ".Lok277:\n"
      "s_nop 1\n"
      "buffer_load_dwordx4 v[48:51], v192, s[36:39], s78 offen offset:1024 sc1\n"
      "buffer_load_dwordx4 v[52:55], v193, s[36:39], s78 offen offset:1024 sc1\n"
      "buffer_load_dwordx4 v[56:59], v194, s[36:39], s78 offen offset:1024 sc1\n"
      "buffer_load_dwordx4 v[60:63], v195, s[36:39], s78 offen offset:1024 sc1\n"
      "buffer_load_dwordx4 v[64:67], v192, s[36:39], s78 offen offset:2048 sc1\n"
      "buffer_load_dwordx4 v[68:71], v193, s[36:39], s78 offen offset:2048 sc1\n"
      "buffer_load_dwordx4 v[72:75], v194, s[36:39], s78 offen offset:2048 sc1\n"
      "buffer_load_dwordx4 v[76:79], v195, s[36:39], s78 offen offset:2048 sc1\n"
      "buffer_load_dwordx4 v[80:83], v192, s[36:39], s79 offen offset:1024 sc1\n"
      "s_waitcnt vmcnt(0)\n"
      "s_branch .Lco275\n"
      ".Lst278:\n"
      ".Llp280:\n"
      "s_nop 1\n"
      "buffer_load_dwordx4 v[48:51], v192, s[36:39], s78 offen offset:1024 sc1\n"
      "buffer_load_dwordx4 v[52:55], v193, s[36:39], s78 offen offset:1024 sc1\n"
      "s_waitcnt vmcnt(0)\n"
      "v_bitop3_b32 v217, v48, v49, s97 bitop3:0x7e\n"
      "v_bitop3_b32 v216, v50, v51, s97 bitop3:0x7e\n"
      "v_bitop3_b32 v196, v52, v53, s97 bitop3:0x7e\n"
      "v_or3_b32 v217, v217, v216, v196\n"
      "v_bitop3_b32 v216, v54, v55, s97 bitop3:0x7e\n"
      "v_bitop3_b32 v217, v217, v216, 1 bitop3:0xa8\n"
      "v_cmp_ne_u32 vcc, 0, v217\n"
      "s_cbranch_vccz .Lok281\n"
      "s_sub_u32 s77, s77, 1\n"
      "s_cmp_gt_i32 s77, 0\n"
      "s_cbranch_scc1 .Llp280\n"
      "s_branch .Lco279\n"
      ".Lok281:\n"
      "s_nop 1\n"
      "buffer_load_dwordx4 v[56:59], v194, s[36:39], s78 offen offset:1024 sc1\n"
      "buffer_load_dwordx4 v[60:63], v195, s[36:39], s78 offen offset:1024 sc1\n"
      "buffer_load_dwordx4 v[64:67], v192, s[36:39], s78 offen offset:2048 sc1\n"
      "buffer_load_dwordx4 v[68:71], v193, s[36:39], s78 offen offset:2048 sc1\n"
      "buffer_load_dwordx4 v[72:75], v194, s[36:39], s78 offen offset:2048 sc1\n"
      "buffer_load_dwordx4 v[76:79], v195, s[36:39], s78 offen offset:2048 sc1\n"
      "buffer_load_dwordx4 v[80:83], v192, s[36:39], s79 offen offset:1024 sc1\n"
      "buffer_load_dwordx4 v[84:87], v193, s[36:39], s79 offen offset:1024 sc1\n"
      "buffer_load_dwordx4 v[88:91], v194, s[36:39], s79 offen offset:1024 sc1\n"
      "s_waitcnt vmcnt(0)\n"
      "s_branch .Lco279\n"
      ".Lst282:\n"
      ".Llp284:\n"
      "s_nop 1\n"
      "buffer_load_dwordx4 v[56:59], v194, s[36:39], s78 offen offset:1024 sc1\n"
      "buffer_load_dwordx4 v[60:63], v195, s[36:39], s78 offen offset:1024 sc1\n"
      "s_waitcnt vmcnt(0)\n"
      "v_bitop3_b32 v217, v56, v57, s97 bitop3:0x7e\n"
      "v_bitop3_b32 v216, v58, v59, s97 bitop3:0x7e\n"
      "v_bitop3_b32 v196, v60, v61, s97 bitop3:0x7e\n"
      "v_or3_b32 v217, v217, v216, v196\n"
      "v_bitop3_b32 v216, v62, v63, s97 bitop3:0x7e\n"
      "v_bitop3_b32 v217, v217, v216, 1 bitop3:0xa8\n"
      "v_cmp_ne_u32 vcc, 0, v217\n"
      "s_cbranch_vccz .Lok285\n"
      "s_sub_u32 s77, s77, 1\n"
      "s_cmp_gt_i32 s77, 0\n"
      "s_cbranch_scc1 .Llp284\n"
      "s_branch .Lco283\n"
      ".Lok285:\n"
      "s_nop 1\n"
      "buffer_load_dwordx4 v[64:67], v192, s[36:39], s78 offen offset:2048 sc1\n"
      "buffer_load_dwordx4 v[68:71], v193, s[36:39], s78 offen offset:2048 sc1\n"
      "buffer_load_dwordx4 v[72:75], v194, s[36:39], s78 offen offset:2048 sc1\n"
      "buffer_load_dwordx4 v[76:79], v195, s[36:39], s78 offen offset:2048 sc1\n"
      "buffer_load_dwordx4 v[80:83], v192, s[36:39], s79 offen offset:1024 sc1\n"
      "buffer_load_dwordx4 v[84:87], v193, s[36:39], s79 offen offset:1024 sc1\n"
      "buffer_load_dwordx4 v[88:91], v194, s[36:39], s79 offen offset:1024 sc1\n"
      "buffer_load_dwordx4 v[92:95], v195, s[36:39], s79 offen offset:1024 sc1\n"
      "buffer_load_dwordx4 v[96:99], v192, s[36:39], s79 offen offset:2048 sc1\n"
      "s_waitcnt vmcnt(0)\n"
      "s_branch .Lco283\n"
      ".Lst286:\n"
      ".Llp288:\n"
      "s_nop 1\n"
      "buffer_load_dwordx4 v[64:67], v192, s[36:39], s78 offen offset:2048 sc1\n"
      "buffer_load_dwordx4 v[68:71], v193, s[36:39], s78 offen offset:2048 sc1\n"
      "s_waitcnt vmcnt(0)\n"
      "v_bitop3_b32 v217, v64, v65, s97 bitop3:0x7e\n"
      "v_bitop3_b32 v216, v66, v67, s97 bitop3:0x7e\n"
      "v_bitop3_b32 v196, v68, v69, s97 bitop3:0x7e\n"
      "v_or3_b32 v217, v217, v216, v196\n"
      "v_bitop3_b32 v216, v70, v71, s97 bitop3:0x7e\n"
      "v_bitop3_b32 v217, v217, v216, 1 bitop3:0xa8\n"
      "v_cmp_ne_u32 vcc, 0, v217\n"
      "s_cbranch_vccz .Lok289\n"
      "s_sub_u32 s77, s77, 1\n"
      "s_cmp_gt_i32 s77, 0\n"
      "s_cbranch_scc1 .Llp288\n"
      "s_branch .Lco287\n"
      ".Lok289:\n"
      "s_nop 1\n"
      "buffer_load_dwordx4 v[72:75], v194, s[36:39], s78 offen offset:2048 sc1\n"
      "buffer_load_dwordx4 v[76:79], v195, s[36:39], s78 offen offset:2048 sc1\n"
      "buffer_load_dwordx4 v[80:83], v192, s[36:39], s79 offen offset:1024 sc1\n"
      "buffer_load_dwordx4 v[84:87], v193, s[36:39], s79 offen offset:1024 sc1\n"
      "buffer_load_dwordx4 v[88:91], v194, s[36:39], s79 offen offset:1024 sc1\n"
      "buffer_load_dwordx4 v[92:95], v195, s[36:39], s79 offen offset:1024 sc1\n"
      "buffer_load_dwordx4 v[96:99], v192, s[36:39], s79 offen offset:2048 sc1\n"
      "buffer_load_dwordx4 v[100:103], v193, s[36:39], s79 offen offset:2048 sc1\n"
      "buffer_load_dwordx4 v[104:107], v194, s[36:39], s79 offen offset:2048 sc1\n"
      "s_waitcnt vmcnt(0)\n"
      "s_branch .Lco287\n"
      ".Lst290:\n"
      ".Llp292:\n"
      "s_nop 1\n"
      "buffer_load_dwordx4 v[72:75], v194, s[36:39], s78 offen offset:2048 sc1\n"
      "buffer_load_dwordx4 v[76:79], v195, s[36:39], s78 offen offset:2048 sc1\n"
      "s_waitcnt vmcnt(0)\n"
      "v_bitop3_b32 v217, v72, v73, s97 bitop3:0x7e\n"
      "v_bitop3_b32 v216, v74, v75, s97 bitop3:0x7e\n"
      "v_bitop3_b32 v196, v76, v77, s97 bitop3:0x7e\n"
      "v_or3_b32 v217, v217, v216, v196\n"
      "v_bitop3_b32 v216, v78, v79, s97 bitop3:0x7e\n"
      "v_bitop3_b32 v217, v217, v216, 1 bitop3:0xa8\n"
      "v_cmp_ne_u32 vcc, 0, v217\n"
      "s_cbranch_vccz .Lok293\n"
      "s_sub_u32 s77, s77, 1\n"
      "s_cmp_gt_i32 s77, 0\n"
      "s_cbranch_scc1 .Llp292\n"
      "s_branch .Lco291\n"
      ".Lok293:\n"
      "s_nop 1\n"
      "buffer_load_dwordx4 v[80:83], v192, s[36:39], s79 offen offset:1024 sc1\n"
      "buffer_load_dwordx4 v[84:87], v193, s[36:39], s79 offen offset:1024 sc1\n"
      "buffer_load_dwordx4 v[88:91], v194, s[36:39], s79 offen offset:1024 sc1\n"
      "buffer_load_dwordx4 v[92:95], v195, s[36:39], s79 offen offset:1024 sc1\n"
      "buffer_load_dwordx4 v[96:99], v192, s[36:39], s79 offen offset:2048 sc1\n"
      "buffer_load_dwordx4 v[100:103], v193, s[36:39], s79 offen offset:2048 sc1\n"
      "buffer_load_dwordx4 v[104:107], v194, s[36:39], s79 offen offset:2048 sc1\n"
      "buffer_load_dwordx4 v[108:111], v195, s[36:39], s79 offen offset:2048 sc1\n"
      "s_waitcnt vmcnt(0)\n"
      "s_branch .Lco291\n"
      ".Lst294:\n"
      ".Llp296:\n"
      "s_nop 1\n"
      "buffer_load_dwordx4 v[80:83], v192, s[36:39], s79 offen offset:1024 sc1\n"
      "buffer_load_dwordx4 v[84:87], v193, s[36:39], s79 offen offset:1024 sc1\n"
      "s_waitcnt vmcnt(0)\n"
      "v_bitop3_b32 v217, v80, v81, s97 bitop3:0x7e\n"
      "v_bitop3_b32 v216, v82, v83, s97 bitop3:0x7e\n"
      "v_bitop3_b32 v196, v84, v85, s97 bitop3:0x7e\n"
      "v_or3_b32 v217, v217, v216, v196\n"
      "v_bitop3_b32 v216, v86, v87, s97 bitop3:0x7e\n"
      "v_bitop3_b32 v217, v217, v216, 1 bitop3:0xa8\n"
      "v_cmp_ne_u32 vcc, 0, v217\n"
      "s_cbranch_vccz .Lok297\n"
      "s_sub_u32 s77, s77, 1\n"
      "s_cmp_gt_i32 s77, 0\n"
      "s_cbranch_scc1 .Llp296\n"
      "s_branch .Lco295\n"
      ".Lok297:\n"
      "s_nop 1\n"
      "buffer_load_dwordx4 v[88:91], v194, s[36:39], s79 offen offset:1024 sc1\n"
      "buffer_load_dwordx4 v[92:95], v195, s[36:39], s79 offen offset:1024 sc1\n"
      "buffer_load_dwordx4 v[96:99], v192, s[36:39], s79 offen offset:2048 sc1\n"
      "buffer_load_dwordx4 v[100:103], v193, s[36:39], s79 offen offset:2048 sc1\n"
      "buffer_load_dwordx4 v[104:107], v194, s[36:39], s79 offen offset:2048 sc1\n"
      "buffer_load_dwordx4 v[108:111], v195, s[36:39], s79 offen offset:2048 sc1\n"
      "s_waitcnt vmcnt(0)\n"
      "s_branch .Lco295\n"
      ".Lst298:\n"
      ".Llp300:\n"
      "s_nop 1\n"
      "buffer_load_dwordx4 v[88:91], v194, s[36:39], s79 offen offset:1024 sc1\n"
      "buffer_load_dwordx4 v[92:95], v195, s[36:39], s79 offen offset:1024 sc1\n"
      "s_waitcnt vmcnt(0)\n"
      "v_bitop3_b32 v217, v88, v89, s97 bitop3:0x7e\n"
      "v_bitop3_b32 v216, v90, v91, s97 bitop3:0x7e\n"
      "v_bitop3_b32 v196, v92, v93, s97 bitop3:0x7e\n"
      "v_or3_b32 v217, v217, v216, v196\n"
      "v_bitop3_b32 v216, v94, v95, s97 bitop3:0x7e\n"
      "v_bitop3_b32 v217, v217, v216, 1 bitop3:0xa8\n"
      "v_cmp_ne_u32 vcc, 0, v217\n"
      "s_cbranch_vccz .Lok301\n"
      "s_sub_u32 s77, s77, 1\n"
      "s_cmp_gt_i32 s77, 0\n"
      "s_cbranch_scc1 .Llp300\n"
      "s_branch .Lco299\n"
      ".Lok301:\n"
      "s_nop 1\n"
      "buffer_load_dwordx4 v[96:99], v192, s[36:39], s79 offen offset:2048 sc1\n"
      "buffer_load_dwordx4 v[100:103], v193, s[36:39], s79 offen offset:2048 sc1\n"
      "buffer_load_dwordx4 v[104:107], v194, s[36:39], s79 offen offset:2048 sc1\n"
      "buffer_load_dwordx4 v[108:111], v195, s[36:39], s79 offen offset:2048 sc1\n"
      "s_waitcnt vmcnt(0)\n"
      "s_branch .Lco299\n"
      ".Lst302:\n"
      ".Llp304:\n"
      "s_nop 1\n"
      "buffer_load_dwordx4 v[96:99], v192, s[36:39], s79 offen offset:2048 sc1\n"
      "buffer_load_dwordx4 v[100:103], v193, s[36:39], s79 offen offset:2048 sc1\n"
      "s_waitcnt vmcnt(0)\n"
      "v_bitop3_b32 v217, v96, v97, s97 bitop3:0x7e\n"
      "v_bitop3_b32 v216, v98, v99, s97 bitop3:0x7e\n"
      "v_bitop3_b32 v196, v100, v101, s97 bitop3:0x7e\n"
      "v_or3_b32 v217, v217, v216, v196\n"
      "v_bitop3_b32 v216, v102, v103, s97 bitop3:0x7e\n"
      "v_bitop3_b32 v217, v217, v216, 1 bitop3:0xa8\n"
      "v_cmp_ne_u32 vcc, 0, v217\n"
      "s_cbranch_vccz .Lok305\n"
      "s_sub_u32 s77, s77, 1\n"
      "s_cmp_gt_i32 s77, 0\n"
      "s_cbranch_scc1 .Llp304\n"
      "s_branch .Lco303\n"
      ".Lok305:\n"
      "s_nop 1\n"
      "buffer_load_dwordx4 v[104:107], v194, s[36:39], s79 offen offset:2048 sc1\n"
      "buffer_load_dwordx4 v[108:111], v195, s[36:39], s79 offen offset:2048 sc1\n"
      "s_waitcnt vmcnt(0)\n"
      "s_branch .Lco303\n"
      ".Lst306:\n"
      ".Llp308:\n"
      "s_nop 1\n"
      "buffer_load_dwordx4 v[104:107], v194, s[36:39], s79 offen offset:2048 sc1\n"
      "buffer_load_dwordx4 v[108:111], v195, s[36:39], s79 offen offset:2048 sc1\n"
      "s_waitcnt vmcnt(0)\n"
      "v_bitop3_b32 v217, v104, v105, s97 bitop3:0x7e\n"
      "v_bitop3_b32 v216, v106, v107, s97 bitop3:0x7e\n"
      "v_bitop3_b32 v196, v108, v109, s97 bitop3:0x7e\n"
      "v_or3_b32 v217, v217, v216, v196\n"
      "v_bitop3_b32 v216, v110, v111, s97 bitop3:0x7e\n"
      "v_bitop3_b32 v217, v217, v216, 1 bitop3:0xa8\n"
      "v_cmp_ne_u32 vcc, 0, v217\n"
      "s_cbranch_vccz .Lok309\n"
      "s_sub_u32 s77, s77, 1\n"
      "s_cmp_gt_i32 s77, 0\n"
      "s_cbranch_scc1 .Llp308\n"
      "s_branch .Lco307\n"
      ".Lok309:\n"
      "s_nop 1\n"
      "s_waitcnt vmcnt(0)\n"
      "s_branch .Lco307\n"
      ".Lcodeend:\n"
      :
      : "s"(__builtin_amdgcn_kernarg_segment_ptr()), "s"(blockIdx.x), "s"(w)
      : "v1", "v2", "v3", "v4", "v5", "v6", "v7", "v8", "v9", "v10", "v11", "v12", "v13", "v14", "v15", "v16", "v17", "v18", "v19", "v20", "v21", "v22", "v23", "v24", "v25", "v26", "v27", "v28", "v29", "v30", "v31", "v32", "v33", "v34", "v35", "v36", "v37", "v38", "v39", "v40", "v41", "v42", "v43", "v44", "v45", "v46", "v47", "v48", "v49", "v50", "v51", "v52", "v53", "v54", "v55", "v56", "v57", "v58", "v59", "v60", "v61", "v62", "v63", "v64", "v65", "v66", "v67", "v68", "v69", "v70", "v71", "v72", "v73", "v74", "v75", "v76", "v77", "v78", "v79", "v80", "v81", "v82", "v83", "v84", "v85", "v86", "v87", "v88", "v89", "v90", "v91", "v92", "v93", "v94", "v95", "v96", "v97", "v98", "v99", "v100", "v101", "v102", "v103", "v104", "v105", "v106", "v107", "v108", "v109", "v110", "v111", "v112", "v113", "v114", "v115", "v116", "v117", "v118", "v119", "v120", "v121", "v122", "v123", "v124", "v125", "v126", "v127", "v128", "v129", "v130", "v131", "v132", "v133", "v134", "v135", "v136", "v137", "v138", "v139", "v140", "v141", "v142", "v143", "v144", "v145", "v146", "v147", "v148", "v149", "v150", "v151", "v152", "v153", "v154", "v155", "v156", "v157", "v158", "v159", "v160", "v161", "v162", "v163", "v164", "v165", "v166", "v167", "v168", "v169", "v170", "v171", "v172", "v173", "v174", "v175", "v176", "v177", "v178", "v179", "v180", "v181", "v182", "v183", "v184", "v185", "v186", "v187", "v188", "v189", "v190", "v191", "v192", "v193", "v194", "v195", "v196", "v197", "v198", "v199", "v200", "v201", "v202", "v203", "v204", "v205", "v206", "v207", "v208", "v209", "v210", "v211", "v212", "v213", "v214", "v215", "v216", "v217", "v218", "v219", "v220", "v221", "v222", "v223", "v224", "v225", "v226", "v227", "v228", "v229", "v230", "v231", "v232", "v233", "v234", "v235", "v236", "v237", "v238", "v239", "v240", "v241", "v242", "v243", "v244", "v245", "v246", "v247", "v248", "v249", "v250", "v251", "v252", "v253", "v254", "v255", "a0", "a1", "a2", "a3", "a4", "a5", "a6", "a7", "a8", "a9", "a10", "a11", "a12", "a13", "a14", "a15", "a16", "a17", "a18", "a19", "a20", "a21", "a22", "a23", "a24", "a25", "a26", "a27", "a28", "a29", "a30", "a31", "a32", "a33", "a34", "a35", "a36", "a37", "a38", "a39", "a40", "a41", "a42", "a43", "a44", "a45", "a46", "a47", "a48", "a49", "a50", "a51", "a52", "a53", "a54", "a55", "a56", "a57", "a58", "a59", "a60", "a61", "a62", "a63", "a64", "a65", "a66", "a67", "a68", "a69", "a70", "a71", "a72", "a73", "a74", "a75", "a76", "a77", "a78", "a79", "a80", "a81", "a82", "a83", "a84", "a85", "a86", "a87", "a88", "a89", "a90", "a91", "a92", "a93", "a94", "a95", "a96", "a97", "a98", "a99", "a100", "a101", "a102", "a103", "a104", "a105", "a106", "a107", "a108", "a109", "a110", "a111", "a112", "a113", "a114", "a115", "a116", "a117", "a118", "a119", "a120", "a121", "a122", "a123", "a124", "a125", "a126", "a127", "a128", "a129", "a130", "a131", "a132", "a133", "a134", "a135", "a136", "a137", "a138", "a139", "a140", "a141", "a142", "a143", "a144", "a145", "a146", "a147", "a148", "a149", "a150", "a151", "a152", "a153", "a154", "a155", "a156", "a157", "a158", "a159", "a160", "a161", "a162", "a163", "a164", "a165", "a166", "a167", "a168", "a169", "a170", "a171", "a172", "a173", "a174", "a175", "a176", "a177", "a178", "a179", "a180", "a181", "a182", "a183", "a184", "a185", "a186", "a187", "a188", "a189", "a190", "a191", "a192", "a193", "a194", "a195", "a196", "a197", "a198", "a199", "a200", "a201", "a202", "a203", "a204", "a205", "a206", "a207", "a208", "a209", "a210", "a211", "a212", "a213", "a214", "a215", "a216", "a217", "a218", "a219", "a220", "a221", "a222", "a223", "a224", "a225", "a226", "a227", "a228", "a229", "a230", "a231", "a232", "a233", "a234", "a235", "a236", "a237", "a238", "a239", "a240", "a241", "a242", "a243", "a244", "a245", "a246", "a247", "a248", "a249", "a250", "a251", "a252", "a253", "a254", "a255", "s4", "s5", "s6", "s7", "s8", "s9", "s10", "s11", "s12", "s13", "s14", "s15", "s16", "s17", "s18", "s19", "s20", "s21", "s22", "s23", "s24", "s25", "s26", "s27", "s28", "s29", "s30", "s31", "s33", "s34", "s35", "s36", "s37", "s38", "s39", "s40", "s41", "s42", "s43", "s44", "s45", "s46", "s47", "s48", "s49", "s50", "s51", "s52", "s53", "s54", "s55", "s56", "s57", "s58", "s59", "s60", "s61", "s62", "s63", "s64", "s65", "s66", "s67", "s68", "s69", "s70", "s71", "s72", "s73", "s74", "s75", "s76", "s77", "s78", "s79", "s80", "s81", "s82", "s83", "s84", "s85", "s86", "s87", "s88", "s89", "s90", "s91", "s92", "s93", "s94", "s95", "s96", "s97", "s98", "s99", "vcc", "scc", "memory");
}

extern "C" void kernel_launch(void* const* d_in, const int* in_sizes, int n_in, void* d_out, int out_size, void* d_ws, size_t ws_size,
                              hipStream_t stream) {
  fused_kernel<<<256, 256, 0, stream>>>(d_in[0], d_in[1], d_in[2], d_in[4], d_in[5], d_in[6], d_out, d_ws);
}
```
